```python
import torch

def _softmax(x, axis=-1):
    return torch.softmax(x, dim=axis)

def _gelu(x):
    return torch.nn.functional.gelu(x, approximate='tanh')

def _silu(x):
    return torch.nn.functional.silu(x)

def _sigmoid(x):
    return torch.sigmoid(x)

def _layernorm(x, axis=-1, eps=1e-05):
    m = x.mean(dim=axis, keepdim=True)
    v = x.var(dim=axis, keepdim=True, unbiased=False)
    return (x - m) / torch.sqrt(v + eps)

def _scatter_add(tgt, idx, src, axis=0):
    return tgt.clone().index_add_(axis, idx, src)

def _segment_sum(src, seg, n):
    out = torch.zeros((n,) + src.shape[1:], dtype=src.dtype)
    return out.index_add_(0, seg, src)

def _pad(x, pw):
    flat = tuple((v for pair in reversed(pw) for v in pair))
    return torch.nn.functional.pad(x, flat)
kernel_name = 'adaLN_cond__spectral_gfnet__glu_gated_bilinear__prompt'

def setup_inputs():

    _w = lambda a: (lambda t: t.to(torch.bfloat16) if t.is_floating_point() else t)(torch.as_tensor(a))

    def _A_setup():
        B, S, D = (1, 2048, 576)
        Df = D // 2 + 1
        return {'x': _w((lambda *s: torch.randn(*s, dtype=torch.bfloat16) * 0.05)(B, S, D)), 'w_r': _w((lambda *s: torch.randn(*s, dtype=torch.bfloat16) * 3.2)(S, Df) * 0.02), 'w_i': _w((lambda *s: torch.randn(*s, dtype=torch.bfloat16) * 3.2)(S, Df) * 0.02)}
    _A_in = _A_setup()

    def _B_setup():
        B, S, D, F = (1, 2048, 576, 4608)
        return {'x': _w((lambda *s: torch.randn(*s, dtype=torch.bfloat16) * 0.05)(B, S, D)), 'w_gate': _w((lambda *s: torch.randn(*s, dtype=torch.bfloat16) * 0.4)(D, F) * D ** (-0.5)), 'w_up': _w((lambda *s: torch.randn(*s, dtype=torch.bfloat16) * 0.4)(D, F) * D ** (-0.5)), 'w_down': _w((lambda *s: torch.randn(*s, dtype=torch.bfloat16) * 0.4)(F, D) * F ** (-0.5))}
    _B_in = _B_setup()
    return {'A_x': _A_in['x'], 'A_w_r': _A_in['w_r'], 'A_w_i': _A_in['w_i'], 'B_x': _B_in['x'], 'B_w_gate': _B_in['w_gate'], 'B_w_up': _B_in['w_up'], 'B_w_down': _B_in['w_down'], 'c': _w((lambda *s: torch.randn(*s, dtype=torch.bfloat16) * 0.1)(1, 576) * 8.0), 'w_mod': _w((lambda *s: torch.randn(*s, dtype=torch.bfloat16) * 0.1)(576, 6 * 576) * 576 ** (-0.5) * 8.0)}

def reference(A_x, A_w_r, A_w_i, B_x, B_w_gate, B_w_up, B_w_down, c, w_mod):

    def _A_ref(x, w_r, w_i):
        B, S, D = x.shape
        X = (lambda x, axes=(-2, -1): torch.fft.rfft2(x.float(), dim=axes))(x, axes=(-2, -1))
        W = w_r + 1j * w_i
        X = X * W
        y = (lambda x, s, axes=(-2, -1): torch.fft.irfft2(x, s=s, dim=axes).to(torch.bfloat16))(X, (S, D), axes=(-2, -1))
        return y

    def _B_ref(x, w_gate, w_up, w_down):
        g = torch.einsum('bsd,df->bsf', x, w_gate)
        u = torch.einsum('bsd,df->bsf', x, w_up)
        h = g * u
        return torch.einsum('bsf,fd->bsd', h, w_down)
    x = A_x
    mod = (_silu(c) @ w_mod).reshape(c.shape[0], 6, 1, -1)
    sa, ca, ga, sb, cb, gb = (mod[:, 0].clone(), mod[:, 1].clone(), mod[:, 2].clone(), mod[:, 3].clone(), mod[:, 4].clone(), mod[:, 5].clone())
    h = _layernorm(x, axis=-1) * (1 + ca) + sa
    x = x + ga * _A_ref(h, A_w_r, A_w_i)
    h = _layernorm(x, axis=-1) * (1 + cb) + sb
    x = x + gb * _B_ref(h, B_w_gate, B_w_up, B_w_down)
    return x
```

```cpp
#include <hip/hip_runtime.h>
#include <hip/hip_bf16.h>
#define NEXTRA 0
typedef unsigned short u16;
typedef short bf16x8 __attribute__((ext_vector_type(8)));
typedef float f32x4 __attribute__((ext_vector_type(4)));
constexpr int S = 2048, D = 576, DF = 289, F = 4608;
__device__ __forceinline__ float b2f(u16 v) { return __uint_as_float((unsigned)v << 16); }
__device__ __forceinline__ u16 f2b(float f) { unsigned u = __float_as_uint(f); u += 0x7fff + ((u >> 16) & 1); return (u16)(u >> 16); }

__global__ void k_mod(const u16* c, const u16* w, float* mod) {
  int j = blockIdx.x * 64 + (threadIdx.x & 63); int part = threadIdx.x >> 6;
  __shared__ float red[4][64];
  float acc = 0;
  for (int k = part; k < D; k += 4) { float cv = b2f(c[k]); float s = b2f(f2b(cv / (1.f + expf(-cv)))); acc += s * b2f(w[k * 6 * D + j]); }
  red[part][threadIdx.x & 63] = acc; __syncthreads();
  if (part == 0) mod[j] = b2f(f2b(red[0][threadIdx.x] + red[1][threadIdx.x] + red[2][threadIdx.x] + red[3][threadIdx.x]));
}
__device__ float block_sum(float v, float* red) {
  for (int o = 32; o; o >>= 1) v += __shfl_xor(v, o);
  __syncthreads();
  if ((threadIdx.x & 63) == 0) red[threadIdx.x >> 6] = v;
  __syncthreads();
  float s = 0; for (int i = 0; i < (int)blockDim.x / 64; ++i) s += red[i];
  return s;
}
__global__ void k_ln_dft(const u16* x, const float* mod, float2* Xd) {
  __shared__ float h[D]; __shared__ float2 tw[D]; __shared__ float red[8];
  int s = blockIdx.x, t = threadIdx.x;
  float v0 = t < 288 ? b2f(x[s * D + t]) : 0.f, v1 = t < 288 ? b2f(x[s * D + t + 288]) : 0.f;
  float m = block_sum(v0 + v1, red) / D;
  float d0 = t < 288 ? v0 - m : 0.f, d1 = t < 288 ? v1 - m : 0.f;
  float var = block_sum(d0 * d0 + d1 * d1, red) / D;
  float r = rsqrtf(var + 1e-5f);
  if (t < 288) {
    h[t] = d0 * r * (1.f + mod[D + t]) + mod[t]; h[t + 288] = d1 * r * (1.f + mod[D + t + 288]) + mod[t + 288];
    float sn, cs; sincospif(-2.f * t / D, &sn, &cs); tw[t] = make_float2(cs, sn); tw[t + 288] = make_float2(-cs, -sn);
  }
  __syncthreads();
  if (t < DF) {
    float re = 0, im = 0; int idx = 0;
    for (int d = 0; d < D; ++d) { float2 w = tw[idx]; re += h[d] * w.x; im += h[d] * w.y; idx += t; if (idx >= D) idx -= D; }
    Xd[t * S + s] = make_float2(re, im);
  }
}
__global__ __launch_bounds__(1024) void k_fft(float2* Xd, const u16* wr, const u16* wi) {
  __shared__ float2 a[S];
  int kd = blockIdx.x, t = threadIdx.x;
  a[t] = Xd[kd * S + t]; a[t + 1024] = Xd[kd * S + t + 1024];
  __syncthreads();
  for (int len = 1024; len >= 1; len >>= 1) {
    int j = t & (len - 1), base = (t / len) * 2 * len + j;
    float2 u = a[base], v = a[base + len];
    float sn, cs; sincospif(-(float)j / len, &sn, &cs);
    float dx = u.x - v.x, dy = u.y - v.y;
    a[base] = make_float2(u.x + v.x, u.y + v.y); a[base + len] = make_float2(dx * cs - dy * sn, dx * sn + dy * cs);
    __syncthreads();
  }
  for (int i = t; i < S; i += 1024) {
    int ks = __brev(i) >> 21;
    float wre = b2f(wr[ks * DF + kd]), wim = b2f(wi[ks * DF + kd]);
    float2 u = a[i]; a[i] = make_float2(u.x * wre - u.y * wim, u.x * wim + u.y * wre);
  }
  __syncthreads();
  for (int len = 1; len <= 1024; len <<= 1) {
    int j = t & (len - 1), base = (t / len) * 2 * len + j;
    float2 u = a[base], v = a[base + len];
    float sn, cs; sincospif((float)j / len, &sn, &cs);
    float vx = v.x * cs - v.y * sn, vy = v.x * sn + v.y * cs;
    a[base] = make_float2(u.x + vx, u.y + vy); a[base + len] = make_float2(u.x - vx, u.y - vy);
    __syncthreads();
  }
  Xd[kd * S + t] = a[t]; Xd[kd * S + t + 1024] = a[t + 1024];
}
__global__ __launch_bounds__(576) void k_idft_ln(const float2* Xd, const u16* x, const float* mod, u16* x2, u16* h2) {
  __shared__ float2 z[DF]; __shared__ float2 tw[D]; __shared__ float red[16];
  int s = blockIdx.x, t = threadIdx.x;
  if (t < DF) z[t] = Xd[t * S + s];
  float sn, cs; sincospif(2.f * t / D, &sn, &cs); tw[t] = make_float2(cs, sn);
  __syncthreads();
  float acc = 0; int idx = t;
  for (int k = 1; k < 288; ++k) { float2 w = tw[idx]; acc += z[k].x * w.x - z[k].y * w.y; idx += t; if (idx >= D) idx -= D; }
  float y = (2.f * acc + z[0].x + ((t & 1) ? -z[288].x : z[288].x)) * (1.f / (S * D));
  float v = b2f(f2b(b2f(x[s * D + t]) + mod[2 * D + t] * y));
  float m = block_sum(v, red) / D; float d = v - m;
  float var = block_sum(d * d, red) / D;
  x2[s * D + t] = f2b(v);
  h2[s * D + t] = f2b(d * rsqrtf(var + 1e-5f) * (1.f + mod[4 * D + t]) + mod[3 * D + t]);
}
template<int MODE>
__global__ __launch_bounds__(256) void k_gemm(const u16* A, const u16* B0, const u16* B1, u16* C, int N, int K, const u16* x2, const float* mod) {
  __shared__ u16 As[64][40], Bs[2][64][40];
  int bm = blockIdx.y * 64, bn = blockIdx.x * 64, t = threadIdx.x, wv = t >> 6, l = t & 63;
  int wm = (wv >> 1) * 32, wn = (wv & 1) * 32;
  f32x4 acc[2][2][2] = {};
  for (int k0 = 0; k0 < K; k0 += 32) {
    { int r = t >> 2, c = (t & 3) * 8; *(uint4*)&As[r][c] = *(const uint4*)&A[(size_t)(bm + r) * K + k0 + c]; }
    { int kk = t >> 3, c = (t & 7) * 8;
      uint4 v = *(const uint4*)&B0[(size_t)(k0 + kk) * N + bn + c]; const u16* p = (const u16*)&v;
      for (int i = 0; i < 8; ++i) Bs[0][c + i][kk] = p[i];
      if (MODE == 0) { uint4 w = *(const uint4*)&B1[(size_t)(k0 + kk) * N + bn + c]; const u16* q = (const u16*)&w; for (int i = 0; i < 8; ++i) Bs[1][c + i][kk] = q[i]; } }
    __syncthreads();
    for (int i = 0; i < 2; ++i) for (int j = 0; j < 2; ++j) {
      bf16x8 a = *(bf16x8*)&As[wm + i * 16 + (l & 15)][(l >> 4) * 8];
      bf16x8 b = *(bf16x8*)&Bs[0][wn + j * 16 + (l & 15)][(l >> 4) * 8];
      acc[0][i][j] = __builtin_amdgcn_mfma_f32_16x16x32_bf16(a, b, acc[0][i][j], 0, 0, 0);
      if (MODE == 0) { bf16x8 b1 = *(bf16x8*)&Bs[1][wn + j * 16 + (l & 15)][(l >> 4) * 8];
        acc[1][i][j] = __builtin_amdgcn_mfma_f32_16x16x32_bf16(a, b1, acc[1][i][j], 0, 0, 0); }
    }
    __syncthreads();
  }
  for (int i = 0; i < 2; ++i) for (int j = 0; j < 2; ++j) for (int r = 0; r < 4; ++r) {
    int row = bm + wm + i * 16 + (l >> 4) * 4 + r, col = bn + wn + j * 16 + (l & 15);
    if (MODE == 0) C[(size_t)row * N + col] = f2b(acc[0][i][j][r] * acc[1][i][j][r]);
    else C[(size_t)row * N + col] = f2b(b2f(x2[(size_t)row * N + col]) + mod[5 * D + col] * acc[0][i][j][r]);
  }
}
__global__ void k_empty(unsigned* p) { if (p == nullptr) __builtin_trap(); }

extern "C" void kernel_launch(void* const* d_in, const int* in_sizes, int n_in, void* d_out, int out_size, void* d_ws, size_t ws_size, hipStream_t stream) {
  const u16 *x = (const u16*)d_in[0], *wr = (const u16*)d_in[1], *wi = (const u16*)d_in[2], *wg = (const u16*)d_in[4], *wu = (const u16*)d_in[5], *wd = (const u16*)d_in[6], *c = (const u16*)d_in[7], *wm = (const u16*)d_in[8];
  char* ws = (char*)d_ws;
  float* mod = (float*)ws; float2* Xd = (float2*)(ws + (1 << 20)); u16* x2 = (u16*)(ws + (8 << 20)); u16* h2 = (u16*)(ws + (12 << 20)); u16* hh = (u16*)(ws + (16 << 20));
  k_mod<<<54, 256, 0, stream>>>(c, wm, mod);
  k_ln_dft<<<S, 320, 0, stream>>>(x, mod, Xd);
  k_fft<<<DF, 1024, 0, stream>>>(Xd, wr, wi);
  k_idft_ln<<<S, 576, 0, stream>>>(Xd, x, mod, x2, h2);
  for (int i = 0; i < NEXTRA; ++i) k_empty<<<256, 256, 0, stream>>>((unsigned*)ws);
  k_gemm<0><<<dim3(F / 64, S / 64), 256, 0, stream>>>(h2, wg, wu, hh, F, D, nullptr, mod);
  k_gemm<1><<<dim3(D / 64, S / 64), 256, 0, stream>>>(hh, wd, nullptr, (u16*)d_out, D, F, x2, mod);
}
```

```cpp
#include <hip/hip_runtime.h>
constexpr int MLP_CH = 2, MLP_LDS = 163840, MLP_NPRE = 4, MLP_XBUF = 98304;
#define PRE_ASM \
  ".Lentry_%=:\n" \
  "s_load_dwordx16 s[80:95], s[0:1], 0x0\n" \
  "s_load_dwordx4 s[96:99], s[0:1], 0x40\n" \
  "v_readfirstlane_b32 s3, v0\n" \
  "s_and_b32 s4, s2, 7\n" \
  "s_lshr_b32 s5, s2, 3\n" \
  "v_and_b32 v1, 63, v0\n" \
  "s_lshr_b32 s3, s3, 6\n" \
  "s_cmp_lg_u32 s5, 0\n" \
  "s_cbranch_scc1 .Lnopf_%=\n" \
  "s_getpc_b64 s[16:17]\n" \
  ".Lpc_%=:\n" \
  "v_lshlrev_b32 v2, 7, v0\n" \
  "v_min_u32 v2, .Lcode_end_%=-.Lpc_%=-4, v2\n" \
  "global_load_dword v253, v2, s[16:17]\n" \
  ".Lnopf_%=:\n" \
  "s_add_u32 s5, s5, 31\n" \
  "s_and_b32 s5, s5, 31\n" \
  "v_lshlrev_b32 v10, 4, v1\n" \
  "s_mul_i32 s6, s5, 43\n" \
  "s_lshr_b32 s6, s6, 8\n" \
  "s_mul_i32 s7, s6, 6\n" \
  "s_sub_u32 s7, s5, s7\n" \
  "s_lshl_b32 s6, s6, 3\n" \
  "s_add_u32 s6, s6, s4\n" \
  "s_lshl_b32 s24, s5, 3\n" \
  "s_add_u32 s24, s24, s4\n" \
  "s_sub_u32 s24, s24, 192\n" \
  "s_mul_i32 s16, s4, 7\n" \
  "s_min_u32 s16, s16, 48\n" \
  "s_add_u32 s16, s16, s5\n" \
  "s_sub_u32 s16, s16, 24\n" \
  "s_cmp_lt_u32 s24, 54\n" \
  "s_cselect_b32 s24, s16, s24\n" \
  "s_cmp_lt_u32 s3, 4\n" \
  "s_cbranch_scc1 .LM_%=\n" \
  "s_sub_u32 s25, s3, 4\n" \
  "s_cmp_ge_i32 s24, 54\n" \
  "s_cbranch_scc1 .LSzero_%=\n" \
  "s_setprio 2\n" \
  "s_mul_i32 s16, s7, 11\n" \
  "s_lshr_b32 s16, s16, 4\n" \
  "s_add_u32 s16, s16, s25\n" \
  "s_and_b32 s26, s16, 3\n" \
  "s_lshl_b32 s16, s26, 9\n" \
  "s_add_u32 s16, s16, s6\n" \
  "s_mul_i32 s16, s16, 1152\n" \
  "s_mul_i32 s17, s7, 3\n" \
  "s_lshr_b32 s17, s17, 1\n" \
  "s_lshl_b32 s17, s17, 7\n" \
  "s_mul_i32 s18, s25, 995328\n" \
  "s_lshl_b32 s19, s24, 7\n" \
  "s_add_u32 s18, s18, s19\n" \
  "s_mov_b32 s19, 124416\n" \
  "s_movk_i32 s20, 6912\n" \
  "s_movk_i32 s21, 13824\n" \
  "s_mov_b32 s10, 3981312\n" \
  "s_cmp_lt_i32 s24, 0\n" \
  "s_cselect_b32 s16, s16, s18\n" \
  "s_cselect_b32 s17, s17, 0\n" \
  "s_cselect_b32 s19, 36864, s19\n" \
  "s_cselect_b32 s20, 294912, s20\n" \
  "s_cselect_b32 s21, 128, s21\n" \
  "s_cselect_b32 s10, 2359296, s10\n" \
  "v_lshrrev_b32 v7, 3, v1\n" \
  "v_and_b32 v6, 0x70, v10\n" \
  "v_mul_u32_u24 v2, s19, v7\n" \
  "v_add3_u32 v28, v2, v6, s16\n" \
  "v_add_u32 v29, s20, v28\n" \
  "s_mov_b32 s11, 0x20000\n" \
  "s_mul_i32 s18, s25, 288\n" \
  "v_lshlrev_b32 v30, 3, v1\n" \
  "s_waitcnt lgkmcnt(0)\n" \
  "s_cselect_b32 s8, s80, s94\n" \
  "s_cselect_b32 s9, s81, s95\n" \
  "s_cbranch_scc1 .LSld_%=\n" \
  "s_cmp_lt_u32 s24, 18\n" \
  "s_cbranch_scc1 .LSc_%=\n" \
  "s_and_b32 s9, s9, 0xffff\n" \
  "s_sub_u32 s22, s24, 18\n" \
  "s_min_u32 s23, s22, 31\n" \
  "s_mul_i32 s23, s23, 18\n" \
  "s_mul_i32 s22, s25, 3\n" \
  "s_add_u32 s23, s23, s22\n" \
  "s_add_u32 s23, s23, 6\n" \
  "s_mul_i32 s23, s23, 6912\n" \
  "v_mul_u32_u24 v2, 57, v1\n" \
  "v_lshrrev_b32 v2, 10, v2\n" \
  "v_mul_u32_u24 v2, 4608, v2\n" \
  "v_lshl_add_u32 v2, v1, 7, v2\n" \
  "s_mov_b32 exec_hi, 0x3fffff\n" \
  "buffer_load_dword v253, v2, s[8:11], s23 offen\n" \
  "s_mov_b32 exec_hi, -1\n" \
  "s_sleep 1\n" \
  ".LSc_%=:\n" \
  "s_mov_b32 s12, s92\n" \
  "s_and_b32 s13, s93, 0xffff\n" \
  "s_mov_b32 s14, 1152\n" \
  "s_mov_b32 s15, 0x20000\n" \
  "buffer_load_dwordx2 v[4:5], v30, s[12:15], s18 offen\n" \
  ".LSld_%=:\n" \
  "s_and_b32 s9, s9, 0xffff\n" \
  "buffer_load_dwordx4 v[32:35], v28, s[8:11], s17 offen\n" \
  "buffer_load_dwordx4 v[36:39], v29, s[8:11], s17 offen\n" \
  "s_add_u32 s17, s17, s21\n" \
  "s_cmpk_eq_u32 s17, 1152\n" \
  "s_cselect_b32 s17, 0, s17\n" \
  "buffer_load_dwordx4 v[40:43], v28, s[8:11], s17 offen\n" \
  "buffer_load_dwordx4 v[44:47], v29, s[8:11], s17 offen\n" \
  "s_add_u32 s17, s17, s21\n" \
  "s_cmpk_eq_u32 s17, 1152\n" \
  "s_cselect_b32 s17, 0, s17\n" \
  "buffer_load_dwordx4 v[48:51], v28, s[8:11], s17 offen\n" \
  "buffer_load_dwordx4 v[52:55], v29, s[8:11], s17 offen\n" \
  "s_add_u32 s17, s17, s21\n" \
  "s_cmpk_eq_u32 s17, 1152\n" \
  "s_cselect_b32 s17, 0, s17\n" \
  "buffer_load_dwordx4 v[56:59], v28, s[8:11], s17 offen\n" \
  "buffer_load_dwordx4 v[60:63], v29, s[8:11], s17 offen\n" \
  "s_add_u32 s17, s17, s21\n" \
  "s_cmpk_eq_u32 s17, 1152\n" \
  "s_cselect_b32 s17, 0, s17\n" \
  "buffer_load_dwordx4 v[64:67], v28, s[8:11], s17 offen\n" \
  "buffer_load_dwordx4 v[68:71], v29, s[8:11], s17 offen\n" \
  "s_add_u32 s17, s17, s21\n" \
  "s_cmpk_eq_u32 s17, 1152\n" \
  "s_cselect_b32 s17, 0, s17\n" \
  "buffer_load_dwordx4 v[72:75], v28, s[8:11], s17 offen\n" \
  "buffer_load_dwordx4 v[76:79], v29, s[8:11], s17 offen\n" \
  "s_add_u32 s17, s17, s21\n" \
  "s_cmpk_eq_u32 s17, 1152\n" \
  "s_cselect_b32 s17, 0, s17\n" \
  "buffer_load_dwordx4 v[80:83], v28, s[8:11], s17 offen\n" \
  "buffer_load_dwordx4 v[84:87], v29, s[8:11], s17 offen\n" \
  "s_add_u32 s17, s17, s21\n" \
  "s_cmpk_eq_u32 s17, 1152\n" \
  "s_cselect_b32 s17, 0, s17\n" \
  "buffer_load_dwordx4 v[88:91], v28, s[8:11], s17 offen\n" \
  "buffer_load_dwordx4 v[92:95], v29, s[8:11], s17 offen\n" \
  "s_add_u32 s17, s17, s21\n" \
  "s_cmpk_eq_u32 s17, 1152\n" \
  "s_cselect_b32 s17, 0, s17\n" \
  "buffer_load_dwordx4 v[96:99], v28, s[8:11], s17 offen\n" \
  "buffer_load_dwordx4 v[100:103], v29, s[8:11], s17 offen\n" \
  "s_mov_b32 s40, s96\n" \
  "s_and_b32 s41, s97, 0xffff\n" \
  "s_mov_b32 s42, 134217728\n" \
  "s_mov_b32 s43, 0x20000\n" \
  "s_cmp_ge_i32 s24, 0\n" \
  "s_cbranch_scc1 .LSgemv_%=\n" \
  "v_mov_b32 v2, 0\n" \
  "v_mov_b32 v3, 0\n" \
  "v_mov_b32 v4, 0\n" \
  "v_mov_b32 v5, 0\n" \
  "v_mov_b32 v31, 0x3f803f80\n" \
  "v_mul_u32_u24 v7, 288, v7\n" \
  "s_mul_i32 s16, s26, 4608\n" \
  "v_add3_u32 v30, v7, v6, s16\n" \
  "s_mov_b32 s18, 0x3ae38e39\n" \
  "s_mov_b32 s20, 0x3727c5ac\n" \
  "s_waitcnt vmcnt(16)\n" \
  "v_dot2c_f32_bf16 v2, v32, v31\n" \
  "v_dot2c_f32_bf16 v4, v32, v32\n" \
  "v_lshlrev_b32 v104, 16, v32\n" \
  "v_and_b32 v105, 0xffff0000, v32\n" \
  "v_dot2c_f32_bf16 v3, v36, v31\n" \
  "v_dot2c_f32_bf16 v5, v36, v36\n" \
  "v_lshlrev_b32 v120, 16, v36\n" \
  "v_and_b32 v121, 0xffff0000, v36\n" \
  "v_dot2c_f32_bf16 v2, v33, v31\n" \
  "v_dot2c_f32_bf16 v4, v33, v33\n" \
  "v_lshlrev_b32 v106, 16, v33\n" \
  "v_and_b32 v107, 0xffff0000, v33\n" \
  "v_dot2c_f32_bf16 v3, v37, v31\n" \
  "v_dot2c_f32_bf16 v5, v37, v37\n" \
  "v_lshlrev_b32 v122, 16, v37\n" \
  "v_and_b32 v123, 0xffff0000, v37\n" \
  "v_dot2c_f32_bf16 v2, v34, v31\n" \
  "v_dot2c_f32_bf16 v4, v34, v34\n" \
  "v_lshlrev_b32 v108, 16, v34\n" \
  "v_and_b32 v109, 0xffff0000, v34\n" \
  "v_dot2c_f32_bf16 v3, v38, v31\n" \
  "v_dot2c_f32_bf16 v5, v38, v38\n" \
  "v_lshlrev_b32 v124, 16, v38\n" \
  "v_and_b32 v125, 0xffff0000, v38\n" \
  "v_dot2c_f32_bf16 v2, v35, v31\n" \
  "v_dot2c_f32_bf16 v4, v35, v35\n" \
  "v_lshlrev_b32 v110, 16, v35\n" \
  "v_and_b32 v111, 0xffff0000, v35\n" \
  "v_dot2c_f32_bf16 v3, v39, v31\n" \
  "v_dot2c_f32_bf16 v5, v39, v39\n" \
  "v_lshlrev_b32 v126, 16, v39\n" \
  "v_and_b32 v127, 0xffff0000, v39\n" \
  "s_waitcnt vmcnt(14)\n" \
  "v_dot2c_f32_bf16 v2, v40, v31\n" \
  "v_dot2c_f32_bf16 v4, v40, v40\n" \
  "v_lshlrev_b32 v112, 16, v40\n" \
  "v_and_b32 v113, 0xffff0000, v40\n" \
  "v_dot2c_f32_bf16 v3, v44, v31\n" \
  "v_dot2c_f32_bf16 v5, v44, v44\n" \
  "v_lshlrev_b32 v128, 16, v44\n" \
  "v_and_b32 v129, 0xffff0000, v44\n" \
  "v_dot2c_f32_bf16 v2, v41, v31\n" \
  "v_dot2c_f32_bf16 v4, v41, v41\n" \
  "v_lshlrev_b32 v114, 16, v41\n" \
  "v_and_b32 v115, 0xffff0000, v41\n" \
  "v_dot2c_f32_bf16 v3, v45, v31\n" \
  "v_dot2c_f32_bf16 v5, v45, v45\n" \
  "v_lshlrev_b32 v130, 16, v45\n" \
  "v_and_b32 v131, 0xffff0000, v45\n" \
  "v_dot2c_f32_bf16 v2, v42, v31\n" \
  "v_dot2c_f32_bf16 v4, v42, v42\n" \
  "v_lshlrev_b32 v116, 16, v42\n" \
  "v_and_b32 v117, 0xffff0000, v42\n" \
  "v_dot2c_f32_bf16 v3, v46, v31\n" \
  "v_dot2c_f32_bf16 v5, v46, v46\n" \
  "v_lshlrev_b32 v132, 16, v46\n" \
  "v_and_b32 v133, 0xffff0000, v46\n" \
  "v_dot2c_f32_bf16 v2, v43, v31\n" \
  "v_dot2c_f32_bf16 v4, v43, v43\n" \
  "v_lshlrev_b32 v118, 16, v43\n" \
  "v_and_b32 v119, 0xffff0000, v43\n" \
  "v_dot2c_f32_bf16 v3, v47, v31\n" \
  "v_dot2c_f32_bf16 v5, v47, v47\n" \
  "v_lshlrev_b32 v134, 16, v47\n" \
  "v_and_b32 v135, 0xffff0000, v47\n" \
  "s_waitcnt vmcnt(12)\n" \
  "v_dot2c_f32_bf16 v2, v48, v31\n" \
  "v_dot2c_f32_bf16 v4, v48, v48\n" \
  "v_dot2c_f32_bf16 v3, v52, v31\n" \
  "v_dot2c_f32_bf16 v5, v52, v52\n" \
  "v_dot2c_f32_bf16 v2, v49, v31\n" \
  "v_dot2c_f32_bf16 v4, v49, v49\n" \
  "v_dot2c_f32_bf16 v3, v53, v31\n" \
  "v_dot2c_f32_bf16 v5, v53, v53\n" \
  "v_dot2c_f32_bf16 v2, v50, v31\n" \
  "v_dot2c_f32_bf16 v4, v50, v50\n" \
  "v_dot2c_f32_bf16 v3, v54, v31\n" \
  "v_dot2c_f32_bf16 v5, v54, v54\n" \
  "v_dot2c_f32_bf16 v2, v51, v31\n" \
  "v_dot2c_f32_bf16 v4, v51, v51\n" \
  "v_dot2c_f32_bf16 v3, v55, v31\n" \
  "v_dot2c_f32_bf16 v5, v55, v55\n" \
  "s_waitcnt vmcnt(10)\n" \
  "v_dot2c_f32_bf16 v2, v56, v31\n" \
  "v_dot2c_f32_bf16 v4, v56, v56\n" \
  "v_dot2c_f32_bf16 v3, v60, v31\n" \
  "v_dot2c_f32_bf16 v5, v60, v60\n" \
  "v_dot2c_f32_bf16 v2, v57, v31\n" \
  "v_dot2c_f32_bf16 v4, v57, v57\n" \
  "v_dot2c_f32_bf16 v3, v61, v31\n" \
  "v_dot2c_f32_bf16 v5, v61, v61\n" \
  "v_dot2c_f32_bf16 v2, v58, v31\n" \
  "v_dot2c_f32_bf16 v4, v58, v58\n" \
  "v_dot2c_f32_bf16 v3, v62, v31\n" \
  "v_dot2c_f32_bf16 v5, v62, v62\n" \
  "v_dot2c_f32_bf16 v2, v59, v31\n" \
  "v_dot2c_f32_bf16 v4, v59, v59\n" \
  "v_dot2c_f32_bf16 v3, v63, v31\n" \
  "v_dot2c_f32_bf16 v5, v63, v63\n" \
  "s_waitcnt vmcnt(8)\n" \
  "v_dot2c_f32_bf16 v2, v64, v31\n" \
  "v_dot2c_f32_bf16 v4, v64, v64\n" \
  "v_dot2c_f32_bf16 v3, v68, v31\n" \
  "v_dot2c_f32_bf16 v5, v68, v68\n" \
  "v_dot2c_f32_bf16 v2, v65, v31\n" \
  "v_dot2c_f32_bf16 v4, v65, v65\n" \
  "v_dot2c_f32_bf16 v3, v69, v31\n" \
  "v_dot2c_f32_bf16 v5, v69, v69\n" \
  "v_dot2c_f32_bf16 v2, v66, v31\n" \
  "v_dot2c_f32_bf16 v4, v66, v66\n" \
  "v_dot2c_f32_bf16 v3, v70, v31\n" \
  "v_dot2c_f32_bf16 v5, v70, v70\n" \
  "v_dot2c_f32_bf16 v2, v67, v31\n" \
  "v_dot2c_f32_bf16 v4, v67, v67\n" \
  "v_dot2c_f32_bf16 v3, v71, v31\n" \
  "v_dot2c_f32_bf16 v5, v71, v71\n" \
  "s_sub_u32 s16, s2, 8\n" \
  "s_mul_i32 s17, s25, 192\n" \
  "s_add_u32 s16, s16, s17\n" \
  "s_bitcmp1_b32 s16, 0\n" \
  "s_cselect_b32 s8, s88, s86\n" \
  "s_cselect_b32 s9, s89, s87\n" \
  "s_and_b32 s9, s9, 0xffff\n" \
  "s_mov_b32 s10, 2654208\n" \
  "s_lshr_b32 s16, s16, 1\n" \
  "s_lshl_b32 s16, s16, 13\n" \
  "v_lshl_add_u32 v252, v1, 7, s16\n" \
  "buffer_load_dword v253, v252, s[8:11], 0 offen\n" \
  "s_waitcnt vmcnt(7)\n" \
  "v_dot2c_f32_bf16 v2, v72, v31\n" \
  "v_dot2c_f32_bf16 v4, v72, v72\n" \
  "v_dot2c_f32_bf16 v3, v76, v31\n" \
  "v_dot2c_f32_bf16 v5, v76, v76\n" \
  "v_dot2c_f32_bf16 v2, v73, v31\n" \
  "v_dot2c_f32_bf16 v4, v73, v73\n" \
  "v_dot2c_f32_bf16 v3, v77, v31\n" \
  "v_dot2c_f32_bf16 v5, v77, v77\n" \
  "v_dot2c_f32_bf16 v2, v74, v31\n" \
  "v_dot2c_f32_bf16 v4, v74, v74\n" \
  "v_dot2c_f32_bf16 v3, v78, v31\n" \
  "v_dot2c_f32_bf16 v5, v78, v78\n" \
  "v_dot2c_f32_bf16 v2, v75, v31\n" \
  "v_dot2c_f32_bf16 v4, v75, v75\n" \
  "v_dot2c_f32_bf16 v3, v79, v31\n" \
  "v_dot2c_f32_bf16 v5, v79, v79\n" \
  "s_waitcnt vmcnt(5)\n" \
  "v_dot2c_f32_bf16 v2, v80, v31\n" \
  "v_dot2c_f32_bf16 v4, v80, v80\n" \
  "v_dot2c_f32_bf16 v3, v84, v31\n" \
  "v_dot2c_f32_bf16 v5, v84, v84\n" \
  "v_dot2c_f32_bf16 v2, v81, v31\n" \
  "v_dot2c_f32_bf16 v4, v81, v81\n" \
  "v_dot2c_f32_bf16 v3, v85, v31\n" \
  "v_dot2c_f32_bf16 v5, v85, v85\n" \
  "v_dot2c_f32_bf16 v2, v82, v31\n" \
  "v_dot2c_f32_bf16 v4, v82, v82\n" \
  "v_dot2c_f32_bf16 v3, v86, v31\n" \
  "v_dot2c_f32_bf16 v5, v86, v86\n" \
  "v_dot2c_f32_bf16 v2, v83, v31\n" \
  "v_dot2c_f32_bf16 v4, v83, v83\n" \
  "v_dot2c_f32_bf16 v3, v87, v31\n" \
  "v_dot2c_f32_bf16 v5, v87, v87\n" \
  "s_waitcnt vmcnt(3)\n" \
  "v_dot2c_f32_bf16 v2, v88, v31\n" \
  "v_dot2c_f32_bf16 v4, v88, v88\n" \
  "v_dot2c_f32_bf16 v3, v92, v31\n" \
  "v_dot2c_f32_bf16 v5, v92, v92\n" \
  "v_dot2c_f32_bf16 v2, v89, v31\n" \
  "v_dot2c_f32_bf16 v4, v89, v89\n" \
  "v_dot2c_f32_bf16 v3, v93, v31\n" \
  "v_dot2c_f32_bf16 v5, v93, v93\n" \
  "v_dot2c_f32_bf16 v2, v90, v31\n" \
  "v_dot2c_f32_bf16 v4, v90, v90\n" \
  "v_dot2c_f32_bf16 v3, v94, v31\n" \
  "v_dot2c_f32_bf16 v5, v94, v94\n" \
  "v_dot2c_f32_bf16 v2, v91, v31\n" \
  "v_dot2c_f32_bf16 v4, v91, v91\n" \
  "v_dot2c_f32_bf16 v3, v95, v31\n" \
  "v_dot2c_f32_bf16 v5, v95, v95\n" \
  "s_waitcnt vmcnt(1)\n" \
  "v_dot2c_f32_bf16 v2, v96, v31\n" \
  "v_dot2c_f32_bf16 v4, v96, v96\n" \
  "v_dot2c_f32_bf16 v3, v100, v31\n" \
  "v_dot2c_f32_bf16 v5, v100, v100\n" \
  "v_dot2c_f32_bf16 v2, v97, v31\n" \
  "v_dot2c_f32_bf16 v4, v97, v97\n" \
  "v_dot2c_f32_bf16 v3, v101, v31\n" \
  "v_dot2c_f32_bf16 v5, v101, v101\n" \
  "v_dot2c_f32_bf16 v2, v98, v31\n" \
  "v_dot2c_f32_bf16 v4, v98, v98\n" \
  "v_dot2c_f32_bf16 v3, v102, v31\n" \
  "v_dot2c_f32_bf16 v5, v102, v102\n" \
  "v_dot2c_f32_bf16 v2, v99, v31\n" \
  "v_dot2c_f32_bf16 v4, v99, v99\n" \
  "v_dot2c_f32_bf16 v3, v103, v31\n" \
  "v_dot2c_f32_bf16 v5, v103, v103\n" \
  "v_add_f32_dpp v2, v2, v2 quad_perm:[1,0,3,2] row_mask:0xf bank_mask:0xf\n" \
  "v_add_f32_dpp v4, v4, v4 quad_perm:[1,0,3,2] row_mask:0xf bank_mask:0xf\n" \
  "v_add_f32_dpp v3, v3, v3 quad_perm:[1,0,3,2] row_mask:0xf bank_mask:0xf\n" \
  "v_add_f32_dpp v5, v5, v5 quad_perm:[1,0,3,2] row_mask:0xf bank_mask:0xf\n" \
  "v_add_f32_dpp v2, v2, v2 quad_perm:[2,3,0,1] row_mask:0xf bank_mask:0xf\n" \
  "v_add_f32_dpp v4, v4, v4 quad_perm:[2,3,0,1] row_mask:0xf bank_mask:0xf\n" \
  "v_add_f32_dpp v3, v3, v3 quad_perm:[2,3,0,1] row_mask:0xf bank_mask:0xf\n" \
  "v_add_f32_dpp v5, v5, v5 quad_perm:[2,3,0,1] row_mask:0xf bank_mask:0xf\n" \
  "v_add_f32_dpp v2, v2, v2 row_half_mirror row_mask:0xf bank_mask:0xf\n" \
  "v_add_f32_dpp v4, v4, v4 row_half_mirror row_mask:0xf bank_mask:0xf\n" \
  "v_add_f32_dpp v3, v3, v3 row_half_mirror row_mask:0xf bank_mask:0xf\n" \
  "v_add_f32_dpp v5, v5, v5 row_half_mirror row_mask:0xf bank_mask:0xf\n" \
  "v_pk_mul_f32 v[2:3], v[2:3], s[18:19] op_sel_hi:[1,0]\n" \
  "v_pk_fma_f32 v[6:7], v[2:3], v[2:3], s[20:21] op_sel_hi:[1,1,0] neg_lo:[1,0,0] neg_hi:[1,0,0]\n" \
  "v_pk_fma_f32 v[4:5], v[4:5], s[18:19], v[6:7] op_sel_hi:[1,0,1]\n" \
  "v_max_f32 v4, s20, v4\n" \
  "v_max_f32 v5, s20, v5\n" \
  "v_rsq_f32 v4, v4\n" \
  "v_rsq_f32 v5, v5\n" \
  "s_nop 0\n" \
  "v_pk_mul_f32 v[2:3], v[2:3], v[4:5] neg_lo:[1,0] neg_hi:[1,0]\n" \
  "v_pk_fma_f32 v[104:105], v[104:105], v[4:5], v[2:3] op_sel:[0,0,0] op_sel_hi:[1,0,0]\n" \
  "v_pk_fma_f32 v[106:107], v[106:107], v[4:5], v[2:3] op_sel:[0,0,0] op_sel_hi:[1,0,0]\n" \
  "v_pk_fma_f32 v[108:109], v[108:109], v[4:5], v[2:3] op_sel:[0,0,0] op_sel_hi:[1,0,0]\n" \
  "v_pk_fma_f32 v[110:111], v[110:111], v[4:5], v[2:3] op_sel:[0,0,0] op_sel_hi:[1,0,0]\n" \
  "v_cvt_pk_bf16_f32 v104, v104, v105\n" \
  "v_cvt_pk_bf16_f32 v105, v106, v107\n" \
  "v_cvt_pk_bf16_f32 v106, v108, v109\n" \
  "v_cvt_pk_bf16_f32 v107, v110, v111\n" \
  "ds_write_b128 v30, v[104:107] offset:0\n" \
  "v_pk_fma_f32 v[112:113], v[112:113], v[4:5], v[2:3] op_sel:[0,0,0] op_sel_hi:[1,0,0]\n" \
  "v_pk_fma_f32 v[114:115], v[114:115], v[4:5], v[2:3] op_sel:[0,0,0] op_sel_hi:[1,0,0]\n" \
  "v_pk_fma_f32 v[116:117], v[116:117], v[4:5], v[2:3] op_sel:[0,0,0] op_sel_hi:[1,0,0]\n" \
  "v_pk_fma_f32 v[118:119], v[118:119], v[4:5], v[2:3] op_sel:[0,0,0] op_sel_hi:[1,0,0]\n" \
  "v_cvt_pk_bf16_f32 v112, v112, v113\n" \
  "v_cvt_pk_bf16_f32 v113, v114, v115\n" \
  "v_cvt_pk_bf16_f32 v114, v116, v117\n" \
  "v_cvt_pk_bf16_f32 v115, v118, v119\n" \
  "ds_write_b128 v30, v[112:115] offset:128\n" \
  "v_pk_fma_f32 v[120:121], v[120:121], v[4:5], v[2:3] op_sel:[0,1,1] op_sel_hi:[1,1,1]\n" \
  "v_pk_fma_f32 v[122:123], v[122:123], v[4:5], v[2:3] op_sel:[0,1,1] op_sel_hi:[1,1,1]\n" \
  "v_pk_fma_f32 v[124:125], v[124:125], v[4:5], v[2:3] op_sel:[0,1,1] op_sel_hi:[1,1,1]\n" \
  "v_pk_fma_f32 v[126:127], v[126:127], v[4:5], v[2:3] op_sel:[0,1,1] op_sel_hi:[1,1,1]\n" \
  "v_cvt_pk_bf16_f32 v120, v120, v121\n" \
  "v_cvt_pk_bf16_f32 v121, v122, v123\n" \
  "v_cvt_pk_bf16_f32 v122, v124, v125\n" \
  "v_cvt_pk_bf16_f32 v123, v126, v127\n" \
  "ds_write_b128 v30, v[120:123] offset:2304\n" \
  "v_pk_fma_f32 v[128:129], v[128:129], v[4:5], v[2:3] op_sel:[0,1,1] op_sel_hi:[1,1,1]\n" \
  "v_pk_fma_f32 v[130:131], v[130:131], v[4:5], v[2:3] op_sel:[0,1,1] op_sel_hi:[1,1,1]\n" \
  "v_pk_fma_f32 v[132:133], v[132:133], v[4:5], v[2:3] op_sel:[0,1,1] op_sel_hi:[1,1,1]\n" \
  "v_pk_fma_f32 v[134:135], v[134:135], v[4:5], v[2:3] op_sel:[0,1,1] op_sel_hi:[1,1,1]\n" \
  "v_cvt_pk_bf16_f32 v128, v128, v129\n" \
  "v_cvt_pk_bf16_f32 v129, v130, v131\n" \
  "v_cvt_pk_bf16_f32 v130, v132, v133\n" \
  "v_cvt_pk_bf16_f32 v131, v134, v135\n" \
  "ds_write_b128 v30, v[128:131] offset:2432\n" \
  ".LSjoin_%=:\n" \
  "s_setprio 0\n" \
  "s_waitcnt lgkmcnt(0)\n" \
  "s_barrier\n" \
  "s_lshr_b32 s16, s5, 3\n" \
  "s_lshl_b32 s16, s16, 3\n" \
  "s_add_u32 s16, s16, s4\n" \
  "s_mul_i32 s0, s16, 76800\n" \
  "s_add_u32 s0, s0, 7340032\n" \
  "s_lshl_b32 s16, s16, 3\n" \
  "s_and_b32 s17, s5, 7\n" \
  "s_add_u32 s16, s16, s17\n" \
  "s_lshr_b32 s17, s16, 1\n" \
  "s_mul_i32 s1, s17, 18432\n" \
  "s_and_b32 s17, s16, 1\n" \
  "s_lshl_b32 s17, s17, 7\n" \
  "s_add_u32 s1, s1, s17\n" \
  "v_and_b32 v24, 7, v1\n" \
  "v_lshrrev_b32 v26, 3, v1\n" \
  "s_lshl_b32 s17, s3, 3\n" \
  "v_add_u32 v26, s17, v26\n" \
  "v_lshlrev_b32 v27, 4, v26\n" \
  "v_lshlrev_b32 v26, 8, v26\n" \
  "v_lshl_add_u32 v26, v24, 4, v26\n" \
  "v_lshlrev_b32 v24, 5, v24\n" \
  "v_add_u32 v24, 76800, v24\n" \
  "s_lshl_b32 s17, s3, 10\n" \
  "s_add_u32 s17, s17, 155648\n" \
  "v_add_u32 v25, s17, v10\n" \
  "s_and_b32 s18, s3, 1\n" \
  "s_bfe_u32 s19, s3, 0x10001\n" \
  "v_and_b32 v2, 1, v1\n" \
  "v_bfe_u32 v3, v1, 1, 3\n" \
  "v_lshrrev_b32 v4, 4, v1\n" \
  "s_and_b32 s17, s16, 7\n" \
  "s_lshl_b32 s17, s17, 8\n" \
  "s_lshr_b32 s16, s16, 3\n" \
  "s_add_u32 s16, s16, s17\n" \
  "v_lshl_add_u32 v5, v3, 5, s16\n" \
  "v_mul_u32_u24 v6, 0x480, v5\n" \
  "s_lshl_b32 s20, s18, 5\n" \
  "v_lshl_add_u32 v7, v4, 3, s20\n" \
  "v_lshrrev_b32 v28, 1, v4\n" \
  "s_lshl_b32 s20, s18, 1\n" \
  "v_add_u32 v28, s20, v28\n" \
  "v_lshlrev_b32 v28, 7, v28\n" \
  "v_lshl_add_u32 v28, v3, 4, v28\n" \
  "v_and_b32 v29, 1, v4\n" \
  "v_lshl_add_u32 v28, v29, 3, v28\n" \
  "s_cmp_eq_u32 s19, 0\n" \
  "s_cselect_b32 s20, 0, 9\n" \
  "s_cselect_b32 s21, 0, 0\n" \
  "v_mul_i32_i24 v29, s21, v2\n" \
  "v_add_u32 v29, s20, v29\n" \
  "v_lshl_add_u32 v32, v29, 6, v7\n" \
  "v_add_u32 v44, v32, v6\n" \
  "v_lshrrev_b32 v30, 4, v29\n" \
  "v_mul_i32_i24 v30, 4294880512, v30\n" \
  "v_lshl_add_u32 v29, v29, 9, v28\n" \
  "s_mov_b32 s20, 155648\n" \
  "v_add3_u32 v37, v29, v30, s20\n" \
  "s_cmp_eq_u32 s19, 0\n" \
  "s_cselect_b32 s20, 10, 1\n" \
  "s_cselect_b32 s21, -2, 16\n" \
  "v_mul_i32_i24 v29, s21, v2\n" \
  "v_add_u32 v29, s20, v29\n" \
  "v_lshl_add_u32 v33, v29, 6, v7\n" \
  "v_add_u32 v45, v33, v6\n" \
  "v_lshrrev_b32 v30, 4, v29\n" \
  "v_mul_i32_i24 v30, 4294880512, v30\n" \
  "v_lshl_add_u32 v29, v29, 9, v28\n" \
  "s_mov_b32 s20, 155648\n" \
  "v_add3_u32 v38, v29, v30, s20\n" \
  "s_cmp_eq_u32 s19, 0\n" \
  "s_cselect_b32 s20, 2, 11\n" \
  "s_cselect_b32 s21, 14, -4\n" \
  "v_mul_i32_i24 v29, s21, v2\n" \
  "v_add_u32 v29, s20, v29\n" \
  "v_lshl_add_u32 v34, v29, 6, v7\n" \
  "v_add_u32 v46, v34, v6\n" \
  "v_lshrrev_b32 v30, 4, v29\n" \
  "v_mul_i32_i24 v30, 4294880512, v30\n" \
  "v_lshl_add_u32 v29, v29, 9, v28\n" \
  "s_mov_b32 s20, 155648\n" \
  "v_add3_u32 v39, v29, v30, s20\n" \
  "s_cmp_eq_u32 s19, 0\n" \
  "s_cselect_b32 s20, 12, 3\n" \
  "s_cselect_b32 s21, -6, 12\n" \
  "v_mul_i32_i24 v29, s21, v2\n" \
  "v_add_u32 v29, s20, v29\n" \
  "v_lshl_add_u32 v35, v29, 6, v7\n" \
  "v_add_u32 v47, v35, v6\n" \
  "v_lshrrev_b32 v30, 4, v29\n" \
  "v_mul_i32_i24 v30, 4294880512, v30\n" \
  "v_lshl_add_u32 v29, v29, 9, v28\n" \
  "s_mov_b32 s20, 155648\n" \
  "v_add3_u32 v40, v29, v30, s20\n" \
  "s_cmp_eq_u32 s19, 0\n" \
  "s_cselect_b32 s20, 4, 13\n" \
  "s_cselect_b32 s21, 10, -8\n" \
  "v_mul_i32_i24 v29, s21, v2\n" \
  "v_add_u32 v29, s20, v29\n" \
  "v_lshl_add_u32 v36, v29, 6, v7\n" \
  "v_add_u32 v48, v36, v6\n" \
  "v_lshrrev_b32 v30, 4, v29\n" \
  "v_mul_i32_i24 v30, 4294880512, v30\n" \
  "v_lshl_add_u32 v29, v29, 9, v28\n" \
  "s_mov_b32 s20, 155648\n" \
  "v_add3_u32 v41, v29, v30, s20\n" \
  "v_cvt_f32_u32 v6, v5\n" \
  "v_mul_f32 v6, 0x3a000000, v6\n" \
  "v_cvt_f32_u32 v42, v2\n" \
  "v_lshlrev_b32 v7, 2, v4\n" \
  "v_cvt_f32_u32 v7, v7\n" \
  "v_mul_f32 v28, 0xbe800000, v42\n" \
  "v_fma_f32 v7, v6, v7, v28\n" \
  "s_cmp_eq_u32 s19, 0\n" \
  "s_cselect_b32 s20, 0, 0x80008000\n" \
  "v_cmp_eq_u32 s[22:23], 0, v4\n" \
  "s_nop 1\n" \
  "v_cndmask_b32 v43, 1.0, 0.5, s[22:23]\n" \
  "v_cos_f32 v30, v7\n" \
  "v_sin_f32 v31, v7\n" \
  "v_mul_f32 v30, v43, v30\n" \
  "v_mul_f32 v31, v43, v31\n" \
  "v_cvt_pk_bf16_f32 v64, v30, -v31\n" \
  "v_xor_b32 v72, s20, v64\n" \
  "v_fmamk_f32 v29, v6, 0x3f800000, v7\n" \
  "v_cos_f32 v30, v29\n" \
  "v_sin_f32 v31, v29\n" \
  "s_nop 0\n" \
  "v_cvt_pk_bf16_f32 v65, v30, -v31\n" \
  "v_xor_b32 v73, s20, v65\n" \
  "v_fmamk_f32 v29, v6, 0x40000000, v7\n" \
  "v_cos_f32 v30, v29\n" \
  "v_sin_f32 v31, v29\n" \
  "s_nop 0\n" \
  "v_cvt_pk_bf16_f32 v66, v30, -v31\n" \
  "v_xor_b32 v74, s20, v66\n" \
  "v_fmamk_f32 v29, v6, 0x40400000, v7\n" \
  "v_cos_f32 v30, v29\n" \
  "v_sin_f32 v31, v29\n" \
  "s_nop 0\n" \
  "v_cvt_pk_bf16_f32 v67, v30, -v31\n" \
  "v_xor_b32 v75, s20, v67\n" \
  "v_fmamk_f32 v29, v6, 0x41800000, v7\n" \
  "v_cos_f32 v30, v29\n" \
  "v_sin_f32 v31, v29\n" \
  "s_nop 0\n" \
  "v_cvt_pk_bf16_f32 v68, v30, -v31\n" \
  "v_xor_b32 v76, s20, v68\n" \
  "v_fmamk_f32 v29, v6, 0x41880000, v7\n" \
  "v_cos_f32 v30, v29\n" \
  "v_sin_f32 v31, v29\n" \
  "s_nop 0\n" \
  "v_cvt_pk_bf16_f32 v69, v30, -v31\n" \
  "v_xor_b32 v77, s20, v69\n" \
  "v_fmamk_f32 v29, v6, 0x41900000, v7\n" \
  "v_cos_f32 v30, v29\n" \
  "v_sin_f32 v31, v29\n" \
  "s_nop 0\n" \
  "v_cvt_pk_bf16_f32 v70, v30, -v31\n" \
  "v_xor_b32 v78, s20, v70\n" \
  "v_fmamk_f32 v29, v6, 0x41980000, v7\n" \
  "v_cos_f32 v30, v29\n" \
  "v_sin_f32 v31, v29\n" \
  "s_nop 0\n" \
  "v_cvt_pk_bf16_f32 v71, v30, -v31\n" \
  "v_xor_b32 v79, s20, v71\n" \
  "v_fmamk_f32 v29, v6, 0x42000000, v28\n" \
  "v_cos_f32 v30, v29\n" \
  "v_sin_f32 v31, v29\n" \
  "v_cndmask_b32 v43, 0, 0.5, s[22:23]\n" \
  "v_mul_f32 v30, v43, v30\n" \
  "v_mul_f32 v31, v43, v31\n" \
  "v_cvt_pk_bf16_f32 v80, v30, -v31\n" \
  "v_xor_b32 v81, s20, v80\n" \
  "v_mov_b32 v82, 0\n" \
  "v_mov_b32 v83, 0\n" \
  ".LSx1_%=:\n" \
  "s_barrier\n" \
  "v_bfe_u32 v29, v1, 3, 1\n" \
  "v_lshl_add_u32 v30, v4, 2, v29\n" \
  "v_lshlrev_b32 v30, 8, v30\n" \
  "v_xor_b32 v29, s18, v29\n" \
  "v_lshl_add_u32 v30, v29, 5, v30\n" \
  "v_bfe_u32 v29, v1, 2, 1\n" \
  "v_lshl_add_u32 v30, v29, 6, v30\n" \
  "v_and_b32 v31, 3, v1\n" \
  "v_lshl_add_u32 v30, v31, 3, v30\n" \
  "v_and_b32 v29, 1, v4\n" \
  "v_lshl_add_u32 v94, v29, 7, v30\n" \
  "v_xor_b32 v95, 0x80, v94\n" \
  "v_bfe_u32 v29, v1, 2, 2\n" \
  "v_lshlrev_b32 v29, 6, v29\n" \
  "v_lshl_add_u32 v29, v31, 3, v29\n" \
  "s_lshl_b32 s20, s18, 5\n" \
  "s_add_u32 s20, s20, 8192\n" \
  "v_add_u32 v96, s20, v29\n" \
  "v_fma_f32 v97, v42, -2.0, 1.0\n" \
  "v_sub_f32 v98, 1.0, v42\n" \
  "v_sub_f32 v106, 0, v97\n" \
  "v_mov_b32 v29, 0x3f441b7d\n" \
  "v_fmamk_f32 v99, v42, 0xbdfc6e14, v29\n" \
  "v_mul_f32 v103, v97, v99\n" \
  "v_mov_b32 v29, 0x3e31d0d4\n" \
  "v_fmamk_f32 v100, v42, 0x3f4fa827, v29\n" \
  "v_mul_f32 v104, v97, v100\n" \
  "v_mov_b32 v29, 0xbf000000\n" \
  "v_fmamk_f32 v101, v42, 0x3faed9ec, v29\n" \
  "v_mul_f32 v105, v97, v101\n" \
  "v_mov_b32 v29, 0xbf708fb2\n" \
  "v_fmamk_f32 v102, v42, 0x3fa40f2a, v29\n" \
  "v_lshrrev_b32 v29, 5, v1\n" \
  "v_lshlrev_b32 v29, 2, v29\n" \
  "v_lshl_add_u32 v29, v3, 5, v29\n" \
  "s_lshl_b32 s20, s25, 3\n" \
  "s_add_u32 s20, s20, 76800\n" \
  "v_add_u32 v107, s20, v29\n" \
  "v_cmp_eq_u32 s[44:45], 0, v2\n" \
  "s_cmp_gt_u32 s6, 32\n" \
  "s_cbranch_scc1 .LSidle_%=\n" \
  "v_and_b32 v2, 7, v1\n" \
  "v_lshrrev_b32 v3, 3, v1\n" \
  "s_mul_i32 s16, s7, 11\n" \
  "s_lshr_b32 s16, s16, 4\n" \
  "s_add_u32 s16, s16, s25\n" \
  "s_and_b32 s16, s16, 3\n" \
  "s_lshl_b32 s16, s16, 3\n" \
  "v_add_u32 v3, s16, v3\n" \
  "v_lshlrev_b32 v29, 4, v2\n" \
  "v_mul_u32_u24 v28, 0x900, v3\n" \
  "v_add_u32 v28, v28, v29\n" \
  "v_and_b32 v4, 4, v2\n" \
  "v_and_b32 v5, 1, v2\n" \
  "v_lshl_or_b32 v4, v5, 1, v4\n" \
  "v_bfe_u32 v5, v2, 1, 1\n" \
  "v_or_b32 v4, v4, v5\n" \
  "v_mul_u32_u24 v30, 80, v3\n" \
  "v_lshl_add_u32 v30, v4, 3, v30\n" \
  "v_cvt_f32_u32 v4, v2\n" \
  "s_mul_i32 s17, s6, 73728\n" \
  "s_add_u32 s17, s17, 1048576\n" \
  "s_mul_i32 s18, s7, 5120\n" \
  "s_lshl_b32 s19, s25, 10\n" \
  "s_add_u32 s18, s18, s19\n" \
  "s_add_u32 s18, s18, 32768\n" \
  "v_mul_f32 v5, 0xbd2aaaab, v4\n" \
  "v_cos_f32 v108, v5\n" \
  "v_sin_f32 v112, v5\n" \
  "v_add_f32 v5, 0xbc2aaaab, v5\n" \
  "v_cos_f32 v109, v5\n" \
  "v_sin_f32 v113, v5\n" \
  "v_add_f32 v5, 0xbc2aaaab, v5\n" \
  "v_cos_f32 v110, v5\n" \
  "v_sin_f32 v114, v5\n" \
  "v_add_f32 v5, 0xbc2aaaab, v5\n" \
  "v_cos_f32 v111, v5\n" \
  "v_sin_f32 v115, v5\n" \
  "v_mul_f32 v5, 0xbdaaaaab, v4\n" \
  "v_cos_f32 v116, v5\n" \
  "v_sin_f32 v120, v5\n" \
  "v_add_f32 v5, 0xbcaaaaab, v5\n" \
  "v_cos_f32 v117, v5\n" \
  "v_sin_f32 v121, v5\n" \
  "v_add_f32 v5, 0xbcaaaaab, v5\n" \
  "v_cos_f32 v118, v5\n" \
  "v_sin_f32 v122, v5\n" \
  "v_add_f32 v5, 0xbcaaaaab, v5\n" \
  "v_cos_f32 v119, v5\n" \
  "v_sin_f32 v123, v5\n" \
  "v_mov_b32 v124, 0\n" \
  "v_mov_b32 v125, 0\n" \
  "v_mov_b32 v126, 0\n" \
  "v_mov_b32 v127, 0\n" \
  "v_mov_b32 v128, 0\n" \
  "v_mov_b32 v129, 0\n" \
  "v_mov_b32 v130, 0\n" \
  "v_mov_b32 v131, 0\n" \
  "v_mov_b32 v132, 0\n" \
  "v_mov_b32 v133, 0\n" \
  "v_mov_b32 v134, 0\n" \
  "v_mov_b32 v135, 0\n" \
  "v_mov_b32 v136, 0\n" \
  "v_mov_b32 v137, 0\n" \
  "v_mov_b32 v138, 0\n" \
  "v_mov_b32 v139, 0\n" \
  "v_mov_b32 v140, 0\n" \
  "v_mov_b32 v141, 0\n" \
  "v_mov_b32 v142, 0\n" \
  "v_mov_b32 v143, 0\n" \
  "v_mov_b32 v144, 0\n" \
  "v_mov_b32 v145, 0\n" \
  "v_mov_b32 v146, 0\n" \
  "v_mov_b32 v147, 0\n" \
  "s_mov_b32 s20, 0x3f5db3d7\n" \
  "s_mov_b32 m0, s19\n" \
  "s_setprio 2\n" \
  "s_cmp_eq_u32 s25, 3\n" \
  "s_barrier\n" \
  "buffer_load_dwordx4 v10, s[40:43], s18 offen sc1 lds\n" \
  "s_cbranch_scc0 .LSt4_%=\n" \
  "buffer_load_dwordx4 v10, s[40:43], s18 offen offset:1024 sc1 lds\n" \
  ".LSt4_%=:\n" \
  "buffer_load_dwordx4 v[148:151], v28, s[40:43], s17 offen offset:0 sc1\n" \
  "buffer_load_dwordx4 v[152:155], v28, s[40:43], s17 offen offset:128 sc1\n" \
  "buffer_load_dwordx4 v[156:159], v28, s[40:43], s17 offen offset:256 sc1\n" \
  "buffer_load_dwordx4 v[160:163], v28, s[40:43], s17 offen offset:384 sc1\n" \
  "buffer_load_dwordx4 v[164:167], v28, s[40:43], s17 offen offset:512 sc1\n" \
  "buffer_load_dwordx4 v[168:171], v28, s[40:43], s17 offen offset:640 sc1\n" \
  "buffer_load_dwordx4 v[172:175], v28, s[40:43], s17 offen offset:768 sc1\n" \
  "buffer_load_dwordx4 v[176:179], v28, s[40:43], s17 offen offset:896 sc1\n" \
  "buffer_load_dwordx4 v[180:183], v28, s[40:43], s17 offen offset:1024 sc1\n" \
  "buffer_load_dwordx4 v[184:187], v28, s[40:43], s17 offen offset:1152 sc1\n" \
  "buffer_load_dwordx4 v[188:191], v28, s[40:43], s17 offen offset:1280 sc1\n" \
  "buffer_load_dwordx4 v[192:195], v28, s[40:43], s17 offen offset:1408 sc1\n" \
  "buffer_load_dwordx4 v[196:199], v28, s[40:43], s17 offen offset:1536 sc1\n" \
  "buffer_load_dwordx4 v[200:203], v28, s[40:43], s17 offen offset:1664 sc1\n" \
  "buffer_load_dwordx4 v[204:207], v28, s[40:43], s17 offen offset:1792 sc1\n" \
  "buffer_load_dwordx4 v[208:211], v28, s[40:43], s17 offen offset:1920 sc1\n" \
  "buffer_load_dwordx4 v[212:215], v28, s[40:43], s17 offen offset:2048 sc1\n" \
  "buffer_load_dwordx4 v[216:219], v28, s[40:43], s17 offen offset:2176 sc1\n" \
  "s_waitcnt vmcnt(18)\n" \
  "s_barrier\n" \
  "s_cmp_eq_u32 s6, 0\n" \
  "s_cbranch_scc0 .LSdcx_%=\n" \
  "v_lshlrev_b32 v2, 1, v29\n" \
  "ds_read_b128 v[124:127], v2 offset:5120\n" \
  "ds_read_b128 v[128:131], v2 offset:5136\n" \
  "ds_read_b128 v[132:135], v2 offset:5376\n" \
  "ds_read_b128 v[136:139], v2 offset:5392\n" \
  "ds_read_b128 v[140:143], v2 offset:5632\n" \
  "ds_read_b128 v[144:147], v2 offset:5648\n" \
  ".LSdcx_%=:\n" \
  "ds_read_b128 v[220:223], v29 offset:0\n" \
  "ds_read_b128 v[224:227], v29 offset:2304\n" \
  "ds_read_b128 v[228:231], v29 offset:128\n" \
  "ds_read_b128 v[232:235], v29 offset:2432\n" \
  "ds_read_b128 v[236:239], v29 offset:256\n" \
  "ds_read_b128 v[240:243], v29 offset:2560\n" \
  "ds_read_b128 v[244:247], v29 offset:384\n" \
  "ds_read_b128 v[248:251], v29 offset:2688\n" \
  "s_waitcnt vmcnt(17) lgkmcnt(6)\n" \
  "v_dot2c_f32_bf16 v124, v148, v220\n" \
  "v_dot2c_f32_bf16 v128, v148, v224\n" \
  "v_dot2c_f32_bf16 v125, v149, v221\n" \
  "v_dot2c_f32_bf16 v129, v149, v225\n" \
  "v_dot2c_f32_bf16 v126, v150, v222\n" \
  "v_dot2c_f32_bf16 v130, v150, v226\n" \
  "v_dot2c_f32_bf16 v127, v151, v223\n" \
  "v_dot2c_f32_bf16 v131, v151, v227\n" \
  "ds_read_b128 v[220:223], v29 offset:512\n" \
  "ds_read_b128 v[224:227], v29 offset:2816\n" \
  "s_waitcnt vmcnt(16) lgkmcnt(6)\n" \
  "v_dot2c_f32_bf16 v132, v152, v228\n" \
  "v_dot2c_f32_bf16 v136, v152, v232\n" \
  "v_dot2c_f32_bf16 v133, v153, v229\n" \
  "v_dot2c_f32_bf16 v137, v153, v233\n" \
  "v_dot2c_f32_bf16 v134, v154, v230\n" \
  "v_dot2c_f32_bf16 v138, v154, v234\n" \
  "v_dot2c_f32_bf16 v135, v155, v231\n" \
  "v_dot2c_f32_bf16 v139, v155, v235\n" \
  "ds_read_b128 v[228:231], v29 offset:640\n" \
  "ds_read_b128 v[232:235], v29 offset:2944\n" \
  "s_waitcnt vmcnt(15) lgkmcnt(6)\n" \
  "v_dot2c_f32_bf16 v140, v156, v236\n" \
  "v_dot2c_f32_bf16 v144, v156, v240\n" \
  "v_dot2c_f32_bf16 v141, v157, v237\n" \
  "v_dot2c_f32_bf16 v145, v157, v241\n" \
  "v_dot2c_f32_bf16 v142, v158, v238\n" \
  "v_dot2c_f32_bf16 v146, v158, v242\n" \
  "v_dot2c_f32_bf16 v143, v159, v239\n" \
  "v_dot2c_f32_bf16 v147, v159, v243\n" \
  "ds_read_b128 v[236:239], v29 offset:768\n" \
  "ds_read_b128 v[240:243], v29 offset:3072\n" \
  "s_waitcnt vmcnt(14) lgkmcnt(6)\n" \
  "v_dot2c_f32_bf16 v124, v160, v244\n" \
  "v_dot2c_f32_bf16 v128, v160, v248\n" \
  "v_dot2c_f32_bf16 v125, v161, v245\n" \
  "v_dot2c_f32_bf16 v129, v161, v249\n" \
  "v_dot2c_f32_bf16 v126, v162, v246\n" \
  "v_dot2c_f32_bf16 v130, v162, v250\n" \
  "v_dot2c_f32_bf16 v127, v163, v247\n" \
  "v_dot2c_f32_bf16 v131, v163, v251\n" \
  "ds_read_b128 v[244:247], v29 offset:896\n" \
  "ds_read_b128 v[248:251], v29 offset:3200\n" \
  "s_waitcnt vmcnt(13) lgkmcnt(6)\n" \
  "v_dot2c_f32_bf16 v132, v164, v220\n" \
  "v_dot2c_f32_bf16 v136, v164, v224\n" \
  "v_dot2c_f32_bf16 v133, v165, v221\n" \
  "v_dot2c_f32_bf16 v137, v165, v225\n" \
  "v_dot2c_f32_bf16 v134, v166, v222\n" \
  "v_dot2c_f32_bf16 v138, v166, v226\n" \
  "v_dot2c_f32_bf16 v135, v167, v223\n" \
  "v_dot2c_f32_bf16 v139, v167, v227\n" \
  "ds_read_b128 v[220:223], v29 offset:1024\n" \
  "ds_read_b128 v[224:227], v29 offset:3328\n" \
  "s_waitcnt vmcnt(12) lgkmcnt(6)\n" \
  "v_dot2c_f32_bf16 v140, v168, v228\n" \
  "v_dot2c_f32_bf16 v144, v168, v232\n" \
  "v_dot2c_f32_bf16 v141, v169, v229\n" \
  "v_dot2c_f32_bf16 v145, v169, v233\n" \
  "v_dot2c_f32_bf16 v142, v170, v230\n" \
  "v_dot2c_f32_bf16 v146, v170, v234\n" \
  "v_dot2c_f32_bf16 v143, v171, v231\n" \
  "v_dot2c_f32_bf16 v147, v171, v235\n" \
  "ds_read_b128 v[228:231], v29 offset:1152\n" \
  "ds_read_b128 v[232:235], v29 offset:3456\n" \
  "s_waitcnt vmcnt(11) lgkmcnt(6)\n" \
  "v_dot2c_f32_bf16 v124, v172, v236\n" \
  "v_dot2c_f32_bf16 v128, v172, v240\n" \
  "v_dot2c_f32_bf16 v125, v173, v237\n" \
  "v_dot2c_f32_bf16 v129, v173, v241\n" \
  "v_dot2c_f32_bf16 v126, v174, v238\n" \
  "v_dot2c_f32_bf16 v130, v174, v242\n" \
  "v_dot2c_f32_bf16 v127, v175, v239\n" \
  "v_dot2c_f32_bf16 v131, v175, v243\n" \
  "ds_read_b128 v[236:239], v29 offset:1280\n" \
  "ds_read_b128 v[240:243], v29 offset:3584\n" \
  "s_waitcnt vmcnt(10) lgkmcnt(6)\n" \
  "v_dot2c_f32_bf16 v132, v176, v244\n" \
  "v_dot2c_f32_bf16 v136, v176, v248\n" \
  "v_dot2c_f32_bf16 v133, v177, v245\n" \
  "v_dot2c_f32_bf16 v137, v177, v249\n" \
  "v_dot2c_f32_bf16 v134, v178, v246\n" \
  "v_dot2c_f32_bf16 v138, v178, v250\n" \
  "v_dot2c_f32_bf16 v135, v179, v247\n" \
  "v_dot2c_f32_bf16 v139, v179, v251\n" \
  "ds_read_b128 v[244:247], v29 offset:1408\n" \
  "ds_read_b128 v[248:251], v29 offset:3712\n" \
  "s_waitcnt vmcnt(9) lgkmcnt(6)\n" \
  "v_dot2c_f32_bf16 v140, v180, v220\n" \
  "v_dot2c_f32_bf16 v144, v180, v224\n" \
  "v_dot2c_f32_bf16 v141, v181, v221\n" \
  "v_dot2c_f32_bf16 v145, v181, v225\n" \
  "v_dot2c_f32_bf16 v142, v182, v222\n" \
  "v_dot2c_f32_bf16 v146, v182, v226\n" \
  "v_dot2c_f32_bf16 v143, v183, v223\n" \
  "v_dot2c_f32_bf16 v147, v183, v227\n" \
  "ds_read_b128 v[220:223], v29 offset:1536\n" \
  "ds_read_b128 v[224:227], v29 offset:3840\n" \
  "s_waitcnt vmcnt(8) lgkmcnt(6)\n" \
  "v_dot2c_f32_bf16 v124, v184, v228\n" \
  "v_dot2c_f32_bf16 v128, v184, v232\n" \
  "v_dot2c_f32_bf16 v125, v185, v229\n" \
  "v_dot2c_f32_bf16 v129, v185, v233\n" \
  "v_dot2c_f32_bf16 v126, v186, v230\n" \
  "v_dot2c_f32_bf16 v130, v186, v234\n" \
  "v_dot2c_f32_bf16 v127, v187, v231\n" \
  "v_dot2c_f32_bf16 v131, v187, v235\n" \
  "ds_read_b128 v[228:231], v29 offset:1664\n" \
  "ds_read_b128 v[232:235], v29 offset:3968\n" \
  "s_waitcnt vmcnt(7) lgkmcnt(6)\n" \
  "v_dot2c_f32_bf16 v132, v188, v236\n" \
  "v_dot2c_f32_bf16 v136, v188, v240\n" \
  "v_dot2c_f32_bf16 v133, v189, v237\n" \
  "v_dot2c_f32_bf16 v137, v189, v241\n" \
  "v_dot2c_f32_bf16 v134, v190, v238\n" \
  "v_dot2c_f32_bf16 v138, v190, v242\n" \
  "v_dot2c_f32_bf16 v135, v191, v239\n" \
  "v_dot2c_f32_bf16 v139, v191, v243\n" \
  "ds_read_b128 v[236:239], v29 offset:1792\n" \
  "ds_read_b128 v[240:243], v29 offset:4096\n" \
  "s_waitcnt vmcnt(6) lgkmcnt(6)\n" \
  "v_dot2c_f32_bf16 v140, v192, v244\n" \
  "v_dot2c_f32_bf16 v144, v192, v248\n" \
  "v_dot2c_f32_bf16 v141, v193, v245\n" \
  "v_dot2c_f32_bf16 v145, v193, v249\n" \
  "v_dot2c_f32_bf16 v142, v194, v246\n" \
  "v_dot2c_f32_bf16 v146, v194, v250\n" \
  "v_dot2c_f32_bf16 v143, v195, v247\n" \
  "v_dot2c_f32_bf16 v147, v195, v251\n" \
  "ds_read_b128 v[244:247], v29 offset:1920\n" \
  "ds_read_b128 v[248:251], v29 offset:4224\n" \
  "s_waitcnt vmcnt(5) lgkmcnt(6)\n" \
  "v_dot2c_f32_bf16 v124, v196, v220\n" \
  "v_dot2c_f32_bf16 v128, v196, v224\n" \
  "v_dot2c_f32_bf16 v125, v197, v221\n" \
  "v_dot2c_f32_bf16 v129, v197, v225\n" \
  "v_dot2c_f32_bf16 v126, v198, v222\n" \
  "v_dot2c_f32_bf16 v130, v198, v226\n" \
  "v_dot2c_f32_bf16 v127, v199, v223\n" \
  "v_dot2c_f32_bf16 v131, v199, v227\n" \
  "ds_read_b128 v[220:223], v29 offset:2048\n" \
  "ds_read_b128 v[224:227], v29 offset:4352\n" \
  "s_waitcnt vmcnt(4) lgkmcnt(6)\n" \
  "v_dot2c_f32_bf16 v132, v200, v228\n" \
  "v_dot2c_f32_bf16 v136, v200, v232\n" \
  "v_dot2c_f32_bf16 v133, v201, v229\n" \
  "v_dot2c_f32_bf16 v137, v201, v233\n" \
  "v_dot2c_f32_bf16 v134, v202, v230\n" \
  "v_dot2c_f32_bf16 v138, v202, v234\n" \
  "v_dot2c_f32_bf16 v135, v203, v231\n" \
  "v_dot2c_f32_bf16 v139, v203, v235\n" \
  "ds_read_b128 v[228:231], v29 offset:2176\n" \
  "ds_read_b128 v[232:235], v29 offset:4480\n" \
  "s_waitcnt vmcnt(3) lgkmcnt(6)\n" \
  "v_dot2c_f32_bf16 v140, v204, v236\n" \
  "v_dot2c_f32_bf16 v144, v204, v240\n" \
  "v_dot2c_f32_bf16 v141, v205, v237\n" \
  "v_dot2c_f32_bf16 v145, v205, v241\n" \
  "v_dot2c_f32_bf16 v142, v206, v238\n" \
  "v_dot2c_f32_bf16 v146, v206, v242\n" \
  "v_dot2c_f32_bf16 v143, v207, v239\n" \
  "v_dot2c_f32_bf16 v147, v207, v243\n" \
  "s_waitcnt vmcnt(2) lgkmcnt(4)\n" \
  "v_dot2c_f32_bf16 v124, v208, v244\n" \
  "v_dot2c_f32_bf16 v128, v208, v248\n" \
  "v_dot2c_f32_bf16 v125, v209, v245\n" \
  "v_dot2c_f32_bf16 v129, v209, v249\n" \
  "v_dot2c_f32_bf16 v126, v210, v246\n" \
  "v_dot2c_f32_bf16 v130, v210, v250\n" \
  "v_dot2c_f32_bf16 v127, v211, v247\n" \
  "v_dot2c_f32_bf16 v131, v211, v251\n" \
  "s_waitcnt vmcnt(1) lgkmcnt(2)\n" \
  "v_dot2c_f32_bf16 v132, v212, v220\n" \
  "v_dot2c_f32_bf16 v136, v212, v224\n" \
  "v_dot2c_f32_bf16 v133, v213, v221\n" \
  "v_dot2c_f32_bf16 v137, v213, v225\n" \
  "v_dot2c_f32_bf16 v134, v214, v222\n" \
  "v_dot2c_f32_bf16 v138, v214, v226\n" \
  "v_dot2c_f32_bf16 v135, v215, v223\n" \
  "v_dot2c_f32_bf16 v139, v215, v227\n" \
  "s_waitcnt vmcnt(0) lgkmcnt(0)\n" \
  "v_dot2c_f32_bf16 v140, v216, v228\n" \
  "v_dot2c_f32_bf16 v144, v216, v232\n" \
  "v_dot2c_f32_bf16 v141, v217, v229\n" \
  "v_dot2c_f32_bf16 v145, v217, v233\n" \
  "v_dot2c_f32_bf16 v142, v218, v230\n" \
  "v_dot2c_f32_bf16 v146, v218, v234\n" \
  "v_dot2c_f32_bf16 v143, v219, v231\n" \
  "v_dot2c_f32_bf16 v147, v219, v235\n" \
  "v_pk_add_f32 v[148:149], v[132:133], v[140:141]\n" \
  "v_pk_add_f32 v[150:151], v[136:137], v[144:145]\n" \
  "v_pk_add_f32 v[152:153], v[132:133], v[140:141] neg_lo:[0,1] neg_hi:[0,1]\n" \
  "v_pk_add_f32 v[154:155], v[136:137], v[144:145] neg_lo:[0,1] neg_hi:[0,1]\n" \
  "v_pk_fma_f32 v[156:157], v[148:149], 0.5, v[124:125] op_sel_hi:[1,0,1] neg_lo:[1,0,0] neg_hi:[1,0,0]\n" \
  "v_pk_fma_f32 v[158:159], v[150:151], 0.5, v[128:129] op_sel_hi:[1,0,1] neg_lo:[1,0,0] neg_hi:[1,0,0]\n" \
  "v_pk_add_f32 v[148:149], v[148:149], v[124:125]\n" \
  "v_pk_add_f32 v[150:151], v[150:151], v[128:129]\n" \
  "v_cvt_pk_bf16_f32 v196, v148, v149\n" \
  "v_cvt_pk_bf16_f32 v198, v150, v151\n" \
  "v_add_u32 v31, 5888, v30\n" \
  "v_pk_fma_f32 v[160:161], v[154:155], s[20:21], v[156:157] op_sel_hi:[1,0,1]\n" \
  "v_pk_fma_f32 v[162:163], v[152:153], s[20:21], v[158:159] op_sel_hi:[1,0,1] neg_lo:[1,0,0] neg_hi:[1,0,0]\n" \
  "v_pk_fma_f32 v[164:165], v[154:155], s[20:21], v[156:157] op_sel_hi:[1,0,1] neg_lo:[1,0,0] neg_hi:[1,0,0]\n" \
  "v_pk_fma_f32 v[166:167], v[152:153], s[20:21], v[158:159] op_sel_hi:[1,0,1]\n" \
  "v_pk_mul_f32 v[168:169], v[162:163], v[112:113]\n" \
  "v_pk_mul_f32 v[170:171], v[160:161], v[112:113]\n" \
  "v_pk_fma_f32 v[168:169], v[160:161], v[108:109], v[168:169] neg_lo:[0,0,1] neg_hi:[0,0,1]\n" \
  "v_pk_fma_f32 v[170:171], v[162:163], v[108:109], v[170:171]\n" \
  "v_cvt_pk_bf16_f32 v200, v168, v169\n" \
  "v_cvt_pk_bf16_f32 v202, v170, v171\n" \
  "v_pk_mul_f32 v[168:169], v[166:167], v[120:121]\n" \
  "v_pk_mul_f32 v[170:171], v[164:165], v[120:121]\n" \
  "v_pk_fma_f32 v[168:169], v[164:165], v[116:117], v[168:169] neg_lo:[0,0,1] neg_hi:[0,0,1]\n" \
  "v_pk_fma_f32 v[170:171], v[166:167], v[116:117], v[170:171]\n" \
  "v_cvt_pk_bf16_f32 v204, v168, v169\n" \
  "v_cvt_pk_bf16_f32 v206, v170, v171\n" \
  "v_pk_add_f32 v[172:173], v[134:135], v[142:143]\n" \
  "v_pk_add_f32 v[174:175], v[138:139], v[146:147]\n" \
  "v_pk_add_f32 v[176:177], v[134:135], v[142:143] neg_lo:[0,1] neg_hi:[0,1]\n" \
  "v_pk_add_f32 v[178:179], v[138:139], v[146:147] neg_lo:[0,1] neg_hi:[0,1]\n" \
  "v_pk_fma_f32 v[180:181], v[172:173], 0.5, v[126:127] op_sel_hi:[1,0,1] neg_lo:[1,0,0] neg_hi:[1,0,0]\n" \
  "v_pk_fma_f32 v[182:183], v[174:175], 0.5, v[130:131] op_sel_hi:[1,0,1] neg_lo:[1,0,0] neg_hi:[1,0,0]\n" \
  "v_pk_add_f32 v[172:173], v[172:173], v[126:127]\n" \
  "v_pk_add_f32 v[174:175], v[174:175], v[130:131]\n" \
  "v_cvt_pk_bf16_f32 v197, v172, v173\n" \
  "v_cvt_pk_bf16_f32 v199, v174, v175\n" \
  "ds_write2st64_b64 v31, v[196:197], v[198:199] offset0:0 offset1:5\n" \
  "v_pk_fma_f32 v[184:185], v[178:179], s[20:21], v[180:181] op_sel_hi:[1,0,1]\n" \
  "v_pk_fma_f32 v[186:187], v[176:177], s[20:21], v[182:183] op_sel_hi:[1,0,1] neg_lo:[1,0,0] neg_hi:[1,0,0]\n" \
  "v_pk_fma_f32 v[188:189], v[178:179], s[20:21], v[180:181] op_sel_hi:[1,0,1] neg_lo:[1,0,0] neg_hi:[1,0,0]\n" \
  "v_pk_fma_f32 v[190:191], v[176:177], s[20:21], v[182:183] op_sel_hi:[1,0,1]\n" \
  "v_pk_mul_f32 v[192:193], v[186:187], v[114:115]\n" \
  "v_pk_mul_f32 v[194:195], v[184:185], v[114:115]\n" \
  "v_pk_fma_f32 v[192:193], v[184:185], v[110:111], v[192:193] neg_lo:[0,0,1] neg_hi:[0,0,1]\n" \
  "v_pk_fma_f32 v[194:195], v[186:187], v[110:111], v[194:195]\n" \
  "v_cvt_pk_bf16_f32 v201, v192, v193\n" \
  "v_cvt_pk_bf16_f32 v203, v194, v195\n" \
  "ds_write2st64_b64 v31, v[200:201], v[202:203] offset0:10 offset1:15\n" \
  "v_pk_mul_f32 v[192:193], v[190:191], v[122:123]\n" \
  "v_pk_mul_f32 v[194:195], v[188:189], v[122:123]\n" \
  "v_pk_fma_f32 v[192:193], v[188:189], v[118:119], v[192:193] neg_lo:[0,0,1] neg_hi:[0,0,1]\n" \
  "v_pk_fma_f32 v[194:195], v[190:191], v[118:119], v[194:195]\n" \
  "v_cvt_pk_bf16_f32 v205, v192, v193\n" \
  "v_cvt_pk_bf16_f32 v207, v194, v195\n" \
  "ds_write2st64_b64 v31, v[204:205], v[206:207] offset0:20 offset1:25\n" \
  "s_setprio 0\n" \
  "s_waitcnt lgkmcnt(0)\n" \
  "s_barrier\n" \
  ".LSq2x_%=:\n" \
  "s_mov_b32 s8, s80\n" \
  "s_and_b32 s9, s81, 0xffff\n" \
  "s_mov_b32 s10, 2359296\n" \
  "s_mov_b32 s11, 0x20000\n" \
  "buffer_load_dwordx2 v[168:169], v44, s[8:11], 0 offen\n" \
  "buffer_load_dwordx2 v[170:171], v45, s[8:11], 0 offen\n" \
  "buffer_load_dwordx2 v[172:173], v46, s[8:11], 0 offen\n" \
  "buffer_load_dwordx2 v[174:175], v47, s[8:11], 0 offen\n" \
  "buffer_load_dwordx2 v[176:177], v48, s[8:11], 0 offen\n" \
  "s_movk_i32 s16, 18688\n" \
  "buffer_load_dwordx2 v[84:85], v32, s[40:43], s16 offen sc1\n" \
  "buffer_load_dwordx2 v[86:87], v33, s[40:43], s16 offen sc1\n" \
  "buffer_load_dwordx2 v[88:89], v34, s[40:43], s16 offen sc1\n" \
  "buffer_load_dwordx2 v[90:91], v35, s[40:43], s16 offen sc1\n" \
  "buffer_load_dwordx2 v[92:93], v36, s[40:43], s16 offen sc1\n" \
  "buffer_load_dwordx4 v[160:163], v27, s[40:43], s16 offen offset:2304 sc1\n" \
  "buffer_load_dwordx4 v[164:167], v27, s[40:43], s16 offen offset:1152 sc1\n" \
  "s_and_b32 s16, s4, 3\n" \
  "s_and_b32 s17, s4, 4\n" \
  "s_lshr_b32 s18, s5, 3\n" \
  "s_add_u32 s17, s17, s18\n" \
  "s_and_b32 s18, s5, 7\n" \
  "s_lshr_b32 s19, s18, 2\n" \
  "s_and_b32 s20, s18, 3\n" \
  "s_lshr_b32 s21, s3, 2\n" \
  "s_and_b32 s22, s3, 3\n" \
  "s_cmp_lt_u32 s3, 4\n" \
  "s_cselect_b32 s48, s86, s88\n" \
  "s_cselect_b32 s49, s87, s89\n" \
  "s_mov_b32 s50, s96\n" \
  "s_mov_b32 s51, s97\n" \
  "s_mov_b32 s52, s98\n" \
  "s_mov_b32 s53, s99\n" \
  "s_lshl_b32 s23, s16, 1\n" \
  "s_add_u32 s23, s23, s19\n" \
  "s_mul_i32 s54, s23, 1152\n" \
  "s_lshl_b32 s24, s20, 8\n" \
  "s_add_u32 s54, s54, s24\n" \
  "s_lshl_b32 s57, s3, 10\n" \
  "s_lshl_b32 s24, s16, 3\n" \
  "s_add_u32 s24, s24, s18\n" \
  "s_mul_i32 s55, s24, 40960\n" \
  "s_add_u32 s55, s55, s57\n" \
  "s_add_u32 s55, s55, 48234496\n" \
  "s_lshl_b32 s24, s17, 3\n" \
  "s_add_u32 s25, s24, s3\n" \
  "s_lshl_b32 s56, s25, 2\n" \
  "s_add_u32 s56, s56, s16\n" \
  "s_lshl_b32 s56, s56, 6\n" \
  "s_add_u32 s56, s56, 262144\n" \
  "s_mul_i32 s60, s25, 36864\n" \
  "s_add_u32 s60, s60, 13631488\n" \
  "s_lshl_b32 s58, s4, 11\n" \
  "s_add_u32 s58, s58, 147456\n" \
  "s_add_u32 s25, s24, s23\n" \
  "s_lshl_b32 s25, s25, 3\n" \
  "s_add_u32 s25, s25, s3\n" \
  "s_mul_i32 s61, s25, 36864\n" \
  "s_add_u32 s61, s61, 16777216\n" \
  "s_lshl_b32 s66, s25, 6\n" \
  "s_add_u32 s66, s66, 229376\n" \
  "s_lshr_b32 s26, s20, 1\n" \
  "s_lshl_b32 s27, s26, 11\n" \
  "s_and_b32 s28, s20, 1\n" \
  "s_lshl_b32 s28, s28, 9\n" \
  "s_add_u32 s62, s61, s27\n" \
  "s_add_u32 s62, s62, s28\n" \
  "s_add_u32 s62, s62, 16384\n" \
  "s_lshl_b32 s27, s20, 11\n" \
  "s_add_u32 s61, s61, s27\n" \
  "s_lshl_b32 s25, s16, 1\n" \
  "s_add_u32 s25, s25, s24\n" \
  "s_add_u32 s25, s25, s21\n" \
  "s_lshl_b32 s25, s25, 1\n" \
  "s_add_u32 s25, s25, s19\n" \
  "s_lshl_b32 s25, s25, 2\n" \
  "s_add_u32 s25, s25, s22\n" \
  "s_mul_i32 s63, s25, 36864\n" \
  "s_add_u32 s63, s63, 16777216\n" \
  "s_lshl_b32 s67, s25, 6\n" \
  "s_add_u32 s67, s67, 229376\n" \
  "s_lshl_b32 s27, s21, 10\n" \
  "s_add_u32 s63, s63, s27\n" \
  "s_xor_b32 s64, s63, 1024\n" \
  "s_lshl_b32 s29, s16, 2\n" \
  "s_add_u32 s25, s29, s20\n" \
  "s_mul_i32 s65, s25, 335872\n" \
  "s_add_u32 s65, s65, s57\n" \
  "s_add_u32 s65, s65, 41943040\n" \
  "s_add_u32 s25, s24, s18\n" \
  "s_lshl_b32 s25, s25, 5\n" \
  "s_add_u32 s25, s25, s3\n" \
  "s_lshl_b32 s27, s25, 13\n" \
  "s_add_u32 s27, s27, 50331648\n" \
  "s_lshl_b32 s25, s25, 5\n" \
  "s_add_u32 s25, s25, 163840\n" \
  "s_add_u32 s29, s16, 0\n" \
  "s_and_b32 s29, s29, 3\n" \
  "s_lshl_b32 s73, s29, 16\n" \
  "s_add_u32 s73, s73, s27\n" \
  "s_lshl_b32 s29, s29, 8\n" \
  "s_add_u32 s76, s29, s25\n" \
  "s_add_u32 s29, s16, 1\n" \
  "s_and_b32 s29, s29, 3\n" \
  "s_lshl_b32 s68, s29, 16\n" \
  "s_add_u32 s68, s68, s27\n" \
  "s_lshl_b32 s29, s29, 8\n" \
  "s_add_u32 s78, s29, s25\n" \
  "s_add_u32 s29, s16, 2\n" \
  "s_and_b32 s29, s29, 3\n" \
  "s_lshl_b32 s69, s29, 16\n" \
  "s_add_u32 s69, s69, s27\n" \
  "s_lshl_b32 s29, s29, 8\n" \
  "s_add_u32 s79, s29, s25\n" \
  "s_add_u32 s29, s16, 3\n" \
  "s_and_b32 s29, s29, 3\n" \
  "s_lshl_b32 s70, s29, 16\n" \
  "s_add_u32 s70, s70, s27\n" \
  "s_lshl_b32 s29, s29, 8\n" \
  "s_add_u32 s59, s29, s25\n" \
  "s_sub_u32 s29, s22, s16\n" \
  "s_sub_u32 s29, s29, 1\n" \
  "s_and_b32 s29, s29, 3\n" \
  "s_add_u32 s74, s29, 1\n" \
  "s_lshr_b32 s74, s74, 2\n" \
  "s_lshl_b32 s71, s22, 7\n" \
  "s_add_u32 s71, s71, s29\n" \
  "s_lshl_b32 s71, s71, 9\n" \
  "s_add_u32 s71, s71, s27\n" \
  "s_add_u32 s71, s71, 3072\n" \
  "s_lshl_b32 s72, s22, 8\n" \
  "s_add_u32 s72, s72, s25\n" \
  "s_lshl_b32 s25, s17, 1\n" \
  "s_add_u32 s25, s25, s19\n" \
  "s_lshl_b32 s25, s25, 2\n" \
  "s_add_u32 s25, s25, s22\n" \
  "s_lshl_b32 s25, s25, 1\n" \
  "s_add_u32 s25, s25, s21\n" \
  "s_mul_i32 s27, s25, 18432\n" \
  "s_add_u32 s27, s27, 10485760\n" \
  "s_lshl_b32 s29, s20, 2\n" \
  "s_add_u32 s29, s29, s16\n" \
  "s_lshl_b32 s75, s29, 10\n" \
  "s_add_u32 s75, s75, s27\n" \
  "s_lshl_b32 s77, s20, 9\n" \
  "s_add_u32 s77, s77, s27\n" \
  "s_add_u32 s77, s77, 16384\n" \
  "s_lshr_b32 s31, s2, 1\n" \
  "s_lshl_b32 s31, s31, 13\n" \
  "s_and_b32 s27, s2, 1\n" \
  "s_cmp_eq_u32 s27, s21\n" \
  "s_cselect_b32 s31, s31, 0x40000000\n" \
  "s_cmp_lt_u32 s2, 216\n" \
  "s_cselect_b32 s31, s31, 0x40000000\n" \
  "s_or_b32 s31, s31, s22\n" \
  "v_lshrrev_b32 v2, 3, v1\n" \
  "s_lshl_b32 s27, s22, 3\n" \
  "v_add_u32 v2, s27, v2\n" \
  "v_mul_u32_u24 v8, 0x2400, v2\n" \
  "v_bfe_u32 v3, v1, 4, 1\n" \
  "s_and_b32 s27, s22, 1\n" \
  "s_lshl_b32 s27, s27, 1\n" \
  "v_or_b32 v3, s27, v3\n" \
  "v_lshlrev_b32 v3, 5, v3\n" \
  "v_and_b32 v4, 0x70, v10\n" \
  "v_xor_b32 v3, v3, v4\n" \
  "v_add_u32 v8, v8, v3\n" \
  "v_mov_b32 v9, v10\n" \
  "v_lshlrev_b32 v11, 3, v1\n" \
  "v_lshrrev_b32 v2, 4, v1\n" \
  "v_bfe_u32 v3, v1, 2, 2\n" \
  "v_lshl_add_u32 v4, v2, 3, v3\n" \
  "v_lshlrev_b32 v4, 7, v4\n" \
  "v_and_b32 v5, 24, v11\n" \
  "v_or_b32 v4, v4, v5\n" \
  "v_bfe_u32 v5, v1, 3, 2\n" \
  "v_lshl_or_b32 v12, v5, 5, v4\n" \
  "v_xor_b32 v13, 32, v12\n" \
  "v_xor_b32 v14, 64, v12\n" \
  "v_xor_b32 v15, 0x60, v12\n" \
  "v_and_b32 v3, 15, v1\n" \
  "v_lshrrev_b32 v4, 5, v1\n" \
  "v_lshl_add_u32 v4, v4, 4, v3\n" \
  "v_and_b32 v5, 1, v2\n" \
  "v_lshlrev_b32 v5, 3, v5\n" \
  "v_lshl_or_b32 v16, v4, 4, v5\n" \
  "s_lshl_b32 s27, s21, 12\n" \
  "v_add_u32 v17, s27, v10\n" \
  "s_mul_i32 s27, s3, 5120\n" \
  "s_add_u32 s27, s27, 98304\n" \
  "v_add_u32 v18, s27, v10\n" \
  "s_xor_b32 s27, s3, 4\n" \
  "s_mul_i32 s27, s27, 5120\n" \
  "s_add_u32 s27, s27, 98304\n" \
  "v_add_u32 v19, s27, v10\n" \
  "s_and_b32 s27, s25, 3\n" \
  "s_lshl_b32 s27, s27, 9\n" \
  "s_lshr_b32 s25, s25, 2\n" \
  "s_add_u32 s25, s25, s27\n" \
  "s_mul_i32 s25, s25, 1152\n" \
  "v_mul_u32_u24 v3, 0x9000, v3\n" \
  "v_add_u32 v3, s25, v3\n" \
  "s_lshl_b32 s29, s29, 5\n" \
  "v_lshl_add_u32 v4, v2, 3, s29\n" \
  "s_lshl_b32 s27, s20, 4\n" \
  "s_add_u32 s27, s27, 512\n" \
  "v_lshl_add_u32 v5, v2, 2, s27\n" \
  "v_lshl_add_u32 v20, v4, 1, v3\n" \
  "v_lshl_add_u32 v21, v5, 1, v3\n" \
  "s_movk_i32 s27, 11520\n" \
  "v_lshl_add_u32 v22, v4, 2, s27\n" \
  "v_lshl_add_u32 v23, v5, 2, s27\n" \
  "s_barrier\n" \
  "s_waitcnt vmcnt(0)\n" \
  "s_mov_b32 s18, 0x35e38e39\n" \
  "v_lshlrev_b32 v124, 16, v84\n" \
  "v_and_b32 v125, 0xffff0000, v84\n" \
  "v_pk_mul_f32 v[124:125], v[124:125], s[18:19] op_sel_hi:[1,0]\n" \
  "v_lshlrev_b32 v126, 16, v85\n" \
  "v_and_b32 v127, 0xffff0000, v85\n" \
  "v_pk_mul_f32 v[126:127], v[126:127], s[18:19] op_sel_hi:[1,0]\n" \
  "v_lshlrev_b32 v128, 16, v86\n" \
  "v_and_b32 v129, 0xffff0000, v86\n" \
  "v_pk_mul_f32 v[128:129], v[128:129], s[18:19] op_sel_hi:[1,0]\n" \
  "v_lshlrev_b32 v130, 16, v87\n" \
  "v_and_b32 v131, 0xffff0000, v87\n" \
  "v_pk_mul_f32 v[130:131], v[130:131], s[18:19] op_sel_hi:[1,0]\n" \
  "v_lshlrev_b32 v132, 16, v88\n" \
  "v_and_b32 v133, 0xffff0000, v88\n" \
  "v_pk_mul_f32 v[132:133], v[132:133], s[18:19] op_sel_hi:[1,0]\n" \
  "v_lshlrev_b32 v134, 16, v89\n" \
  "v_and_b32 v135, 0xffff0000, v89\n" \
  "v_pk_mul_f32 v[134:135], v[134:135], s[18:19] op_sel_hi:[1,0]\n" \
  "v_lshlrev_b32 v136, 16, v90\n" \
  "v_and_b32 v137, 0xffff0000, v90\n" \
  "v_pk_mul_f32 v[136:137], v[136:137], s[18:19] op_sel_hi:[1,0]\n" \
  "v_lshlrev_b32 v138, 16, v91\n" \
  "v_and_b32 v139, 0xffff0000, v91\n" \
  "v_pk_mul_f32 v[138:139], v[138:139], s[18:19] op_sel_hi:[1,0]\n" \
  "v_lshlrev_b32 v140, 16, v92\n" \
  "v_and_b32 v141, 0xffff0000, v92\n" \
  "v_pk_mul_f32 v[140:141], v[140:141], s[18:19] op_sel_hi:[1,0]\n" \
  "v_lshlrev_b32 v142, 16, v93\n" \
  "v_and_b32 v143, 0xffff0000, v93\n" \
  "v_pk_mul_f32 v[142:143], v[142:143], s[18:19] op_sel_hi:[1,0]\n" \
  "v_lshlrev_b32 v144, 16, v160\n" \
  "v_and_b32 v145, 0xffff0000, v160\n" \
  "v_lshlrev_b32 v152, 16, v164\n" \
  "v_and_b32 v153, 0xffff0000, v164\n" \
  "v_lshlrev_b32 v146, 16, v161\n" \
  "v_and_b32 v147, 0xffff0000, v161\n" \
  "v_lshlrev_b32 v154, 16, v165\n" \
  "v_and_b32 v155, 0xffff0000, v165\n" \
  "v_lshlrev_b32 v148, 16, v162\n" \
  "v_and_b32 v149, 0xffff0000, v162\n" \
  "v_lshlrev_b32 v156, 16, v166\n" \
  "v_and_b32 v157, 0xffff0000, v166\n" \
  "v_lshlrev_b32 v150, 16, v163\n" \
  "v_and_b32 v151, 0xffff0000, v163\n" \
  "v_lshlrev_b32 v158, 16, v167\n" \
  "v_and_b32 v159, 0xffff0000, v167\n" \
  "v_pk_add_f32 v[144:145], v[144:145], 1.0 op_sel_hi:[1,0]\n" \
  "v_pk_add_f32 v[146:147], v[146:147], 1.0 op_sel_hi:[1,0]\n" \
  "v_pk_add_f32 v[148:149], v[148:149], 1.0 op_sel_hi:[1,0]\n" \
  "v_pk_add_f32 v[150:151], v[150:151], 1.0 op_sel_hi:[1,0]\n" \
  "v_lshlrev_b32 v44, 16, v168\n" \
  "v_and_b32 v45, 0xffff0000, v168\n" \
  "v_lshlrev_b32 v46, 16, v169\n" \
  "v_and_b32 v47, 0xffff0000, v169\n" \
  "v_lshlrev_b32 v48, 16, v170\n" \
  "v_and_b32 v49, 0xffff0000, v170\n" \
  "v_lshlrev_b32 v50, 16, v171\n" \
  "v_and_b32 v51, 0xffff0000, v171\n" \
  "v_lshlrev_b32 v52, 16, v172\n" \
  "v_and_b32 v53, 0xffff0000, v172\n" \
  "v_lshlrev_b32 v54, 16, v173\n" \
  "v_and_b32 v55, 0xffff0000, v173\n" \
  "v_lshlrev_b32 v56, 16, v174\n" \
  "v_and_b32 v57, 0xffff0000, v174\n" \
  "v_lshlrev_b32 v58, 16, v175\n" \
  "v_and_b32 v59, 0xffff0000, v175\n" \
  "v_lshlrev_b32 v60, 16, v176\n" \
  "v_and_b32 v61, 0xffff0000, v176\n" \
  "v_lshlrev_b32 v62, 16, v177\n" \
  "v_and_b32 v63, 0xffff0000, v177\n" \
  "v_mov_b32 v230, 0\n" \
  "v_mov_b32 v231, 0\n" \
  "v_mov_b32 v234, 0\n" \
  "v_mov_b32 v235, 0\n" \
  "v_mov_b32 v238, 0\n" \
  "v_mov_b32 v239, 0\n" \
  "v_mov_b32 v242, 0\n" \
  "v_mov_b32 v243, 0\n" \
  "v_mov_b32 v2, 0\n" \
  "v_mov_b32 v3, 0\n" \
  "s_setprio 3\n" \
  "s_barrier\n" \
  "s_barrier\n" \
  "ds_read_b64_tr_b16 v[228:229], v96 offset:0\n" \
  "ds_read_b64_tr_b16 v[196:197], v94 offset:0\n" \
  "ds_read_b64_tr_b16 v[198:199], v94 offset:512\n" \
  "ds_read_b64_tr_b16 v[200:201], v95 offset:0\n" \
  "ds_read_b64_tr_b16 v[202:203], v95 offset:512\n" \
  "ds_read_b64_tr_b16 v[204:205], v94 offset:4096\n" \
  "ds_read_b64_tr_b16 v[206:207], v94 offset:4608\n" \
  "ds_read_b64_tr_b16 v[208:209], v95 offset:4096\n" \
  "ds_read_b64_tr_b16 v[210:211], v95 offset:4608\n" \
  "ds_read_b64_tr_b16 v[232:233], v96 offset:8448\n" \
  "ds_read_b64_tr_b16 v[212:213], v94 offset:8448\n" \
  "ds_read_b64_tr_b16 v[214:215], v94 offset:8960\n" \
  "s_waitcnt lgkmcnt(9)\n" \
  "v_mfma_f32_16x16x32_bf16 v[160:163], v[228:231], v[80:83], 0\n" \
  "v_mfma_f32_16x16x32_bf16 v[160:163], v[196:199], v[64:67], v[160:163]\n" \
  "ds_read_b64_tr_b16 v[216:217], v95 offset:8448\n" \
  "ds_read_b64_tr_b16 v[218:219], v95 offset:8960\n" \
  "s_waitcnt lgkmcnt(9)\n" \
  "v_mfma_f32_16x16x32_bf16 v[160:163], v[200:203], v[72:75], v[160:163]\n" \
  "ds_read_b64_tr_b16 v[220:221], v94 offset:12544\n" \
  "ds_read_b64_tr_b16 v[222:223], v94 offset:13056\n" \
  "s_waitcnt lgkmcnt(9)\n" \
  "v_mfma_f32_16x16x32_bf16 v[160:163], v[204:207], v[68:71], v[160:163]\n" \
  "ds_read_b64_tr_b16 v[224:225], v95 offset:12544\n" \
  "ds_read_b64_tr_b16 v[226:227], v95 offset:13056\n" \
  "s_waitcnt lgkmcnt(9)\n" \
  "v_mfma_f32_16x16x32_bf16 v[160:163], v[208:211], v[76:79], v[160:163]\n" \
  "ds_read_b64_tr_b16 v[236:237], v96 offset:16896\n" \
  "ds_read_b64_tr_b16 v[196:197], v94 offset:16896\n" \
  "ds_read_b64_tr_b16 v[198:199], v94 offset:17408\n" \
  "s_waitcnt lgkmcnt(9)\n" \
  "v_mfma_f32_16x16x32_bf16 v[164:167], v[232:235], v[80:83], 0\n" \
  "v_mfma_f32_16x16x32_bf16 v[164:167], v[212:215], v[64:67], v[164:167]\n" \
  "ds_read_b64_tr_b16 v[200:201], v95 offset:16896\n" \
  "ds_read_b64_tr_b16 v[202:203], v95 offset:17408\n" \
  "s_waitcnt lgkmcnt(9)\n" \
  "v_mfma_f32_16x16x32_bf16 v[164:167], v[216:219], v[72:75], v[164:167]\n" \
  "ds_read_b64_tr_b16 v[204:205], v94 offset:20992\n" \
  "ds_read_b64_tr_b16 v[206:207], v94 offset:21504\n" \
  "s_waitcnt lgkmcnt(9)\n" \
  "v_mfma_f32_16x16x32_bf16 v[164:167], v[220:223], v[68:71], v[164:167]\n" \
  "ds_read_b64_tr_b16 v[208:209], v95 offset:20992\n" \
  "ds_read_b64_tr_b16 v[210:211], v95 offset:21504\n" \
  "s_waitcnt lgkmcnt(9)\n" \
  "v_mfma_f32_16x16x32_bf16 v[164:167], v[224:227], v[76:79], v[164:167]\n" \
  "s_barrier\n" \
  "ds_read_b64_tr_b16 v[240:241], v96 offset:25344\n" \
  "ds_read_b64_tr_b16 v[212:213], v94 offset:25344\n" \
  "ds_read_b64_tr_b16 v[214:215], v94 offset:25856\n" \
  "s_waitcnt lgkmcnt(9)\n" \
  "v_mfma_f32_16x16x32_bf16 v[168:171], v[236:239], v[80:83], 0\n" \
  "v_mfma_f32_16x16x32_bf16 v[168:171], v[196:199], v[64:67], v[168:171]\n" \
  "ds_read_b64_tr_b16 v[216:217], v95 offset:25344\n" \
  "ds_read_b64_tr_b16 v[218:219], v95 offset:25856\n" \
  "s_waitcnt lgkmcnt(9)\n" \
  "v_mfma_f32_16x16x32_bf16 v[168:171], v[200:203], v[72:75], v[168:171]\n" \
  "ds_read_b64_tr_b16 v[220:221], v94 offset:29440\n" \
  "ds_read_b64_tr_b16 v[222:223], v94 offset:29952\n" \
  "s_waitcnt lgkmcnt(9)\n" \
  "v_mfma_f32_16x16x32_bf16 v[168:171], v[204:207], v[68:71], v[168:171]\n" \
  "ds_read_b64_tr_b16 v[224:225], v95 offset:29440\n" \
  "ds_read_b64_tr_b16 v[226:227], v95 offset:29952\n" \
  "s_waitcnt lgkmcnt(9)\n" \
  "v_mfma_f32_16x16x32_bf16 v[168:171], v[208:211], v[76:79], v[168:171]\n" \
  "ds_read_b64_tr_b16 v[228:229], v96 offset:33792\n" \
  "ds_read_b64_tr_b16 v[196:197], v94 offset:33792\n" \
  "ds_read_b64_tr_b16 v[198:199], v94 offset:34304\n" \
  "s_waitcnt lgkmcnt(9)\n" \
  "v_mfma_f32_16x16x32_bf16 v[172:175], v[240:243], v[80:83], 0\n" \
  "v_mfma_f32_16x16x32_bf16 v[172:175], v[212:215], v[64:67], v[172:175]\n" \
  "ds_read_b64_tr_b16 v[200:201], v95 offset:33792\n" \
  "ds_read_b64_tr_b16 v[202:203], v95 offset:34304\n" \
  "s_waitcnt lgkmcnt(9)\n" \
  "v_mfma_f32_16x16x32_bf16 v[172:175], v[216:219], v[72:75], v[172:175]\n" \
  "ds_read_b64_tr_b16 v[204:205], v94 offset:37888\n" \
  "ds_read_b64_tr_b16 v[206:207], v94 offset:38400\n" \
  "s_waitcnt lgkmcnt(9)\n" \
  "v_mfma_f32_16x16x32_bf16 v[172:175], v[220:223], v[68:71], v[172:175]\n" \
  "ds_read_b64_tr_b16 v[208:209], v95 offset:37888\n" \
  "ds_read_b64_tr_b16 v[210:211], v95 offset:38400\n" \
  "s_waitcnt lgkmcnt(9)\n" \
  "v_mfma_f32_16x16x32_bf16 v[172:175], v[224:227], v[76:79], v[172:175]\n" \
  "ds_read_b64_tr_b16 v[232:233], v96 offset:42240\n" \
  "ds_read_b64_tr_b16 v[212:213], v94 offset:42240\n" \
  "ds_read_b64_tr_b16 v[214:215], v94 offset:42752\n" \
  "s_waitcnt lgkmcnt(9)\n" \
  "v_mfma_f32_16x16x32_bf16 v[176:179], v[228:231], v[80:83], 0\n" \
  "v_mfma_f32_16x16x32_bf16 v[176:179], v[196:199], v[64:67], v[176:179]\n" \
  "ds_read_b64_tr_b16 v[216:217], v95 offset:42240\n" \
  "ds_read_b64_tr_b16 v[218:219], v95 offset:42752\n" \
  "s_waitcnt lgkmcnt(9)\n" \
  "v_mfma_f32_16x16x32_bf16 v[176:179], v[200:203], v[72:75], v[176:179]\n" \
  "ds_read_b64_tr_b16 v[220:221], v94 offset:46336\n" \
  "ds_read_b64_tr_b16 v[222:223], v94 offset:46848\n" \
  "s_waitcnt lgkmcnt(9)\n" \
  "v_mfma_f32_16x16x32_bf16 v[176:179], v[204:207], v[68:71], v[176:179]\n" \
  "ds_read_b64_tr_b16 v[224:225], v95 offset:46336\n" \
  "ds_read_b64_tr_b16 v[226:227], v95 offset:46848\n" \
  "s_waitcnt lgkmcnt(9)\n" \
  "v_mfma_f32_16x16x32_bf16 v[176:179], v[208:211], v[76:79], v[176:179]\n" \
  "s_barrier\n" \
  "v_add_u32 v94, 50688, v94\n" \
  "v_add_u32 v95, 50688, v95\n" \
  "v_add_u32 v96, 50688, v96\n" \
  "ds_read_b64_tr_b16 v[236:237], v96 offset:0\n" \
  "ds_read_b64_tr_b16 v[196:197], v94 offset:0\n" \
  "ds_read_b64_tr_b16 v[198:199], v94 offset:512\n" \
  "s_waitcnt lgkmcnt(9)\n" \
  "v_mfma_f32_16x16x32_bf16 v[180:183], v[232:235], v[80:83], 0\n" \
  "v_mfma_f32_16x16x32_bf16 v[180:183], v[212:215], v[64:67], v[180:183]\n" \
  "ds_read_b64_tr_b16 v[200:201], v95 offset:0\n" \
  "ds_read_b64_tr_b16 v[202:203], v95 offset:512\n" \
  "s_waitcnt lgkmcnt(9)\n" \
  "v_mfma_f32_16x16x32_bf16 v[180:183], v[216:219], v[72:75], v[180:183]\n" \
  "ds_read_b64_tr_b16 v[204:205], v94 offset:4096\n" \
  "ds_read_b64_tr_b16 v[206:207], v94 offset:4608\n" \
  "s_waitcnt lgkmcnt(9)\n" \
  "v_mfma_f32_16x16x32_bf16 v[180:183], v[220:223], v[68:71], v[180:183]\n" \
  "ds_read_b64_tr_b16 v[208:209], v95 offset:4096\n" \
  "ds_read_b64_tr_b16 v[210:211], v95 offset:4608\n" \
  "s_waitcnt lgkmcnt(9)\n" \
  "v_mfma_f32_16x16x32_bf16 v[180:183], v[224:227], v[76:79], v[180:183]\n" \
  "ds_read_b64_tr_b16 v[240:241], v96 offset:8448\n" \
  "ds_read_b64_tr_b16 v[212:213], v94 offset:8448\n" \
  "ds_read_b64_tr_b16 v[214:215], v94 offset:8960\n" \
  "s_waitcnt lgkmcnt(9)\n" \
  "v_mfma_f32_16x16x32_bf16 v[184:187], v[236:239], v[80:83], 0\n" \
  "v_mfma_f32_16x16x32_bf16 v[184:187], v[196:199], v[64:67], v[184:187]\n" \
  "ds_read_b64_tr_b16 v[216:217], v95 offset:8448\n" \
  "ds_read_b64_tr_b16 v[218:219], v95 offset:8960\n" \
  "s_waitcnt lgkmcnt(9)\n" \
  "v_mfma_f32_16x16x32_bf16 v[184:187], v[200:203], v[72:75], v[184:187]\n" \
  "ds_read_b64_tr_b16 v[220:221], v94 offset:12544\n" \
  "ds_read_b64_tr_b16 v[222:223], v94 offset:13056\n" \
  "s_waitcnt lgkmcnt(9)\n" \
  "v_mfma_f32_16x16x32_bf16 v[184:187], v[204:207], v[68:71], v[184:187]\n" \
  "ds_read_b64_tr_b16 v[224:225], v95 offset:12544\n" \
  "ds_read_b64_tr_b16 v[226:227], v95 offset:13056\n" \
  "s_waitcnt lgkmcnt(9)\n" \
  "v_mfma_f32_16x16x32_bf16 v[184:187], v[208:211], v[76:79], v[184:187]\n" \
  "ds_read_b64_tr_b16 v[228:229], v96 offset:16896\n" \
  "ds_read_b64_tr_b16 v[196:197], v94 offset:16896\n" \
  "ds_read_b64_tr_b16 v[198:199], v94 offset:17408\n" \
  "s_waitcnt lgkmcnt(9)\n" \
  "v_mfma_f32_16x16x32_bf16 v[188:191], v[240:243], v[80:83], 0\n" \
  "v_mfma_f32_16x16x32_bf16 v[188:191], v[212:215], v[64:67], v[188:191]\n" \
  "ds_read_b64_tr_b16 v[200:201], v95 offset:16896\n" \
  "ds_read_b64_tr_b16 v[202:203], v95 offset:17408\n" \
  "s_waitcnt lgkmcnt(9)\n" \
  "v_mfma_f32_16x16x32_bf16 v[188:191], v[216:219], v[72:75], v[188:191]\n" \
  "ds_read_b64_tr_b16 v[204:205], v94 offset:20992\n" \
  "ds_read_b64_tr_b16 v[206:207], v94 offset:21504\n" \
  "s_waitcnt lgkmcnt(9)\n" \
  "v_mfma_f32_16x16x32_bf16 v[188:191], v[220:223], v[68:71], v[188:191]\n" \
  "ds_read_b64_tr_b16 v[208:209], v95 offset:20992\n" \
  "ds_read_b64_tr_b16 v[210:211], v95 offset:21504\n" \
  "s_waitcnt lgkmcnt(9)\n" \
  "v_mfma_f32_16x16x32_bf16 v[188:191], v[224:227], v[76:79], v[188:191]\n" \
  "s_waitcnt lgkmcnt(6)\n" \
  "v_mfma_f32_16x16x32_bf16 v[192:195], v[228:231], v[80:83], 0\n" \
  "v_mfma_f32_16x16x32_bf16 v[192:195], v[196:199], v[64:67], v[192:195]\n" \
  "s_waitcnt lgkmcnt(4)\n" \
  "v_mfma_f32_16x16x32_bf16 v[192:195], v[200:203], v[72:75], v[192:195]\n" \
  "s_waitcnt lgkmcnt(2)\n" \
  "v_mfma_f32_16x16x32_bf16 v[192:195], v[204:207], v[68:71], v[192:195]\n" \
  "s_waitcnt lgkmcnt(0)\n" \
  "v_mfma_f32_16x16x32_bf16 v[192:195], v[208:211], v[76:79], v[192:195]\n" \
  "v_pk_mul_f32 v[216:217], v[160:161], v[98:99] op_sel:[0,0] op_sel_hi:[1,0]\n" \
  "v_pk_mul_f32 v[218:219], v[162:163], v[98:99] op_sel:[0,0] op_sel_hi:[1,0]\n" \
  "v_pk_fma_f32 v[176:177], v[180:181], v[96:97], v[176:177] op_sel:[0,1,0] op_sel_hi:[1,1,1]\n" \
  "v_pk_fma_f32 v[178:179], v[182:183], v[96:97], v[178:179] op_sel:[0,1,0] op_sel_hi:[1,1,1]\n" \
  "v_pk_fma_f32 v[172:173], v[184:185], v[96:97], v[172:173] op_sel:[0,1,0] op_sel_hi:[1,1,1]\n" \
  "v_pk_fma_f32 v[174:175], v[186:187], v[96:97], v[174:175] op_sel:[0,1,0] op_sel_hi:[1,1,1]\n" \
  "v_pk_fma_f32 v[168:169], v[188:189], v[96:97], v[168:169] op_sel:[0,1,0] op_sel_hi:[1,1,1]\n" \
  "v_pk_fma_f32 v[170:171], v[190:191], v[96:97], v[170:171] op_sel:[0,1,0] op_sel_hi:[1,1,1]\n" \
  "v_pk_fma_f32 v[200:201], v[172:173], v[100:101], v[216:217] op_sel:[0,1,0] op_sel_hi:[1,1,1]\n" \
  "v_pk_fma_f32 v[202:203], v[174:175], v[100:101], v[218:219] op_sel:[0,1,0] op_sel_hi:[1,1,1]\n" \
  "v_pk_fma_f32 v[204:205], v[172:173], v[104:105], v[216:217] op_sel:[0,1,0] op_sel_hi:[1,1,1]\n" \
  "v_pk_fma_f32 v[206:207], v[174:175], v[104:105], v[218:219] op_sel:[0,1,0] op_sel_hi:[1,1,1]\n" \
  "v_pk_add_f32 v[196:197], v[160:161], v[172:173]\n" \
  "v_pk_add_f32 v[198:199], v[162:163], v[174:175]\n" \
  "v_pk_fma_f32 v[164:165], v[192:193], v[96:97], v[164:165] op_sel:[0,1,0] op_sel_hi:[1,1,1]\n" \
  "v_pk_fma_f32 v[166:167], v[194:195], v[96:97], v[166:167] op_sel:[0,1,0] op_sel_hi:[1,1,1]\n" \
  "v_pk_fma_f32 v[212:213], v[176:177], v[104:105], v[200:201] op_sel:[0,0,0] op_sel_hi:[1,0,1]\n" \
  "v_pk_fma_f32 v[214:215], v[178:179], v[104:105], v[202:203] op_sel:[0,0,0] op_sel_hi:[1,0,1]\n" \
  "v_pk_fma_f32 v[200:201], v[176:177], v[102:103], v[200:201] op_sel:[0,0,0] op_sel_hi:[1,0,1]\n" \
  "v_pk_fma_f32 v[202:203], v[178:179], v[102:103], v[202:203] op_sel:[0,0,0] op_sel_hi:[1,0,1]\n" \
  "v_pk_fma_f32 v[204:205], v[176:177], v[102:103], v[204:205] op_sel:[0,1,0] op_sel_hi:[1,1,1]\n" \
  "v_pk_fma_f32 v[206:207], v[178:179], v[102:103], v[206:207] op_sel:[0,1,0] op_sel_hi:[1,1,1]\n" \
  "v_pk_mul_f32 v[208:209], v[196:197], v[98:99] op_sel:[0,0] op_sel_hi:[1,0]\n" \
  "v_pk_mul_f32 v[210:211], v[198:199], v[98:99] op_sel:[0,0] op_sel_hi:[1,0]\n" \
  "v_pk_add_f32 v[220:221], v[164:165], v[176:177]\n" \
  "v_pk_add_f32 v[222:223], v[166:167], v[178:179]\n" \
  "v_pk_add_f32 v[196:197], v[196:197], v[168:169]\n" \
  "v_pk_add_f32 v[198:199], v[198:199], v[170:171]\n" \
  "v_pk_fma_f32 v[200:201], v[168:169], v[100:101], v[200:201] op_sel:[0,0,0] op_sel_hi:[1,0,1]\n" \
  "v_pk_fma_f32 v[202:203], v[170:171], v[100:101], v[202:203] op_sel:[0,0,0] op_sel_hi:[1,0,1]\n" \
  "v_pk_fma_f32 v[204:205], v[168:169], v[102:103], v[204:205] op_sel:[0,0,0] op_sel_hi:[1,0,1]\n" \
  "v_pk_fma_f32 v[206:207], v[170:171], v[102:103], v[206:207] op_sel:[0,0,0] op_sel_hi:[1,0,1]\n" \
  "v_pk_fma_f32 v[208:209], v[168:169], v[104:105], v[208:209] op_sel:[0,1,0] op_sel_hi:[1,1,1]\n" \
  "v_pk_fma_f32 v[210:211], v[170:171], v[104:105], v[210:211] op_sel:[0,1,0] op_sel_hi:[1,1,1]\n" \
  "v_pk_fma_f32 v[212:213], v[168:169], v[102:103], v[212:213] op_sel:[0,1,0] op_sel_hi:[1,1,1]\n" \
  "v_pk_fma_f32 v[214:215], v[170:171], v[102:103], v[214:215] op_sel:[0,1,0] op_sel_hi:[1,1,1]\n" \
  "v_pk_fma_f32 v[200:201], v[164:165], v[98:99], v[200:201] op_sel:[0,1,0] op_sel_hi:[1,1,1]\n" \
  "v_pk_fma_f32 v[202:203], v[166:167], v[98:99], v[202:203] op_sel:[0,1,0] op_sel_hi:[1,1,1]\n" \
  "v_pk_fma_f32 v[204:205], v[164:165], v[100:101], v[204:205] op_sel:[0,0,0] op_sel_hi:[1,0,1]\n" \
  "v_pk_fma_f32 v[206:207], v[166:167], v[100:101], v[206:207] op_sel:[0,0,0] op_sel_hi:[1,0,1]\n" \
  "v_pk_fma_f32 v[208:209], v[220:221], v[100:101], v[208:209] op_sel:[0,1,0] op_sel_hi:[1,1,1]\n" \
  "v_pk_fma_f32 v[210:211], v[222:223], v[100:101], v[210:211] op_sel:[0,1,0] op_sel_hi:[1,1,1]\n" \
  "v_pk_fma_f32 v[212:213], v[164:165], v[102:103], v[212:213] op_sel:[0,0,0] op_sel_hi:[1,0,1]\n" \
  "v_pk_fma_f32 v[214:215], v[166:167], v[102:103], v[214:215] op_sel:[0,0,0] op_sel_hi:[1,0,1]\n" \
  "v_pk_add_f32 v[196:197], v[196:197], v[220:221]\n" \
  "v_pk_add_f32 v[198:199], v[198:199], v[222:223]\n" \
  "v_mov_b32 v4, 0x3f803f80\n" \
  "v_fmac_f32_dpp v200, v200, v106 quad_perm:[1,0,3,2] row_mask:0xf bank_mask:0xf\n" \
  "v_fmac_f32_dpp v201, v201, v106 quad_perm:[1,0,3,2] row_mask:0xf bank_mask:0xf\n" \
  "v_fmac_f32_dpp v202, v202, v106 quad_perm:[1,0,3,2] row_mask:0xf bank_mask:0xf\n" \
  "v_fmac_f32_dpp v203, v203, v106 quad_perm:[1,0,3,2] row_mask:0xf bank_mask:0xf\n" \
  "v_pk_fma_f32 v[200:201], v[200:201], v[128:129], v[48:49]\n" \
  "v_pk_fma_f32 v[202:203], v[202:203], v[130:131], v[50:51]\n" \
  "v_cvt_pk_bf16_f32 v200, v200, v201\n" \
  "v_cvt_pk_bf16_f32 v201, v202, v203\n" \
  "ds_write_b64 v38, v[200:201]\n" \
  "v_dot2c_f32_bf16 v2, v200, v4\n" \
  "v_dot2c_f32_bf16 v3, v200, v200\n" \
  "v_dot2c_f32_bf16 v2, v201, v4\n" \
  "v_dot2c_f32_bf16 v3, v201, v201\n" \
  "v_fmac_f32_dpp v204, v204, v106 quad_perm:[1,0,3,2] row_mask:0xf bank_mask:0xf\n" \
  "v_fmac_f32_dpp v205, v205, v106 quad_perm:[1,0,3,2] row_mask:0xf bank_mask:0xf\n" \
  "v_fmac_f32_dpp v206, v206, v106 quad_perm:[1,0,3,2] row_mask:0xf bank_mask:0xf\n" \
  "v_fmac_f32_dpp v207, v207, v106 quad_perm:[1,0,3,2] row_mask:0xf bank_mask:0xf\n" \
  "v_pk_fma_f32 v[204:205], v[204:205], v[132:133], v[52:53]\n" \
  "v_pk_fma_f32 v[206:207], v[206:207], v[134:135], v[54:55]\n" \
  "v_cvt_pk_bf16_f32 v204, v204, v205\n" \
  "v_cvt_pk_bf16_f32 v205, v206, v207\n" \
  "ds_write_b64 v39, v[204:205]\n" \
  "v_dot2c_f32_bf16 v2, v204, v4\n" \
  "v_dot2c_f32_bf16 v3, v204, v204\n" \
  "v_dot2c_f32_bf16 v2, v205, v4\n" \
  "v_dot2c_f32_bf16 v3, v205, v205\n" \
  "v_fmac_f32_dpp v208, v208, v106 quad_perm:[1,0,3,2] row_mask:0xf bank_mask:0xf\n" \
  "v_fmac_f32_dpp v209, v209, v106 quad_perm:[1,0,3,2] row_mask:0xf bank_mask:0xf\n" \
  "v_fmac_f32_dpp v210, v210, v106 quad_perm:[1,0,3,2] row_mask:0xf bank_mask:0xf\n" \
  "v_fmac_f32_dpp v211, v211, v106 quad_perm:[1,0,3,2] row_mask:0xf bank_mask:0xf\n" \
  "v_pk_fma_f32 v[208:209], v[208:209], v[136:137], v[56:57]\n" \
  "v_pk_fma_f32 v[210:211], v[210:211], v[138:139], v[58:59]\n" \
  "v_cvt_pk_bf16_f32 v208, v208, v209\n" \
  "v_cvt_pk_bf16_f32 v209, v210, v211\n" \
  "ds_write_b64 v40, v[208:209]\n" \
  "v_dot2c_f32_bf16 v2, v208, v4\n" \
  "v_dot2c_f32_bf16 v3, v208, v208\n" \
  "v_dot2c_f32_bf16 v2, v209, v4\n" \
  "v_dot2c_f32_bf16 v3, v209, v209\n" \
  "v_fmac_f32_dpp v212, v212, v106 quad_perm:[1,0,3,2] row_mask:0xf bank_mask:0xf\n" \
  "v_fmac_f32_dpp v213, v213, v106 quad_perm:[1,0,3,2] row_mask:0xf bank_mask:0xf\n" \
  "v_fmac_f32_dpp v214, v214, v106 quad_perm:[1,0,3,2] row_mask:0xf bank_mask:0xf\n" \
  "v_fmac_f32_dpp v215, v215, v106 quad_perm:[1,0,3,2] row_mask:0xf bank_mask:0xf\n" \
  "v_pk_fma_f32 v[212:213], v[212:213], v[140:141], v[60:61]\n" \
  "v_pk_fma_f32 v[214:215], v[214:215], v[142:143], v[62:63]\n" \
  "v_cvt_pk_bf16_f32 v212, v212, v213\n" \
  "v_cvt_pk_bf16_f32 v213, v214, v215\n" \
  "ds_write_b64 v41, v[212:213]\n" \
  "v_dot2c_f32_bf16 v2, v212, v4\n" \
  "v_dot2c_f32_bf16 v3, v212, v212\n" \
  "v_dot2c_f32_bf16 v2, v213, v4\n" \
  "v_dot2c_f32_bf16 v3, v213, v213\n" \
  "v_pk_fma_f32 v[196:197], v[196:197], v[124:125], v[44:45]\n" \
  "v_pk_fma_f32 v[198:199], v[198:199], v[126:127], v[46:47]\n" \
  "v_cvt_pk_bf16_f32 v196, v196, v197\n" \
  "v_cvt_pk_bf16_f32 v197, v198, v199\n" \
  "s_mov_b64 exec, s[44:45]\n" \
  "ds_write_b64 v37, v[196:197]\n" \
  "v_dot2c_f32_bf16 v2, v196, v4\n" \
  "v_dot2c_f32_bf16 v3, v196, v196\n" \
  "v_dot2c_f32_bf16 v2, v197, v4\n" \
  "v_dot2c_f32_bf16 v3, v197, v197\n" \
  "s_mov_b64 exec, -1\n" \
  "s_nop 1\n" \
  "v_permlane32_swap_b32 v2, v3\n" \
  "v_add_f32 v2, v2, v3\n" \
  "v_mov_b32 v3, v2\n" \
  "s_nop 1\n" \
  "v_permlane16_swap_b32 v2, v3\n" \
  "v_add_f32 v2, v2, v3\n" \
  "s_nop 1\n" \
  "v_add_f32_dpp v2, v2, v2 quad_perm:[1,0,3,2] row_mask:0xf bank_mask:0xf\n" \
  "ds_write_b32 v107, v2\n" \
  "s_setprio 0\n" \
  ".Lfin_%=:\n" \
  "s_mov_b32 s18, 0x3ae38e39\n" \
  "s_mov_b32 s20, 0x3727c5ac\n" \
  "s_add_u32 s16, s1, 13631488\n" \
  "s_add_u32 s17, s1, 10485760\n" \
  "s_cmp_eq_u32 s3, 3\n" \
  "s_waitcnt lgkmcnt(0)\n" \
  "s_barrier\n" \
  "ds_read_b128 v[28:31], v24\n" \
  "ds_read_b128 v[32:35], v24 offset:16\n" \
  "ds_read_b128 v[36:39], v25\n" \
  "s_waitcnt lgkmcnt(1)\n" \
  "v_pk_add_f32 v[28:29], v[28:29], v[30:31]\n" \
  "v_pk_add_f32 v[32:33], v[32:33], v[34:35]\n" \
  "v_pk_add_f32 v[28:29], v[28:29], v[32:33]\n" \
  "v_pk_mul_f32 v[28:29], v[28:29], s[18:19] op_sel_hi:[1,0]\n" \
  "v_fma_f32 v29, -v28, v28, v29\n" \
  "v_add_f32 v29, s20, v29\n" \
  "v_max_f32 v29, s20, v29\n" \
  "v_rsq_f32 v29, v29\n" \
  "s_waitcnt lgkmcnt(0)\n" \
  "buffer_store_dwordx4 v[36:39], v26, s[40:43], s17 offen sc1\n" \
  "v_lshlrev_b32 v40, 16, v36\n" \
  "v_and_b32 v41, 0xffff0000, v36\n" \
  "v_lshlrev_b32 v42, 16, v37\n" \
  "v_and_b32 v43, 0xffff0000, v37\n" \
  "v_lshlrev_b32 v44, 16, v38\n" \
  "v_and_b32 v45, 0xffff0000, v38\n" \
  "v_lshlrev_b32 v46, 16, v39\n" \
  "v_and_b32 v47, 0xffff0000, v39\n" \
  "v_mul_f32 v28, -v28, v29\n" \
  "v_pk_fma_f32 v[40:41], v[40:41], v[28:29], v[28:29] op_sel:[0,1,0] op_sel_hi:[1,1,0]\n" \
  "v_pk_fma_f32 v[42:43], v[42:43], v[28:29], v[28:29] op_sel:[0,1,0] op_sel_hi:[1,1,0]\n" \
  "v_pk_fma_f32 v[44:45], v[44:45], v[28:29], v[28:29] op_sel:[0,1,0] op_sel_hi:[1,1,0]\n" \
  "v_pk_fma_f32 v[46:47], v[46:47], v[28:29], v[28:29] op_sel:[0,1,0] op_sel_hi:[1,1,0]\n" \
  "v_pk_fma_f32 v[40:41], v[40:41], v[144:145], v[152:153]\n" \
  "v_pk_fma_f32 v[42:43], v[42:43], v[146:147], v[154:155]\n" \
  "v_pk_fma_f32 v[44:45], v[44:45], v[148:149], v[156:157]\n" \
  "v_pk_fma_f32 v[46:47], v[46:47], v[150:151], v[158:159]\n" \
  "v_cvt_pk_bf16_f32 v48, v40, v41\n" \
  "v_cvt_pk_bf16_f32 v49, v42, v43\n" \
  "v_cvt_pk_bf16_f32 v50, v44, v45\n" \
  "v_cvt_pk_bf16_f32 v51, v46, v47\n" \
  "buffer_store_dwordx4 v[48:51], v26, s[40:43], s16 offen sc1\n" \
  "s_cbranch_scc0 .Lfin2_%=\n" \
  "v_add_u32 v30, 77056, v10\n" \
  "ds_read_b128 v[32:35], v30\n" \
  "s_add_u32 s16, s16, 10240\n" \
  "s_add_u32 s17, s17, 10240\n" \
  "s_waitcnt lgkmcnt(0)\n" \
  "buffer_store_dwordx4 v[32:35], v26, s[40:43], s17 offen sc1\n" \
  "v_lshlrev_b32 v40, 16, v32\n" \
  "v_and_b32 v41, 0xffff0000, v32\n" \
  "v_lshlrev_b32 v42, 16, v33\n" \
  "v_and_b32 v43, 0xffff0000, v33\n" \
  "v_lshlrev_b32 v44, 16, v34\n" \
  "v_and_b32 v45, 0xffff0000, v34\n" \
  "v_lshlrev_b32 v46, 16, v35\n" \
  "v_and_b32 v47, 0xffff0000, v35\n" \
  "v_pk_fma_f32 v[40:41], v[40:41], v[28:29], v[28:29] op_sel:[0,1,0] op_sel_hi:[1,1,0]\n" \
  "v_pk_fma_f32 v[42:43], v[42:43], v[28:29], v[28:29] op_sel:[0,1,0] op_sel_hi:[1,1,0]\n" \
  "v_pk_fma_f32 v[44:45], v[44:45], v[28:29], v[28:29] op_sel:[0,1,0] op_sel_hi:[1,1,0]\n" \
  "v_pk_fma_f32 v[46:47], v[46:47], v[28:29], v[28:29] op_sel:[0,1,0] op_sel_hi:[1,1,0]\n" \
  "v_pk_fma_f32 v[40:41], v[40:41], v[124:125], v[132:133]\n" \
  "v_pk_fma_f32 v[42:43], v[42:43], v[126:127], v[134:135]\n" \
  "v_pk_fma_f32 v[44:45], v[44:45], v[128:129], v[136:137]\n" \
  "v_pk_fma_f32 v[46:47], v[46:47], v[130:131], v[138:139]\n" \
  "v_cvt_pk_bf16_f32 v52, v40, v41\n" \
  "v_cvt_pk_bf16_f32 v53, v42, v43\n" \
  "v_cvt_pk_bf16_f32 v54, v44, v45\n" \
  "v_cvt_pk_bf16_f32 v55, v46, v47\n" \
  "buffer_store_dwordx4 v[52:55], v26, s[40:43], s16 offen sc1\n" \
  ".Lfin2_%=:\n" \
  "s_lshr_b32 s16, s5, 3\n" \
  "s_lshl_b32 s16, s16, 3\n" \
  "s_add_u32 s16, s16, s4\n" \
  "s_lshl_b32 s16, s16, 9\n" \
  "s_bfe_u32 s17, s5, 0x10002\n" \
  "s_lshl_b32 s17, s17, 8\n" \
  "s_add_u32 s16, s16, s17\n" \
  "s_add_u32 s16, s16, 262144\n" \
  "v_mov_b32 v2, 1\n" \
  "v_lshlrev_b32 v3, 11, v1\n" \
  "v_lshlrev_b32 v4, 6, v1\n" \
  "s_and_b32 s17, s2, 15\n" \
  "s_lshl_b32 s17, s17, 7\n" \
  "s_add_u32 s17, s17, 147456\n" \
  "s_cmp_lg_u32 s3, 0\n" \
  "s_waitcnt vmcnt(0)\n" \
  "s_barrier\n" \
  "s_cbranch_scc1 .Lmlp_%=\n" \
  "s_mov_b64 exec, 15\n" \
  "buffer_atomic_add v2, v4, s[40:43], s16 offen\n" \
  "s_mov_b64 exec, -1\n" \
  "s_mov_b64 exec, 255\n" \
  "buffer_atomic_add v2, v3, s[40:43], s17 offen\n" \
  "s_mov_b64 exec, -1\n" \
  ".Lmlp_%=:\n" \
  ""
#define MLP_ASM \
  "s_mov_b32 s36, s48\n" \
  "s_and_b32 s37, s49, 0xffff\n" \
  "s_mov_b32 s38, 5308416\n" \
  "s_mov_b32 s39, 0x20000\n" \
  "s_mov_b32 s40, s50\n" \
  "s_and_b32 s41, s51, 0xffff\n" \
  "s_mov_b32 s42, 0x8000000\n" \
  "s_mov_b32 s43, 0x20000\n" \
  "s_mov_b32 s44, s52\n" \
  "s_and_b32 s45, s53, 0xffff\n" \
  "s_mov_b32 s46, 2359296\n" \
  "s_mov_b32 s47, 0x20000\n" \
  "s_add_u32 m0, s57, 114688\n" \
  "s_add_u32 s34, s54, 1179648\n" \
  "buffer_load_dwordx4 v8, s[36:39], s34 offen lds\n" \
  "s_add_u32 m0, s57, 122880\n" \
  "s_add_u32 s34, s54, 1474560\n" \
  "buffer_load_dwordx4 v8, s[36:39], s34 offen lds\n" \
  "s_add_u32 m0, s57, 131072\n" \
  "s_add_u32 s34, s54, 1769472\n" \
  "buffer_load_dwordx4 v8, s[36:39], s34 offen lds\n" \
  "s_add_u32 m0, s57, 139264\n" \
  "s_add_u32 s34, s54, 2064384\n" \
  "buffer_load_dwordx4 v8, s[36:39], s34 offen lds\n" \
  "s_mov_b64 exec, 1\n" \
  "v_mov_b32 v4, 0\n" \
  "buffer_atomic_add v4, off, s[40:43], s56 sc0\n" \
  "s_mov_b64 exec, -1\n" \
  "s_waitcnt vmcnt(0)\n" \
  "s_mov_b32 s35, 0x40000\n" \
  ".Lpoll1_%=:\n" \
  "v_readfirstlane_b32 s34, v4\n" \
  "s_cmp_eq_u32 s34, 4\n" \
  "s_cbranch_scc1 .Lok1_%=\n" \
  "s_mov_b64 exec, 1\n" \
  "v_mov_b32 v4, 0\n" \
  "buffer_atomic_add v4, off, s[40:43], s56 sc0\n" \
  "s_mov_b64 exec, -1\n" \
  "s_sub_u32 s35, s35, 1\n" \
  "s_waitcnt vmcnt(0)\n" \
  "s_cbranch_scc0 .Lpoll1_%=\n" \
  ".Lok1_%=:\n" \
  "buffer_load_dwordx4 v[24:27], v10, s[40:43], s60 offen sc1\n" \
  "s_add_u32 s34, s60, 18432\n" \
  "buffer_load_dwordx4 v[28:31], v10, s[40:43], s34 offen sc1\n" \
  "buffer_load_dwordx4 v[32:35], v10, s[40:43], s60 offen offset:1024 sc1\n" \
  "s_add_u32 s34, s60, 19456\n" \
  "buffer_load_dwordx4 v[36:39], v10, s[40:43], s34 offen sc1\n" \
  "s_barrier\n" \
  "v_add_u32 v12, 81920, v12\n" \
  "ds_read_b64_tr_b16 v[232:233], v12\n" \
  "ds_read_b64_tr_b16 v[234:235], v12 offset:512\n" \
  "ds_read_b64_tr_b16 v[236:237], v12 offset:8192\n" \
  "ds_read_b64_tr_b16 v[238:239], v12 offset:8704\n" \
  "ds_read_b64_tr_b16 v[240:241], v12 offset:4096\n" \
  "ds_read_b64_tr_b16 v[242:243], v12 offset:4608\n" \
  "ds_read_b64_tr_b16 v[244:245], v12 offset:12288\n" \
  "ds_read_b64_tr_b16 v[246:247], v12 offset:12800\n" \
  "s_waitcnt vmcnt(0)\n" \
  "s_waitcnt lgkmcnt(4)\n" \
  "v_mfma_f32_16x16x32_bf16 v[168:171], v[232:235], v[24:27], 0\n" \
  "v_add_u32 v13, 81920, v13\n" \
  "ds_read_b64_tr_b16 v[248:249], v13\n" \
  "buffer_load_dwordx4 v[40:43], v10, s[40:43], s60 offen offset:2048 sc1\n" \
  "v_mfma_f32_16x16x32_bf16 v[168:171], v[236:239], v[32:35], v[168:171]\n" \
  "ds_read_b64_tr_b16 v[250:251], v13 offset:512\n" \
  "v_mfma_f32_16x16x32_bf16 v[172:175], v[236:239], v[36:39], 0\n" \
  "ds_read_b64_tr_b16 v[252:253], v13 offset:8192\n" \
  "v_mfma_f32_16x16x32_bf16 v[172:175], v[232:235], v[28:31], v[172:175]\n" \
  "ds_read_b64_tr_b16 v[254:255], v13 offset:8704\n" \
  "s_waitcnt lgkmcnt(4)\n" \
  "v_mfma_f32_16x16x32_bf16 v[180:183], v[240:243], v[28:31], 0\n" \
  "ds_read_b64_tr_b16 v[232:233], v13 offset:4096\n" \
  "v_mfma_f32_16x16x32_bf16 v[180:183], v[244:247], v[36:39], v[180:183]\n" \
  "ds_read_b64_tr_b16 v[234:235], v13 offset:4608\n" \
  "s_add_u32 s34, s60, 20480\n" \
  "buffer_load_dwordx4 v[44:47], v10, s[40:43], s34 offen sc1\n" \
  "v_mfma_f32_16x16x32_bf16 v[176:179], v[244:247], v[32:35], 0\n" \
  "ds_read_b64_tr_b16 v[236:237], v13 offset:12288\n" \
  "v_mfma_f32_16x16x32_bf16 v[176:179], v[240:243], v[24:27], v[176:179]\n" \
  "ds_read_b64_tr_b16 v[238:239], v13 offset:12800\n" \
  "s_waitcnt lgkmcnt(4)\n" \
  "v_mfma_f32_16x16x32_bf16 v[184:187], v[248:251], v[24:27], 0\n" \
  "v_add_u32 v14, 81920, v14\n" \
  "ds_read_b64_tr_b16 v[240:241], v14\n" \
  "v_mfma_f32_16x16x32_bf16 v[184:187], v[252:255], v[32:35], v[184:187]\n" \
  "ds_read_b64_tr_b16 v[242:243], v14 offset:512\n" \
  "v_mfma_f32_16x16x32_bf16 v[188:191], v[252:255], v[36:39], 0\n" \
  "ds_read_b64_tr_b16 v[244:245], v14 offset:8192\n" \
  "buffer_load_dwordx4 v[48:51], v10, s[40:43], s60 offen offset:3072 sc1\n" \
  "v_mfma_f32_16x16x32_bf16 v[188:191], v[248:251], v[28:31], v[188:191]\n" \
  "ds_read_b64_tr_b16 v[246:247], v14 offset:8704\n" \
  "s_waitcnt lgkmcnt(4)\n" \
  "v_mfma_f32_16x16x32_bf16 v[196:199], v[232:235], v[28:31], 0\n" \
  "ds_read_b64_tr_b16 v[248:249], v14 offset:4096\n" \
  "v_mfma_f32_16x16x32_bf16 v[196:199], v[236:239], v[36:39], v[196:199]\n" \
  "ds_read_b64_tr_b16 v[250:251], v14 offset:4608\n" \
  "v_mfma_f32_16x16x32_bf16 v[192:195], v[236:239], v[32:35], 0\n" \
  "ds_read_b64_tr_b16 v[252:253], v14 offset:12288\n" \
  "v_mfma_f32_16x16x32_bf16 v[192:195], v[232:235], v[24:27], v[192:195]\n" \
  "ds_read_b64_tr_b16 v[254:255], v14 offset:12800\n" \
  "s_add_u32 s34, s60, 21504\n" \
  "buffer_load_dwordx4 v[52:55], v10, s[40:43], s34 offen sc1\n" \
  "s_waitcnt lgkmcnt(4)\n" \
  "v_mfma_f32_16x16x32_bf16 v[200:203], v[240:243], v[24:27], 0\n" \
  "v_add_u32 v15, 81920, v15\n" \
  "ds_read_b64_tr_b16 v[232:233], v15\n" \
  "v_mfma_f32_16x16x32_bf16 v[200:203], v[244:247], v[32:35], v[200:203]\n" \
  "ds_read_b64_tr_b16 v[234:235], v15 offset:512\n" \
  "v_mfma_f32_16x16x32_bf16 v[204:207], v[244:247], v[36:39], 0\n" \
  "ds_read_b64_tr_b16 v[236:237], v15 offset:8192\n" \
  "v_mfma_f32_16x16x32_bf16 v[204:207], v[240:243], v[28:31], v[204:207]\n" \
  "ds_read_b64_tr_b16 v[238:239], v15 offset:8704\n" \
  "s_waitcnt lgkmcnt(4)\n" \
  "v_mfma_f32_16x16x32_bf16 v[212:215], v[248:251], v[28:31], 0\n" \
  "ds_read_b64_tr_b16 v[240:241], v15 offset:4096\n" \
  "s_add_u32 s34, s60, 4096\n" \
  "buffer_load_dwordx4 v[56:59], v10, s[40:43], s34 offen sc1\n" \
  "v_mfma_f32_16x16x32_bf16 v[212:215], v[252:255], v[36:39], v[212:215]\n" \
  "ds_read_b64_tr_b16 v[242:243], v15 offset:4608\n" \
  "v_mfma_f32_16x16x32_bf16 v[208:211], v[252:255], v[32:35], 0\n" \
  "ds_read_b64_tr_b16 v[244:245], v15 offset:12288\n" \
  "v_mfma_f32_16x16x32_bf16 v[208:211], v[248:251], v[24:27], v[208:211]\n" \
  "ds_read_b64_tr_b16 v[246:247], v15 offset:12800\n" \
  "s_waitcnt lgkmcnt(4)\n" \
  "v_mfma_f32_16x16x32_bf16 v[216:219], v[232:235], v[24:27], 0\n" \
  "ds_read_b64_tr_b16 v[248:249], v12 offset:16384\n" \
  "v_mfma_f32_16x16x32_bf16 v[216:219], v[236:239], v[32:35], v[216:219]\n" \
  "ds_read_b64_tr_b16 v[250:251], v12 offset:16896\n" \
  "s_add_u32 s34, s60, 22528\n" \
  "buffer_load_dwordx4 v[60:63], v10, s[40:43], s34 offen sc1\n" \
  "v_mfma_f32_16x16x32_bf16 v[220:223], v[236:239], v[36:39], 0\n" \
  "ds_read_b64_tr_b16 v[252:253], v12 offset:24576\n" \
  "v_mfma_f32_16x16x32_bf16 v[220:223], v[232:235], v[28:31], v[220:223]\n" \
  "ds_read_b64_tr_b16 v[254:255], v12 offset:25088\n" \
  "s_waitcnt lgkmcnt(4)\n" \
  "v_mfma_f32_16x16x32_bf16 v[228:231], v[240:243], v[28:31], 0\n" \
  "ds_read_b64_tr_b16 v[232:233], v12 offset:20480\n" \
  "v_mfma_f32_16x16x32_bf16 v[228:231], v[244:247], v[36:39], v[228:231]\n" \
  "ds_read_b64_tr_b16 v[234:235], v12 offset:20992\n" \
  "v_mfma_f32_16x16x32_bf16 v[224:227], v[244:247], v[32:35], 0\n" \
  "ds_read_b64_tr_b16 v[236:237], v12 offset:28672\n" \
  "s_add_u32 s34, s60, 5120\n" \
  "buffer_load_dwordx4 v[64:67], v10, s[40:43], s34 offen sc1\n" \
  "v_mfma_f32_16x16x32_bf16 v[224:227], v[240:243], v[24:27], v[224:227]\n" \
  "ds_read_b64_tr_b16 v[238:239], v12 offset:29184\n" \
  "s_waitcnt vmcnt(3)\n" \
  "s_waitcnt lgkmcnt(4)\n" \
  "v_mfma_f32_16x16x32_bf16 v[168:171], v[248:251], v[40:43], v[168:171]\n" \
  "ds_read_b64_tr_b16 v[240:241], v13 offset:16384\n" \
  "v_mfma_f32_16x16x32_bf16 v[168:171], v[252:255], v[48:51], v[168:171]\n" \
  "ds_read_b64_tr_b16 v[242:243], v13 offset:16896\n" \
  "v_mfma_f32_16x16x32_bf16 v[172:175], v[252:255], v[52:55], v[172:175]\n" \
  "ds_read_b64_tr_b16 v[244:245], v13 offset:24576\n" \
  "v_mfma_f32_16x16x32_bf16 v[172:175], v[248:251], v[44:47], v[172:175]\n" \
  "ds_read_b64_tr_b16 v[246:247], v13 offset:25088\n" \
  "s_add_u32 s34, s60, 23552\n" \
  "buffer_load_dwordx4 v[68:71], v10, s[40:43], s34 offen sc1\n" \
  "s_waitcnt lgkmcnt(4)\n" \
  "v_mfma_f32_16x16x32_bf16 v[180:183], v[232:235], v[44:47], v[180:183]\n" \
  "ds_read_b64_tr_b16 v[248:249], v13 offset:20480\n" \
  "v_mfma_f32_16x16x32_bf16 v[180:183], v[236:239], v[52:55], v[180:183]\n" \
  "ds_read_b64_tr_b16 v[250:251], v13 offset:20992\n" \
  "v_mfma_f32_16x16x32_bf16 v[176:179], v[236:239], v[48:51], v[176:179]\n" \
  "ds_read_b64_tr_b16 v[252:253], v13 offset:28672\n" \
  "v_mfma_f32_16x16x32_bf16 v[176:179], v[232:235], v[40:43], v[176:179]\n" \
  "ds_read_b64_tr_b16 v[254:255], v13 offset:29184\n" \
  "s_waitcnt lgkmcnt(4)\n" \
  "v_mfma_f32_16x16x32_bf16 v[184:187], v[240:243], v[40:43], v[184:187]\n" \
  "ds_read_b64_tr_b16 v[232:233], v14 offset:16384\n" \
  "s_add_u32 s34, s60, 6144\n" \
  "buffer_load_dwordx4 v[72:75], v10, s[40:43], s34 offen sc1\n" \
  "v_mfma_f32_16x16x32_bf16 v[184:187], v[244:247], v[48:51], v[184:187]\n" \
  "ds_read_b64_tr_b16 v[234:235], v14 offset:16896\n" \
  "v_mfma_f32_16x16x32_bf16 v[188:191], v[244:247], v[52:55], v[188:191]\n" \
  "ds_read_b64_tr_b16 v[236:237], v14 offset:24576\n" \
  "v_mfma_f32_16x16x32_bf16 v[188:191], v[240:243], v[44:47], v[188:191]\n" \
  "ds_read_b64_tr_b16 v[238:239], v14 offset:25088\n" \
  "s_waitcnt lgkmcnt(4)\n" \
  "v_mfma_f32_16x16x32_bf16 v[196:199], v[248:251], v[44:47], v[196:199]\n" \
  "ds_read_b64_tr_b16 v[240:241], v14 offset:20480\n" \
  "v_mfma_f32_16x16x32_bf16 v[196:199], v[252:255], v[52:55], v[196:199]\n" \
  "ds_read_b64_tr_b16 v[242:243], v14 offset:20992\n" \
  "s_add_u32 s34, s60, 24576\n" \
  "buffer_load_dwordx4 v[76:79], v10, s[40:43], s34 offen sc1\n" \
  "v_mfma_f32_16x16x32_bf16 v[192:195], v[252:255], v[48:51], v[192:195]\n" \
  "ds_read_b64_tr_b16 v[244:245], v14 offset:28672\n" \
  "v_mfma_f32_16x16x32_bf16 v[192:195], v[248:251], v[40:43], v[192:195]\n" \
  "ds_read_b64_tr_b16 v[246:247], v14 offset:29184\n" \
  "s_waitcnt lgkmcnt(4)\n" \
  "v_mfma_f32_16x16x32_bf16 v[200:203], v[232:235], v[40:43], v[200:203]\n" \
  "ds_read_b64_tr_b16 v[248:249], v15 offset:16384\n" \
  "v_mfma_f32_16x16x32_bf16 v[200:203], v[236:239], v[48:51], v[200:203]\n" \
  "ds_read_b64_tr_b16 v[250:251], v15 offset:16896\n" \
  "v_mfma_f32_16x16x32_bf16 v[204:207], v[236:239], v[52:55], v[204:207]\n" \
  "ds_read_b64_tr_b16 v[252:253], v15 offset:24576\n" \
  "s_add_u32 s34, s60, 7168\n" \
  "buffer_load_dwordx4 v[80:83], v10, s[40:43], s34 offen sc1\n" \
  "v_mfma_f32_16x16x32_bf16 v[204:207], v[232:235], v[44:47], v[204:207]\n" \
  "ds_read_b64_tr_b16 v[254:255], v15 offset:25088\n" \
  "s_waitcnt lgkmcnt(4)\n" \
  "v_mfma_f32_16x16x32_bf16 v[212:215], v[240:243], v[44:47], v[212:215]\n" \
  "ds_read_b64_tr_b16 v[232:233], v15 offset:20480\n" \
  "v_mfma_f32_16x16x32_bf16 v[212:215], v[244:247], v[52:55], v[212:215]\n" \
  "ds_read_b64_tr_b16 v[234:235], v15 offset:20992\n" \
  "v_mfma_f32_16x16x32_bf16 v[208:211], v[244:247], v[48:51], v[208:211]\n" \
  "ds_read_b64_tr_b16 v[236:237], v15 offset:28672\n" \
  "v_mfma_f32_16x16x32_bf16 v[208:211], v[240:243], v[40:43], v[208:211]\n" \
  "ds_read_b64_tr_b16 v[238:239], v15 offset:29184\n" \
  "s_add_u32 s34, s60, 25600\n" \
  "buffer_load_dwordx4 v[84:87], v10, s[40:43], s34 offen sc1\n" \
  "s_waitcnt lgkmcnt(4)\n" \
  "v_mfma_f32_16x16x32_bf16 v[216:219], v[248:251], v[40:43], v[216:219]\n" \
  "s_barrier\n" \
  "ds_read_b64_tr_b16 v[240:241], v12 offset:32768\n" \
  "v_mfma_f32_16x16x32_bf16 v[216:219], v[252:255], v[48:51], v[216:219]\n" \
  "ds_read_b64_tr_b16 v[242:243], v12 offset:33280\n" \
  "v_mfma_f32_16x16x32_bf16 v[220:223], v[252:255], v[52:55], v[220:223]\n" \
  "ds_read_b64_tr_b16 v[244:245], v12 offset:40960\n" \
  "v_mfma_f32_16x16x32_bf16 v[220:223], v[248:251], v[44:47], v[220:223]\n" \
  "ds_read_b64_tr_b16 v[246:247], v12 offset:41472\n" \
  "s_waitcnt lgkmcnt(4)\n" \
  "v_mfma_f32_16x16x32_bf16 v[228:231], v[232:235], v[44:47], v[228:231]\n" \
  "ds_read_b64_tr_b16 v[248:249], v12 offset:36864\n" \
  "s_add_u32 m0, s57, 147456\n" \
  "s_add_u32 s34, s54, 2359296\n" \
  "buffer_load_dwordx4 v8, s[36:39], s34 offen lds\n" \
  "v_mfma_f32_16x16x32_bf16 v[228:231], v[236:239], v[52:55], v[228:231]\n" \
  "ds_read_b64_tr_b16 v[250:251], v12 offset:37376\n" \
  "v_mfma_f32_16x16x32_bf16 v[224:227], v[236:239], v[48:51], v[224:227]\n" \
  "ds_read_b64_tr_b16 v[252:253], v12 offset:45056\n" \
  "v_mfma_f32_16x16x32_bf16 v[224:227], v[232:235], v[40:43], v[224:227]\n" \
  "ds_read_b64_tr_b16 v[254:255], v12 offset:45568\n" \
  "s_waitcnt vmcnt(5)\n" \
  "s_waitcnt lgkmcnt(4)\n" \
  "v_mfma_f32_16x16x32_bf16 v[168:171], v[240:243], v[56:59], v[168:171]\n" \
  "ds_read_b64_tr_b16 v[232:233], v13 offset:32768\n" \
  "v_mfma_f32_16x16x32_bf16 v[168:171], v[244:247], v[64:67], v[168:171]\n" \
  "ds_read_b64_tr_b16 v[234:235], v13 offset:33280\n" \
  "s_add_u32 m0, s57, 155648\n" \
  "s_add_u32 s34, s54, 2654208\n" \
  "buffer_load_dwordx4 v8, s[36:39], s34 offen lds\n" \
  "v_mfma_f32_16x16x32_bf16 v[172:175], v[244:247], v[68:71], v[172:175]\n" \
  "ds_read_b64_tr_b16 v[236:237], v13 offset:40960\n" \
  "v_mfma_f32_16x16x32_bf16 v[172:175], v[240:243], v[60:63], v[172:175]\n" \
  "ds_read_b64_tr_b16 v[238:239], v13 offset:41472\n" \
  "s_waitcnt lgkmcnt(4)\n" \
  "v_mfma_f32_16x16x32_bf16 v[180:183], v[248:251], v[60:63], v[180:183]\n" \
  "ds_read_b64_tr_b16 v[240:241], v13 offset:36864\n" \
  "v_lshlrev_b32 v7, 3, v10\n" \
  "v_and_b32 v7, 0x780, v7\n" \
  "buffer_load_dword v4, v7, s[40:43], s58 offen sc1\n" \
  "v_mfma_f32_16x16x32_bf16 v[180:183], v[252:255], v[68:71], v[180:183]\n" \
  "ds_read_b64_tr_b16 v[242:243], v13 offset:37376\n" \
  "v_mfma_f32_16x16x32_bf16 v[176:179], v[252:255], v[64:67], v[176:179]\n" \
  "ds_read_b64_tr_b16 v[244:245], v13 offset:45056\n" \
  "v_mfma_f32_16x16x32_bf16 v[176:179], v[248:251], v[56:59], v[176:179]\n" \
  "ds_read_b64_tr_b16 v[246:247], v13 offset:45568\n" \
  "s_waitcnt lgkmcnt(4)\n" \
  "v_mfma_f32_16x16x32_bf16 v[184:187], v[232:235], v[56:59], v[184:187]\n" \
  "ds_read_b64_tr_b16 v[248:249], v14 offset:32768\n" \
  "s_add_u32 m0, s57, 0\n" \
  "s_add_u32 s34, s54, 2949120\n" \
  "buffer_load_dwordx4 v8, s[36:39], s34 offen lds\n" \
  "v_mfma_f32_16x16x32_bf16 v[184:187], v[236:239], v[64:67], v[184:187]\n" \
  "ds_read_b64_tr_b16 v[250:251], v14 offset:33280\n" \
  "v_mfma_f32_16x16x32_bf16 v[188:191], v[236:239], v[68:71], v[188:191]\n" \
  "ds_read_b64_tr_b16 v[252:253], v14 offset:40960\n" \
  "v_mfma_f32_16x16x32_bf16 v[188:191], v[232:235], v[60:63], v[188:191]\n" \
  "ds_read_b64_tr_b16 v[254:255], v14 offset:41472\n" \
  "s_waitcnt lgkmcnt(4)\n" \
  "v_mfma_f32_16x16x32_bf16 v[196:199], v[240:243], v[60:63], v[196:199]\n" \
  "ds_read_b64_tr_b16 v[232:233], v14 offset:36864\n" \
  "v_mfma_f32_16x16x32_bf16 v[196:199], v[244:247], v[68:71], v[196:199]\n" \
  "ds_read_b64_tr_b16 v[234:235], v14 offset:37376\n" \
  "s_add_u32 m0, s57, 8192\n" \
  "s_add_u32 s34, s54, 3244032\n" \
  "buffer_load_dwordx4 v8, s[36:39], s34 offen lds\n" \
  "v_mfma_f32_16x16x32_bf16 v[192:195], v[244:247], v[64:67], v[192:195]\n" \
  "ds_read_b64_tr_b16 v[236:237], v14 offset:45056\n" \
  "v_mfma_f32_16x16x32_bf16 v[192:195], v[240:243], v[56:59], v[192:195]\n" \
  "ds_read_b64_tr_b16 v[238:239], v14 offset:45568\n" \
  "s_waitcnt lgkmcnt(4)\n" \
  "v_mfma_f32_16x16x32_bf16 v[200:203], v[248:251], v[56:59], v[200:203]\n" \
  "ds_read_b64_tr_b16 v[240:241], v15 offset:32768\n" \
  "v_mfma_f32_16x16x32_bf16 v[200:203], v[252:255], v[64:67], v[200:203]\n" \
  "ds_read_b64_tr_b16 v[242:243], v15 offset:33280\n" \
  "v_mfma_f32_16x16x32_bf16 v[204:207], v[252:255], v[68:71], v[204:207]\n" \
  "ds_read_b64_tr_b16 v[244:245], v15 offset:40960\n" \
  "s_add_u32 s34, s60, 8192\n" \
  "buffer_load_dwordx4 v[88:91], v10, s[40:43], s34 offen sc1\n" \
  "v_mfma_f32_16x16x32_bf16 v[204:207], v[248:251], v[60:63], v[204:207]\n" \
  "ds_read_b64_tr_b16 v[246:247], v15 offset:41472\n" \
  "s_waitcnt lgkmcnt(4)\n" \
  "v_mfma_f32_16x16x32_bf16 v[212:215], v[232:235], v[60:63], v[212:215]\n" \
  "ds_read_b64_tr_b16 v[248:249], v15 offset:36864\n" \
  "v_mfma_f32_16x16x32_bf16 v[212:215], v[236:239], v[68:71], v[212:215]\n" \
  "ds_read_b64_tr_b16 v[250:251], v15 offset:37376\n" \
  "v_mfma_f32_16x16x32_bf16 v[208:211], v[236:239], v[64:67], v[208:211]\n" \
  "ds_read_b64_tr_b16 v[252:253], v15 offset:45056\n" \
  "v_mfma_f32_16x16x32_bf16 v[208:211], v[232:235], v[56:59], v[208:211]\n" \
  "ds_read_b64_tr_b16 v[254:255], v15 offset:45568\n" \
  "s_add_u32 s34, s60, 26624\n" \
  "buffer_load_dwordx4 v[92:95], v10, s[40:43], s34 offen sc1\n" \
  "s_waitcnt lgkmcnt(4)\n" \
  "v_mfma_f32_16x16x32_bf16 v[216:219], v[240:243], v[56:59], v[216:219]\n" \
  "ds_read_b64_tr_b16 v[232:233], v12 offset:49152\n" \
  "v_mfma_f32_16x16x32_bf16 v[216:219], v[244:247], v[64:67], v[216:219]\n" \
  "ds_read_b64_tr_b16 v[234:235], v12 offset:49664\n" \
  "v_mfma_f32_16x16x32_bf16 v[220:223], v[244:247], v[68:71], v[220:223]\n" \
  "ds_read_b64_tr_b16 v[236:237], v12 offset:57344\n" \
  "v_mfma_f32_16x16x32_bf16 v[220:223], v[240:243], v[60:63], v[220:223]\n" \
  "ds_read_b64_tr_b16 v[238:239], v12 offset:57856\n" \
  "s_waitcnt lgkmcnt(4)\n" \
  "v_mfma_f32_16x16x32_bf16 v[228:231], v[248:251], v[60:63], v[228:231]\n" \
  "ds_read_b64_tr_b16 v[240:241], v12 offset:53248\n" \
  "s_add_u32 s34, s60, 9216\n" \
  "buffer_load_dwordx4 v[96:99], v10, s[40:43], s34 offen sc1\n" \
  "v_mfma_f32_16x16x32_bf16 v[228:231], v[252:255], v[68:71], v[228:231]\n" \
  "ds_read_b64_tr_b16 v[242:243], v12 offset:53760\n" \
  "v_mfma_f32_16x16x32_bf16 v[224:227], v[252:255], v[64:67], v[224:227]\n" \
  "ds_read_b64_tr_b16 v[244:245], v12 offset:61440\n" \
  "v_mfma_f32_16x16x32_bf16 v[224:227], v[248:251], v[56:59], v[224:227]\n" \
  "ds_read_b64_tr_b16 v[246:247], v12 offset:61952\n" \
  "s_waitcnt vmcnt(8)\n" \
  "s_waitcnt lgkmcnt(4)\n" \
  "v_mfma_f32_16x16x32_bf16 v[168:171], v[232:235], v[72:75], v[168:171]\n" \
  "ds_read_b64_tr_b16 v[248:249], v13 offset:49152\n" \
  "v_mfma_f32_16x16x32_bf16 v[168:171], v[236:239], v[80:83], v[168:171]\n" \
  "ds_read_b64_tr_b16 v[250:251], v13 offset:49664\n" \
  "s_add_u32 s34, s60, 27648\n" \
  "buffer_load_dwordx4 v[100:103], v10, s[40:43], s34 offen sc1\n" \
  "v_mfma_f32_16x16x32_bf16 v[172:175], v[236:239], v[84:87], v[172:175]\n" \
  "ds_read_b64_tr_b16 v[252:253], v13 offset:57344\n" \
  "v_mfma_f32_16x16x32_bf16 v[172:175], v[232:235], v[76:79], v[172:175]\n" \
  "ds_read_b64_tr_b16 v[254:255], v13 offset:57856\n" \
  "s_waitcnt lgkmcnt(4)\n" \
  "v_mfma_f32_16x16x32_bf16 v[180:183], v[240:243], v[76:79], v[180:183]\n" \
  "ds_read_b64_tr_b16 v[232:233], v13 offset:53248\n" \
  "s_waitcnt vmcnt(6)\n" \
  "s_mov_b32 s35, 0\n" \
  ".Lpoll2_%=:\n" \
  "v_cmp_gt_u32 vcc, 16, v4\n" \
  "s_and_b64 vcc, exec, vcc\n" \
  "s_cbranch_vccz .Lok2_%=\n" \
  "s_add_u32 s34, s58, 0\n" \
  "buffer_load_dword v4, v7, s[40:43], s34 offen sc1\n" \
  "s_add_u32 s35, s35, 1\n" \
  "s_cmp_lt_u32 s35, 0x40000\n" \
  "s_waitcnt vmcnt(0)\n" \
  "s_cbranch_scc1 .Lpoll2_%=\n" \
  ".Lok2_%=:\n" \
  "v_mfma_f32_16x16x32_bf16 v[180:183], v[244:247], v[84:87], v[180:183]\n" \
  "ds_read_b64_tr_b16 v[234:235], v13 offset:53760\n" \
  "v_mfma_f32_16x16x32_bf16 v[176:179], v[244:247], v[80:83], v[176:179]\n" \
  "ds_read_b64_tr_b16 v[236:237], v13 offset:61440\n" \
  "s_add_u32 s34, s60, 10240\n" \
  "buffer_load_dwordx4 v[104:107], v10, s[40:43], s34 offen sc1\n" \
  "v_mfma_f32_16x16x32_bf16 v[176:179], v[240:243], v[72:75], v[176:179]\n" \
  "ds_read_b64_tr_b16 v[238:239], v13 offset:61952\n" \
  "s_waitcnt lgkmcnt(4)\n" \
  "v_mfma_f32_16x16x32_bf16 v[184:187], v[248:251], v[72:75], v[184:187]\n" \
  "ds_read_b64_tr_b16 v[240:241], v14 offset:49152\n" \
  "v_mfma_f32_16x16x32_bf16 v[184:187], v[252:255], v[80:83], v[184:187]\n" \
  "ds_read_b64_tr_b16 v[242:243], v14 offset:49664\n" \
  "v_mfma_f32_16x16x32_bf16 v[188:191], v[252:255], v[84:87], v[188:191]\n" \
  "ds_read_b64_tr_b16 v[244:245], v14 offset:57344\n" \
  "v_mfma_f32_16x16x32_bf16 v[188:191], v[248:251], v[76:79], v[188:191]\n" \
  "ds_read_b64_tr_b16 v[246:247], v14 offset:57856\n" \
  "s_add_u32 s34, s60, 28672\n" \
  "buffer_load_dwordx4 v[108:111], v10, s[40:43], s34 offen sc1\n" \
  "s_waitcnt lgkmcnt(4)\n" \
  "v_mfma_f32_16x16x32_bf16 v[196:199], v[232:235], v[76:79], v[196:199]\n" \
  "ds_read_b64_tr_b16 v[248:249], v14 offset:53248\n" \
  "v_mfma_f32_16x16x32_bf16 v[196:199], v[236:239], v[84:87], v[196:199]\n" \
  "ds_read_b64_tr_b16 v[250:251], v14 offset:53760\n" \
  "v_mfma_f32_16x16x32_bf16 v[192:195], v[236:239], v[80:83], v[192:195]\n" \
  "ds_read_b64_tr_b16 v[252:253], v14 offset:61440\n" \
  "v_mfma_f32_16x16x32_bf16 v[192:195], v[232:235], v[72:75], v[192:195]\n" \
  "ds_read_b64_tr_b16 v[254:255], v14 offset:61952\n" \
  "s_waitcnt lgkmcnt(4)\n" \
  "v_mfma_f32_16x16x32_bf16 v[200:203], v[240:243], v[72:75], v[200:203]\n" \
  "ds_read_b64_tr_b16 v[232:233], v15 offset:49152\n" \
  "s_add_u32 s34, s60, 11264\n" \
  "buffer_load_dwordx4 v[112:115], v10, s[40:43], s34 offen sc1\n" \
  "v_mfma_f32_16x16x32_bf16 v[200:203], v[244:247], v[80:83], v[200:203]\n" \
  "ds_read_b64_tr_b16 v[234:235], v15 offset:49664\n" \
  "v_mfma_f32_16x16x32_bf16 v[204:207], v[244:247], v[84:87], v[204:207]\n" \
  "ds_read_b64_tr_b16 v[236:237], v15 offset:57344\n" \
  "v_mfma_f32_16x16x32_bf16 v[204:207], v[240:243], v[76:79], v[204:207]\n" \
  "ds_read_b64_tr_b16 v[238:239], v15 offset:57856\n" \
  "s_waitcnt lgkmcnt(4)\n" \
  "v_mfma_f32_16x16x32_bf16 v[212:215], v[248:251], v[76:79], v[212:215]\n" \
  "ds_read_b64_tr_b16 v[240:241], v15 offset:53248\n" \
  "v_mfma_f32_16x16x32_bf16 v[212:215], v[252:255], v[84:87], v[212:215]\n" \
  "ds_read_b64_tr_b16 v[242:243], v15 offset:53760\n" \
  "s_add_u32 s34, s60, 29696\n" \
  "buffer_load_dwordx4 v[116:119], v10, s[40:43], s34 offen sc1\n" \
  "v_mfma_f32_16x16x32_bf16 v[208:211], v[252:255], v[80:83], v[208:211]\n" \
  "ds_read_b64_tr_b16 v[244:245], v15 offset:61440\n" \
  "v_mfma_f32_16x16x32_bf16 v[208:211], v[248:251], v[72:75], v[208:211]\n" \
  "ds_read_b64_tr_b16 v[246:247], v15 offset:61952\n" \
  "s_waitcnt lgkmcnt(4)\n" \
  "v_mfma_f32_16x16x32_bf16 v[216:219], v[232:235], v[72:75], v[216:219]\n" \
  "s_waitcnt vmcnt(8)\n" \
  "s_barrier\n" \
  "v_add_u32 v12, 65536, v12\n" \
  "ds_read_b64_tr_b16 v[248:249], v12\n" \
  "v_mfma_f32_16x16x32_bf16 v[216:219], v[236:239], v[80:83], v[216:219]\n" \
  "ds_read_b64_tr_b16 v[250:251], v12 offset:512\n" \
  "v_mfma_f32_16x16x32_bf16 v[220:223], v[236:239], v[84:87], v[220:223]\n" \
  "ds_read_b64_tr_b16 v[252:253], v12 offset:8192\n" \
  "s_add_u32 m0, s57, 16384\n" \
  "s_add_u32 s34, s54, 3538944\n" \
  "buffer_load_dwordx4 v8, s[36:39], s34 offen lds\n" \
  "v_mfma_f32_16x16x32_bf16 v[220:223], v[232:235], v[76:79], v[220:223]\n" \
  "ds_read_b64_tr_b16 v[254:255], v12 offset:8704\n" \
  "s_waitcnt lgkmcnt(4)\n" \
  "v_mfma_f32_16x16x32_bf16 v[228:231], v[240:243], v[76:79], v[228:231]\n" \
  "ds_read_b64_tr_b16 v[232:233], v12 offset:4096\n" \
  "v_mfma_f32_16x16x32_bf16 v[228:231], v[244:247], v[84:87], v[228:231]\n" \
  "ds_read_b64_tr_b16 v[234:235], v12 offset:4608\n" \
  "v_mfma_f32_16x16x32_bf16 v[224:227], v[244:247], v[80:83], v[224:227]\n" \
  "ds_read_b64_tr_b16 v[236:237], v12 offset:12288\n" \
  "v_mfma_f32_16x16x32_bf16 v[224:227], v[240:243], v[72:75], v[224:227]\n" \
  "ds_read_b64_tr_b16 v[238:239], v12 offset:12800\n" \
  "s_add_u32 m0, s57, 24576\n" \
  "s_add_u32 s34, s54, 3833856\n" \
  "buffer_load_dwordx4 v8, s[36:39], s34 offen lds\n" \
  "s_waitcnt vmcnt(6)\n" \
  "s_waitcnt lgkmcnt(4)\n" \
  "v_mfma_f32_16x16x32_bf16 v[168:171], v[248:251], v[88:91], v[168:171]\n" \
  "v_add_u32 v13, 65536, v13\n" \
  "ds_read_b64_tr_b16 v[240:241], v13\n" \
  "v_mfma_f32_16x16x32_bf16 v[168:171], v[252:255], v[96:99], v[168:171]\n" \
  "ds_read_b64_tr_b16 v[242:243], v13 offset:512\n" \
  "v_mfma_f32_16x16x32_bf16 v[172:175], v[252:255], v[100:103], v[172:175]\n" \
  "ds_read_b64_tr_b16 v[244:245], v13 offset:8192\n" \
  "v_mfma_f32_16x16x32_bf16 v[172:175], v[248:251], v[92:95], v[172:175]\n" \
  "ds_read_b64_tr_b16 v[246:247], v13 offset:8704\n" \
  "s_waitcnt lgkmcnt(4)\n" \
  "v_mfma_f32_16x16x32_bf16 v[180:183], v[232:235], v[92:95], v[180:183]\n" \
  "ds_read_b64_tr_b16 v[248:249], v13 offset:4096\n" \
  "s_add_u32 m0, s57, 32768\n" \
  "s_add_u32 s34, s54, 4128768\n" \
  "buffer_load_dwordx4 v8, s[36:39], s34 offen lds\n" \
  "v_mfma_f32_16x16x32_bf16 v[180:183], v[236:239], v[100:103], v[180:183]\n" \
  "ds_read_b64_tr_b16 v[250:251], v13 offset:4608\n" \
  "v_mfma_f32_16x16x32_bf16 v[176:179], v[236:239], v[96:99], v[176:179]\n" \
  "ds_read_b64_tr_b16 v[252:253], v13 offset:12288\n" \
  "v_mfma_f32_16x16x32_bf16 v[176:179], v[232:235], v[88:91], v[176:179]\n" \
  "ds_read_b64_tr_b16 v[254:255], v13 offset:12800\n" \
  "s_waitcnt lgkmcnt(4)\n" \
  "v_mfma_f32_16x16x32_bf16 v[184:187], v[240:243], v[88:91], v[184:187]\n" \
  "v_add_u32 v14, 65536, v14\n" \
  "ds_read_b64_tr_b16 v[232:233], v14\n" \
  "v_mfma_f32_16x16x32_bf16 v[184:187], v[244:247], v[96:99], v[184:187]\n" \
  "ds_read_b64_tr_b16 v[234:235], v14 offset:512\n" \
  "s_add_u32 m0, s57, 40960\n" \
  "s_add_u32 s34, s54, 4423680\n" \
  "buffer_load_dwordx4 v8, s[36:39], s34 offen lds\n" \
  "v_mfma_f32_16x16x32_bf16 v[188:191], v[244:247], v[100:103], v[188:191]\n" \
  "ds_read_b64_tr_b16 v[236:237], v14 offset:8192\n" \
  "v_mfma_f32_16x16x32_bf16 v[188:191], v[240:243], v[92:95], v[188:191]\n" \
  "ds_read_b64_tr_b16 v[238:239], v14 offset:8704\n" \
  "s_waitcnt lgkmcnt(4)\n" \
  "v_mfma_f32_16x16x32_bf16 v[196:199], v[248:251], v[92:95], v[196:199]\n" \
  "ds_read_b64_tr_b16 v[240:241], v14 offset:4096\n" \
  "v_mfma_f32_16x16x32_bf16 v[196:199], v[252:255], v[100:103], v[196:199]\n" \
  "ds_read_b64_tr_b16 v[242:243], v14 offset:4608\n" \
  "v_mfma_f32_16x16x32_bf16 v[192:195], v[252:255], v[96:99], v[192:195]\n" \
  "ds_read_b64_tr_b16 v[244:245], v14 offset:12288\n" \
  "s_add_u32 s34, s60, 12288\n" \
  "buffer_load_dwordx4 v[120:123], v10, s[40:43], s34 offen sc1\n" \
  "v_mfma_f32_16x16x32_bf16 v[192:195], v[248:251], v[88:91], v[192:195]\n" \
  "ds_read_b64_tr_b16 v[246:247], v14 offset:12800\n" \
  "s_waitcnt lgkmcnt(4)\n" \
  "v_mfma_f32_16x16x32_bf16 v[200:203], v[232:235], v[88:91], v[200:203]\n" \
  "v_add_u32 v15, 65536, v15\n" \
  "ds_read_b64_tr_b16 v[248:249], v15\n" \
  "v_mfma_f32_16x16x32_bf16 v[200:203], v[236:239], v[96:99], v[200:203]\n" \
  "ds_read_b64_tr_b16 v[250:251], v15 offset:512\n" \
  "v_mfma_f32_16x16x32_bf16 v[204:207], v[236:239], v[100:103], v[204:207]\n" \
  "ds_read_b64_tr_b16 v[252:253], v15 offset:8192\n" \
  "v_mfma_f32_16x16x32_bf16 v[204:207], v[232:235], v[92:95], v[204:207]\n" \
  "ds_read_b64_tr_b16 v[254:255], v15 offset:8704\n" \
  "s_add_u32 s34, s60, 30720\n" \
  "buffer_load_dwordx4 v[124:127], v10, s[40:43], s34 offen sc1\n" \
  "s_waitcnt lgkmcnt(4)\n" \
  "v_mfma_f32_16x16x32_bf16 v[212:215], v[240:243], v[92:95], v[212:215]\n" \
  "ds_read_b64_tr_b16 v[232:233], v15 offset:4096\n" \
  "v_mfma_f32_16x16x32_bf16 v[212:215], v[244:247], v[100:103], v[212:215]\n" \
  "ds_read_b64_tr_b16 v[234:235], v15 offset:4608\n" \
  "v_mfma_f32_16x16x32_bf16 v[208:211], v[244:247], v[96:99], v[208:211]\n" \
  "ds_read_b64_tr_b16 v[236:237], v15 offset:12288\n" \
  "v_mfma_f32_16x16x32_bf16 v[208:211], v[240:243], v[88:91], v[208:211]\n" \
  "ds_read_b64_tr_b16 v[238:239], v15 offset:12800\n" \
  "s_waitcnt lgkmcnt(4)\n" \
  "v_mfma_f32_16x16x32_bf16 v[216:219], v[248:251], v[88:91], v[216:219]\n" \
  "v_add_u32 v12, 4294819840, v12\n" \
  "ds_read_b64_tr_b16 v[240:241], v12\n" \
  "s_add_u32 s34, s60, 13312\n" \
  "buffer_load_dwordx4 v[128:131], v10, s[40:43], s34 offen sc1\n" \
  "v_mfma_f32_16x16x32_bf16 v[216:219], v[252:255], v[96:99], v[216:219]\n" \
  "ds_read_b64_tr_b16 v[242:243], v12 offset:512\n" \
  "v_mfma_f32_16x16x32_bf16 v[220:223], v[252:255], v[100:103], v[220:223]\n" \
  "ds_read_b64_tr_b16 v[244:245], v12 offset:8192\n" \
  "v_mfma_f32_16x16x32_bf16 v[220:223], v[248:251], v[92:95], v[220:223]\n" \
  "ds_read_b64_tr_b16 v[246:247], v12 offset:8704\n" \
  "s_waitcnt lgkmcnt(4)\n" \
  "v_mfma_f32_16x16x32_bf16 v[228:231], v[232:235], v[92:95], v[228:231]\n" \
  "ds_read_b64_tr_b16 v[248:249], v12 offset:4096\n" \
  "v_mfma_f32_16x16x32_bf16 v[228:231], v[236:239], v[100:103], v[228:231]\n" \
  "ds_read_b64_tr_b16 v[250:251], v12 offset:4608\n" \
  "s_add_u32 s34, s60, 31744\n" \
  "buffer_load_dwordx4 v[132:135], v10, s[40:43], s34 offen sc1\n" \
  "v_mfma_f32_16x16x32_bf16 v[224:227], v[236:239], v[96:99], v[224:227]\n" \
  "ds_read_b64_tr_b16 v[252:253], v12 offset:12288\n" \
  "v_mfma_f32_16x16x32_bf16 v[224:227], v[232:235], v[88:91], v[224:227]\n" \
  "ds_read_b64_tr_b16 v[254:255], v12 offset:12800\n" \
  "s_waitcnt vmcnt(8)\n" \
  "s_waitcnt lgkmcnt(4)\n" \
  "v_mfma_f32_16x16x32_bf16 v[168:171], v[240:243], v[104:107], v[168:171]\n" \
  "v_add_u32 v13, 4294819840, v13\n" \
  "ds_read_b64_tr_b16 v[232:233], v13\n" \
  "v_mfma_f32_16x16x32_bf16 v[168:171], v[244:247], v[112:115], v[168:171]\n" \
  "ds_read_b64_tr_b16 v[234:235], v13 offset:512\n" \
  "v_mfma_f32_16x16x32_bf16 v[172:175], v[244:247], v[116:119], v[172:175]\n" \
  "ds_read_b64_tr_b16 v[236:237], v13 offset:8192\n" \
  "s_add_u32 s34, s60, 14336\n" \
  "buffer_load_dwordx4 v[136:139], v10, s[40:43], s34 offen sc1\n" \
  "v_mfma_f32_16x16x32_bf16 v[172:175], v[240:243], v[108:111], v[172:175]\n" \
  "ds_read_b64_tr_b16 v[238:239], v13 offset:8704\n" \
  "s_waitcnt lgkmcnt(4)\n" \
  "v_mfma_f32_16x16x32_bf16 v[180:183], v[248:251], v[108:111], v[180:183]\n" \
  "ds_read_b64_tr_b16 v[240:241], v13 offset:4096\n" \
  "v_mfma_f32_16x16x32_bf16 v[180:183], v[252:255], v[116:119], v[180:183]\n" \
  "ds_read_b64_tr_b16 v[242:243], v13 offset:4608\n" \
  "v_mfma_f32_16x16x32_bf16 v[176:179], v[252:255], v[112:115], v[176:179]\n" \
  "ds_read_b64_tr_b16 v[244:245], v13 offset:12288\n" \
  "v_mfma_f32_16x16x32_bf16 v[176:179], v[248:251], v[104:107], v[176:179]\n" \
  "ds_read_b64_tr_b16 v[246:247], v13 offset:12800\n" \
  "s_add_u32 s34, s60, 32768\n" \
  "buffer_load_dwordx4 v[140:143], v10, s[40:43], s34 offen sc1\n" \
  "s_waitcnt lgkmcnt(4)\n" \
  "v_mfma_f32_16x16x32_bf16 v[184:187], v[232:235], v[104:107], v[184:187]\n" \
  "v_add_u32 v14, 4294819840, v14\n" \
  "ds_read_b64_tr_b16 v[248:249], v14\n" \
  "v_mfma_f32_16x16x32_bf16 v[184:187], v[236:239], v[112:115], v[184:187]\n" \
  "ds_read_b64_tr_b16 v[250:251], v14 offset:512\n" \
  "v_mfma_f32_16x16x32_bf16 v[188:191], v[236:239], v[116:119], v[188:191]\n" \
  "ds_read_b64_tr_b16 v[252:253], v14 offset:8192\n" \
  "v_mfma_f32_16x16x32_bf16 v[188:191], v[232:235], v[108:111], v[188:191]\n" \
  "ds_read_b64_tr_b16 v[254:255], v14 offset:8704\n" \
  "s_waitcnt lgkmcnt(4)\n" \
  "v_mfma_f32_16x16x32_bf16 v[196:199], v[240:243], v[108:111], v[196:199]\n" \
  "ds_read_b64_tr_b16 v[232:233], v14 offset:4096\n" \
  "s_add_u32 s34, s60, 15360\n" \
  "buffer_load_dwordx4 v[144:147], v10, s[40:43], s34 offen sc1\n" \
  "v_mfma_f32_16x16x32_bf16 v[196:199], v[244:247], v[116:119], v[196:199]\n" \
  "ds_read_b64_tr_b16 v[234:235], v14 offset:4608\n" \
  "v_mfma_f32_16x16x32_bf16 v[192:195], v[244:247], v[112:115], v[192:195]\n" \
  "ds_read_b64_tr_b16 v[236:237], v14 offset:12288\n" \
  "v_mfma_f32_16x16x32_bf16 v[192:195], v[240:243], v[104:107], v[192:195]\n" \
  "ds_read_b64_tr_b16 v[238:239], v14 offset:12800\n" \
  "s_waitcnt lgkmcnt(4)\n" \
  "v_mfma_f32_16x16x32_bf16 v[200:203], v[248:251], v[104:107], v[200:203]\n" \
  "v_add_u32 v15, 4294819840, v15\n" \
  "ds_read_b64_tr_b16 v[240:241], v15\n" \
  "v_mfma_f32_16x16x32_bf16 v[200:203], v[252:255], v[112:115], v[200:203]\n" \
  "ds_read_b64_tr_b16 v[242:243], v15 offset:512\n" \
  "s_add_u32 s34, s60, 33792\n" \
  "buffer_load_dwordx4 v[148:151], v10, s[40:43], s34 offen sc1\n" \
  "v_mfma_f32_16x16x32_bf16 v[204:207], v[252:255], v[116:119], v[204:207]\n" \
  "ds_read_b64_tr_b16 v[244:245], v15 offset:8192\n" \
  "v_mfma_f32_16x16x32_bf16 v[204:207], v[248:251], v[108:111], v[204:207]\n" \
  "ds_read_b64_tr_b16 v[246:247], v15 offset:8704\n" \
  "s_waitcnt lgkmcnt(4)\n" \
  "v_mfma_f32_16x16x32_bf16 v[212:215], v[232:235], v[108:111], v[212:215]\n" \
  "ds_read_b64_tr_b16 v[248:249], v15 offset:4096\n" \
  "v_mfma_f32_16x16x32_bf16 v[212:215], v[236:239], v[116:119], v[212:215]\n" \
  "ds_read_b64_tr_b16 v[250:251], v15 offset:4608\n" \
  "v_mfma_f32_16x16x32_bf16 v[208:211], v[236:239], v[112:115], v[208:211]\n" \
  "ds_read_b64_tr_b16 v[252:253], v15 offset:12288\n" \
  "s_add_u32 m0, s57, 49152\n" \
  "s_add_u32 s34, s54, 4718592\n" \
  "buffer_load_dwordx4 v8, s[36:39], s34 offen lds\n" \
  "v_mfma_f32_16x16x32_bf16 v[208:211], v[232:235], v[104:107], v[208:211]\n" \
  "ds_read_b64_tr_b16 v[254:255], v15 offset:12800\n" \
  "s_waitcnt lgkmcnt(4)\n" \
  "v_mfma_f32_16x16x32_bf16 v[216:219], v[240:243], v[104:107], v[216:219]\n" \
  "s_waitcnt vmcnt(9)\n" \
  "s_barrier\n" \
  "ds_read_b64_tr_b16 v[232:233], v12 offset:16384\n" \
  "v_mfma_f32_16x16x32_bf16 v[216:219], v[244:247], v[112:115], v[216:219]\n" \
  "ds_read_b64_tr_b16 v[234:235], v12 offset:16896\n" \
  "v_mfma_f32_16x16x32_bf16 v[220:223], v[244:247], v[116:119], v[220:223]\n" \
  "ds_read_b64_tr_b16 v[236:237], v12 offset:24576\n" \
  "v_mfma_f32_16x16x32_bf16 v[220:223], v[240:243], v[108:111], v[220:223]\n" \
  "ds_read_b64_tr_b16 v[238:239], v12 offset:25088\n" \
  "s_add_u32 m0, s57, 57344\n" \
  "s_add_u32 s34, s54, 5013504\n" \
  "buffer_load_dwordx4 v8, s[36:39], s34 offen lds\n" \
  "s_waitcnt lgkmcnt(4)\n" \
  "v_mfma_f32_16x16x32_bf16 v[228:231], v[248:251], v[108:111], v[228:231]\n" \
  "ds_read_b64_tr_b16 v[240:241], v12 offset:20480\n" \
  "v_mfma_f32_16x16x32_bf16 v[228:231], v[252:255], v[116:119], v[228:231]\n" \
  "ds_read_b64_tr_b16 v[242:243], v12 offset:20992\n" \
  "v_mfma_f32_16x16x32_bf16 v[224:227], v[252:255], v[112:115], v[224:227]\n" \
  "ds_read_b64_tr_b16 v[244:245], v12 offset:28672\n" \
  "v_mfma_f32_16x16x32_bf16 v[224:227], v[248:251], v[104:107], v[224:227]\n" \
  "ds_read_b64_tr_b16 v[246:247], v12 offset:29184\n" \
  "s_waitcnt vmcnt(6)\n" \
  "s_waitcnt lgkmcnt(4)\n" \
  "v_mfma_f32_16x16x32_bf16 v[168:171], v[232:235], v[120:123], v[168:171]\n" \
  "ds_read_b64_tr_b16 v[248:249], v13 offset:16384\n" \
  "s_add_u32 s34, s60, 16384\n" \
  "buffer_load_dwordx4 v[152:155], v10, s[40:43], s34 offen sc1\n" \
  "v_mfma_f32_16x16x32_bf16 v[168:171], v[236:239], v[128:131], v[168:171]\n" \
  "ds_read_b64_tr_b16 v[250:251], v13 offset:16896\n" \
  "v_mfma_f32_16x16x32_bf16 v[172:175], v[236:239], v[132:135], v[172:175]\n" \
  "ds_read_b64_tr_b16 v[252:253], v13 offset:24576\n" \
  "v_mfma_f32_16x16x32_bf16 v[172:175], v[232:235], v[124:127], v[172:175]\n" \
  "ds_read_b64_tr_b16 v[254:255], v13 offset:25088\n" \
  "s_waitcnt lgkmcnt(4)\n" \
  "v_mfma_f32_16x16x32_bf16 v[180:183], v[240:243], v[124:127], v[180:183]\n" \
  "ds_read_b64_tr_b16 v[232:233], v13 offset:20480\n" \
  "v_mfma_f32_16x16x32_bf16 v[180:183], v[244:247], v[132:135], v[180:183]\n" \
  "ds_read_b64_tr_b16 v[234:235], v13 offset:20992\n" \
  "s_add_u32 s34, s60, 34816\n" \
  "buffer_load_dwordx4 v[156:159], v10, s[40:43], s34 offen sc1\n" \
  "v_mfma_f32_16x16x32_bf16 v[176:179], v[244:247], v[128:131], v[176:179]\n" \
  "ds_read_b64_tr_b16 v[236:237], v13 offset:28672\n" \
  "v_mfma_f32_16x16x32_bf16 v[176:179], v[240:243], v[120:123], v[176:179]\n" \
  "ds_read_b64_tr_b16 v[238:239], v13 offset:29184\n" \
  "s_waitcnt lgkmcnt(4)\n" \
  "v_mfma_f32_16x16x32_bf16 v[184:187], v[248:251], v[120:123], v[184:187]\n" \
  "ds_read_b64_tr_b16 v[240:241], v14 offset:16384\n" \
  "v_mfma_f32_16x16x32_bf16 v[184:187], v[252:255], v[128:131], v[184:187]\n" \
  "ds_read_b64_tr_b16 v[242:243], v14 offset:16896\n" \
  "v_mfma_f32_16x16x32_bf16 v[188:191], v[252:255], v[132:135], v[188:191]\n" \
  "ds_read_b64_tr_b16 v[244:245], v14 offset:24576\n" \
  "s_add_u32 s34, s60, 17408\n" \
  "buffer_load_dwordx4 v[160:163], v10, s[40:43], s34 offen sc1\n" \
  "v_mfma_f32_16x16x32_bf16 v[188:191], v[248:251], v[124:127], v[188:191]\n" \
  "ds_read_b64_tr_b16 v[246:247], v14 offset:25088\n" \
  "s_waitcnt lgkmcnt(4)\n" \
  "v_mfma_f32_16x16x32_bf16 v[196:199], v[232:235], v[124:127], v[196:199]\n" \
  "ds_read_b64_tr_b16 v[248:249], v14 offset:20480\n" \
  "v_mfma_f32_16x16x32_bf16 v[196:199], v[236:239], v[132:135], v[196:199]\n" \
  "ds_read_b64_tr_b16 v[250:251], v14 offset:20992\n" \
  "v_mfma_f32_16x16x32_bf16 v[192:195], v[236:239], v[128:131], v[192:195]\n" \
  "ds_read_b64_tr_b16 v[252:253], v14 offset:28672\n" \
  "v_mfma_f32_16x16x32_bf16 v[192:195], v[232:235], v[120:123], v[192:195]\n" \
  "ds_read_b64_tr_b16 v[254:255], v14 offset:29184\n" \
  "s_add_u32 s34, s60, 35840\n" \
  "buffer_load_dwordx4 v[164:167], v10, s[40:43], s34 offen sc1\n" \
  "s_waitcnt lgkmcnt(4)\n" \
  "v_mfma_f32_16x16x32_bf16 v[200:203], v[240:243], v[120:123], v[200:203]\n" \
  "ds_read_b64_tr_b16 v[232:233], v15 offset:16384\n" \
  "v_mfma_f32_16x16x32_bf16 v[200:203], v[244:247], v[128:131], v[200:203]\n" \
  "ds_read_b64_tr_b16 v[234:235], v15 offset:16896\n" \
  "v_mfma_f32_16x16x32_bf16 v[204:207], v[244:247], v[132:135], v[204:207]\n" \
  "ds_read_b64_tr_b16 v[236:237], v15 offset:24576\n" \
  "v_mfma_f32_16x16x32_bf16 v[204:207], v[240:243], v[124:127], v[204:207]\n" \
  "ds_read_b64_tr_b16 v[238:239], v15 offset:25088\n" \
  "s_waitcnt lgkmcnt(4)\n" \
  "v_mfma_f32_16x16x32_bf16 v[212:215], v[248:251], v[124:127], v[212:215]\n" \
  "ds_read_b64_tr_b16 v[240:241], v15 offset:20480\n" \
  "s_add_u32 m0, s57, 65536\n" \
  "s_add_u32 s34, s55, 0\n" \
  "buffer_load_dwordx4 v10, s[40:43], s34 offen sc1 lds\n" \
  "v_mfma_f32_16x16x32_bf16 v[212:215], v[252:255], v[132:135], v[212:215]\n" \
  "ds_read_b64_tr_b16 v[242:243], v15 offset:20992\n" \
  "v_mfma_f32_16x16x32_bf16 v[208:211], v[252:255], v[128:131], v[208:211]\n" \
  "ds_read_b64_tr_b16 v[244:245], v15 offset:28672\n" \
  "v_mfma_f32_16x16x32_bf16 v[208:211], v[248:251], v[120:123], v[208:211]\n" \
  "ds_read_b64_tr_b16 v[246:247], v15 offset:29184\n" \
  "s_waitcnt lgkmcnt(4)\n" \
  "v_mfma_f32_16x16x32_bf16 v[216:219], v[232:235], v[120:123], v[216:219]\n" \
  "ds_read_b64_tr_b16 v[248:249], v12 offset:32768\n" \
  "v_mfma_f32_16x16x32_bf16 v[216:219], v[236:239], v[128:131], v[216:219]\n" \
  "ds_read_b64_tr_b16 v[250:251], v12 offset:33280\n" \
  "s_add_u32 m0, s57, 73728\n" \
  "s_add_u32 s34, s55, 8192\n" \
  "buffer_load_dwordx4 v10, s[40:43], s34 offen sc1 lds\n" \
  "v_mfma_f32_16x16x32_bf16 v[220:223], v[236:239], v[132:135], v[220:223]\n" \
  "ds_read_b64_tr_b16 v[252:253], v12 offset:40960\n" \
  "v_mfma_f32_16x16x32_bf16 v[220:223], v[232:235], v[124:127], v[220:223]\n" \
  "ds_read_b64_tr_b16 v[254:255], v12 offset:41472\n" \
  "s_waitcnt lgkmcnt(4)\n" \
  "v_mfma_f32_16x16x32_bf16 v[228:231], v[240:243], v[124:127], v[228:231]\n" \
  "ds_read_b64_tr_b16 v[232:233], v12 offset:36864\n" \
  "v_mfma_f32_16x16x32_bf16 v[228:231], v[244:247], v[132:135], v[228:231]\n" \
  "ds_read_b64_tr_b16 v[234:235], v12 offset:37376\n" \
  "v_mfma_f32_16x16x32_bf16 v[224:227], v[244:247], v[128:131], v[224:227]\n" \
  "ds_read_b64_tr_b16 v[236:237], v12 offset:45056\n" \
  "s_add_u32 m0, s57, 81920\n" \
  "s_add_u32 s34, s55, 16384\n" \
  "buffer_load_dwordx4 v10, s[40:43], s34 offen sc1 lds\n" \
  "v_mfma_f32_16x16x32_bf16 v[224:227], v[240:243], v[120:123], v[224:227]\n" \
  "ds_read_b64_tr_b16 v[238:239], v12 offset:45568\n" \
  "s_waitcnt vmcnt(9)\n" \
  "s_waitcnt lgkmcnt(4)\n" \
  "v_mfma_f32_16x16x32_bf16 v[168:171], v[248:251], v[136:139], v[168:171]\n" \
  "ds_read_b64_tr_b16 v[240:241], v13 offset:32768\n" \
  "v_mfma_f32_16x16x32_bf16 v[168:171], v[252:255], v[144:147], v[168:171]\n" \
  "ds_read_b64_tr_b16 v[242:243], v13 offset:33280\n" \
  "v_mfma_f32_16x16x32_bf16 v[172:175], v[252:255], v[148:151], v[172:175]\n" \
  "ds_read_b64_tr_b16 v[244:245], v13 offset:40960\n" \
  "v_mfma_f32_16x16x32_bf16 v[172:175], v[248:251], v[140:143], v[172:175]\n" \
  "ds_read_b64_tr_b16 v[246:247], v13 offset:41472\n" \
  "s_add_u32 m0, s57, 90112\n" \
  "s_add_u32 s34, s55, 24576\n" \
  "buffer_load_dwordx4 v10, s[40:43], s34 offen sc1 lds\n" \
  "s_waitcnt lgkmcnt(4)\n" \
  "v_mfma_f32_16x16x32_bf16 v[180:183], v[232:235], v[140:143], v[180:183]\n" \
  "ds_read_b64_tr_b16 v[248:249], v13 offset:36864\n" \
  "v_mfma_f32_16x16x32_bf16 v[180:183], v[236:239], v[148:151], v[180:183]\n" \
  "ds_read_b64_tr_b16 v[250:251], v13 offset:37376\n" \
  "v_mfma_f32_16x16x32_bf16 v[176:179], v[236:239], v[144:147], v[176:179]\n" \
  "ds_read_b64_tr_b16 v[252:253], v13 offset:45056\n" \
  "v_mfma_f32_16x16x32_bf16 v[176:179], v[232:235], v[136:139], v[176:179]\n" \
  "ds_read_b64_tr_b16 v[254:255], v13 offset:45568\n" \
  "s_waitcnt lgkmcnt(4)\n" \
  "v_mfma_f32_16x16x32_bf16 v[184:187], v[240:243], v[136:139], v[184:187]\n" \
  "ds_read_b64_tr_b16 v[232:233], v14 offset:32768\n" \
  "s_add_u32 m0, s57, 98304\n" \
  "s_add_u32 s34, s55, 32768\n" \
  "buffer_load_dwordx4 v10, s[40:43], s34 offen sc1 lds\n" \
  "v_mfma_f32_16x16x32_bf16 v[184:187], v[244:247], v[144:147], v[184:187]\n" \
  "ds_read_b64_tr_b16 v[234:235], v14 offset:33280\n" \
  "v_mfma_f32_16x16x32_bf16 v[188:191], v[244:247], v[148:151], v[188:191]\n" \
  "ds_read_b64_tr_b16 v[236:237], v14 offset:40960\n" \
  "v_mfma_f32_16x16x32_bf16 v[188:191], v[240:243], v[140:143], v[188:191]\n" \
  "ds_read_b64_tr_b16 v[238:239], v14 offset:41472\n" \
  "s_waitcnt lgkmcnt(4)\n" \
  "v_mfma_f32_16x16x32_bf16 v[196:199], v[248:251], v[140:143], v[196:199]\n" \
  "ds_read_b64_tr_b16 v[240:241], v14 offset:36864\n" \
  "v_mfma_f32_16x16x32_bf16 v[196:199], v[252:255], v[148:151], v[196:199]\n" \
  "ds_read_b64_tr_b16 v[242:243], v14 offset:37376\n" \
  "s_add_u32 m0, s57, 106496\n" \
  "s_add_u32 s34, s54, 128\n" \
  "buffer_load_dwordx4 v8, s[36:39], s34 offen lds\n" \
  "v_mfma_f32_16x16x32_bf16 v[192:195], v[252:255], v[144:147], v[192:195]\n" \
  "ds_read_b64_tr_b16 v[244:245], v14 offset:45056\n" \
  "v_mfma_f32_16x16x32_bf16 v[192:195], v[248:251], v[136:139], v[192:195]\n" \
  "ds_read_b64_tr_b16 v[246:247], v14 offset:45568\n" \
  "s_waitcnt lgkmcnt(4)\n" \
  "v_mfma_f32_16x16x32_bf16 v[200:203], v[232:235], v[136:139], v[200:203]\n" \
  "ds_read_b64_tr_b16 v[248:249], v15 offset:32768\n" \
  "v_mfma_f32_16x16x32_bf16 v[200:203], v[236:239], v[144:147], v[200:203]\n" \
  "ds_read_b64_tr_b16 v[250:251], v15 offset:33280\n" \
  "v_mfma_f32_16x16x32_bf16 v[204:207], v[236:239], v[148:151], v[204:207]\n" \
  "ds_read_b64_tr_b16 v[252:253], v15 offset:40960\n" \
  "s_add_u32 m0, s57, 114688\n" \
  "s_add_u32 s34, s54, 295040\n" \
  "buffer_load_dwordx4 v8, s[36:39], s34 offen lds\n" \
  "v_mfma_f32_16x16x32_bf16 v[204:207], v[232:235], v[140:143], v[204:207]\n" \
  "ds_read_b64_tr_b16 v[254:255], v15 offset:41472\n" \
  "s_waitcnt lgkmcnt(4)\n" \
  "v_mfma_f32_16x16x32_bf16 v[212:215], v[240:243], v[140:143], v[212:215]\n" \
  "ds_read_b64_tr_b16 v[232:233], v15 offset:36864\n" \
  "v_mfma_f32_16x16x32_bf16 v[212:215], v[244:247], v[148:151], v[212:215]\n" \
  "ds_read_b64_tr_b16 v[234:235], v15 offset:37376\n" \
  "v_mfma_f32_16x16x32_bf16 v[208:211], v[244:247], v[144:147], v[208:211]\n" \
  "ds_read_b64_tr_b16 v[236:237], v15 offset:45056\n" \
  "v_mfma_f32_16x16x32_bf16 v[208:211], v[240:243], v[136:139], v[208:211]\n" \
  "ds_read_b64_tr_b16 v[238:239], v15 offset:45568\n" \
  "s_add_u32 m0, s57, 122880\n" \
  "s_add_u32 s34, s54, 589952\n" \
  "buffer_load_dwordx4 v8, s[36:39], s34 offen lds\n" \
  "s_waitcnt lgkmcnt(4)\n" \
  "v_mfma_f32_16x16x32_bf16 v[216:219], v[248:251], v[136:139], v[216:219]\n" \
  "s_waitcnt vmcnt(12)\n" \
  "s_barrier\n" \
  "ds_read_b64_tr_b16 v[240:241], v12 offset:49152\n" \
  "v_mfma_f32_16x16x32_bf16 v[216:219], v[252:255], v[144:147], v[216:219]\n" \
  "ds_read_b64_tr_b16 v[242:243], v12 offset:49664\n" \
  "v_mfma_f32_16x16x32_bf16 v[220:223], v[252:255], v[148:151], v[220:223]\n" \
  "ds_read_b64_tr_b16 v[244:245], v12 offset:57344\n" \
  "v_mfma_f32_16x16x32_bf16 v[220:223], v[248:251], v[140:143], v[220:223]\n" \
  "ds_read_b64_tr_b16 v[246:247], v12 offset:57856\n" \
  "s_waitcnt lgkmcnt(4)\n" \
  "v_mfma_f32_16x16x32_bf16 v[228:231], v[232:235], v[140:143], v[228:231]\n" \
  "ds_read_b64_tr_b16 v[248:249], v12 offset:53248\n" \
  "s_add_u32 m0, s57, 131072\n" \
  "s_add_u32 s34, s54, 884864\n" \
  "buffer_load_dwordx4 v8, s[36:39], s34 offen lds\n" \
  "v_mfma_f32_16x16x32_bf16 v[228:231], v[236:239], v[148:151], v[228:231]\n" \
  "ds_read_b64_tr_b16 v[250:251], v12 offset:53760\n" \
  "v_mfma_f32_16x16x32_bf16 v[224:227], v[236:239], v[144:147], v[224:227]\n" \
  "ds_read_b64_tr_b16 v[252:253], v12 offset:61440\n" \
  "v_mfma_f32_16x16x32_bf16 v[224:227], v[232:235], v[136:139], v[224:227]\n" \
  "ds_read_b64_tr_b16 v[254:255], v12 offset:61952\n" \
  "s_waitcnt vmcnt(9)\n" \
  "s_waitcnt lgkmcnt(4)\n" \
  "v_mfma_f32_16x16x32_bf16 v[168:171], v[240:243], v[152:155], v[168:171]\n" \
  "ds_read_b64_tr_b16 v[232:233], v13 offset:49152\n" \
  "v_mfma_f32_16x16x32_bf16 v[168:171], v[244:247], v[160:163], v[168:171]\n" \
  "ds_read_b64_tr_b16 v[234:235], v13 offset:49664\n" \
  "s_add_u32 m0, s57, 139264\n" \
  "s_add_u32 s34, s54, 1179776\n" \
  "buffer_load_dwordx4 v8, s[36:39], s34 offen lds\n" \
  "v_mfma_f32_16x16x32_bf16 v[172:175], v[244:247], v[164:167], v[172:175]\n" \
  "ds_read_b64_tr_b16 v[236:237], v13 offset:57344\n" \
  "v_mfma_f32_16x16x32_bf16 v[172:175], v[240:243], v[156:159], v[172:175]\n" \
  "ds_read_b64_tr_b16 v[238:239], v13 offset:57856\n" \
  "s_waitcnt lgkmcnt(4)\n" \
  "v_mfma_f32_16x16x32_bf16 v[180:183], v[248:251], v[156:159], v[180:183]\n" \
  "ds_read_b64_tr_b16 v[240:241], v13 offset:53248\n" \
  "v_mfma_f32_16x16x32_bf16 v[180:183], v[252:255], v[164:167], v[180:183]\n" \
  "ds_read_b64_tr_b16 v[242:243], v13 offset:53760\n" \
  "v_mfma_f32_16x16x32_bf16 v[176:179], v[252:255], v[160:163], v[176:179]\n" \
  "ds_read_b64_tr_b16 v[244:245], v13 offset:61440\n" \
  "s_add_u32 m0, s57, 147456\n" \
  "s_add_u32 s34, s54, 1474688\n" \
  "buffer_load_dwordx4 v8, s[36:39], s34 offen lds\n" \
  "v_mfma_f32_16x16x32_bf16 v[176:179], v[248:251], v[152:155], v[176:179]\n" \
  "ds_read_b64_tr_b16 v[246:247], v13 offset:61952\n" \
  "s_waitcnt lgkmcnt(4)\n" \
  "v_mfma_f32_16x16x32_bf16 v[184:187], v[232:235], v[152:155], v[184:187]\n" \
  "ds_read_b64_tr_b16 v[248:249], v14 offset:49152\n" \
  "v_mfma_f32_16x16x32_bf16 v[184:187], v[236:239], v[160:163], v[184:187]\n" \
  "ds_read_b64_tr_b16 v[250:251], v14 offset:49664\n" \
  "v_mfma_f32_16x16x32_bf16 v[188:191], v[236:239], v[164:167], v[188:191]\n" \
  "ds_read_b64_tr_b16 v[252:253], v14 offset:57344\n" \
  "v_mfma_f32_16x16x32_bf16 v[188:191], v[232:235], v[156:159], v[188:191]\n" \
  "ds_read_b64_tr_b16 v[254:255], v14 offset:57856\n" \
  "s_add_u32 m0, s57, 155648\n" \
  "s_add_u32 s34, s54, 1769600\n" \
  "buffer_load_dwordx4 v8, s[36:39], s34 offen lds\n" \
  "s_waitcnt lgkmcnt(4)\n" \
  "v_mfma_f32_16x16x32_bf16 v[196:199], v[240:243], v[156:159], v[196:199]\n" \
  "ds_read_b64_tr_b16 v[232:233], v14 offset:53248\n" \
  "v_mfma_f32_16x16x32_bf16 v[196:199], v[244:247], v[164:167], v[196:199]\n" \
  "ds_read_b64_tr_b16 v[234:235], v14 offset:53760\n" \
  "v_mfma_f32_16x16x32_bf16 v[192:195], v[244:247], v[160:163], v[192:195]\n" \
  "ds_read_b64_tr_b16 v[236:237], v14 offset:61440\n" \
  "v_mfma_f32_16x16x32_bf16 v[192:195], v[240:243], v[152:155], v[192:195]\n" \
  "ds_read_b64_tr_b16 v[238:239], v14 offset:61952\n" \
  "s_waitcnt lgkmcnt(4)\n" \
  "v_mfma_f32_16x16x32_bf16 v[200:203], v[248:251], v[152:155], v[200:203]\n" \
  "ds_read_b64_tr_b16 v[240:241], v15 offset:49152\n" \
  "s_add_u32 m0, s57, 0\n" \
  "s_add_u32 s34, s54, 2064512\n" \
  "buffer_load_dwordx4 v8, s[36:39], s34 offen lds\n" \
  "v_mfma_f32_16x16x32_bf16 v[200:203], v[252:255], v[160:163], v[200:203]\n" \
  "ds_read_b64_tr_b16 v[242:243], v15 offset:49664\n" \
  "v_mfma_f32_16x16x32_bf16 v[204:207], v[252:255], v[164:167], v[204:207]\n" \
  "ds_read_b64_tr_b16 v[244:245], v15 offset:57344\n" \
  "v_mfma_f32_16x16x32_bf16 v[204:207], v[248:251], v[156:159], v[204:207]\n" \
  "ds_read_b64_tr_b16 v[246:247], v15 offset:57856\n" \
  "s_waitcnt lgkmcnt(4)\n" \
  "v_mfma_f32_16x16x32_bf16 v[212:215], v[232:235], v[156:159], v[212:215]\n" \
  "ds_read_b64_tr_b16 v[248:249], v15 offset:53248\n" \
  "s_nop 3\n" \
  "v_mul_f32 v168, v168, v176\n" \
  "v_mul_f32 v169, v169, v177\n" \
  "v_mul_f32 v170, v170, v178\n" \
  "v_mul_f32 v171, v171, v179\n" \
  "v_mfma_f32_16x16x32_bf16 v[212:215], v[236:239], v[164:167], v[212:215]\n" \
  "ds_read_b64_tr_b16 v[250:251], v15 offset:53760\n" \
  "v_mul_f32 v184, v184, v192\n" \
  "v_mul_f32 v185, v185, v193\n" \
  "v_mul_f32 v186, v186, v194\n" \
  "v_mul_f32 v187, v187, v195\n" \
  "v_cvt_pk_bf16_f32 v192, v168, v169\n" \
  "s_add_u32 m0, s57, 8192\n" \
  "s_add_u32 s34, s54, 2359424\n" \
  "buffer_load_dwordx4 v8, s[36:39], s34 offen lds\n" \
  "v_mfma_f32_16x16x32_bf16 v[208:211], v[236:239], v[160:163], v[208:211]\n" \
  "ds_read_b64_tr_b16 v[252:253], v15 offset:61440\n" \
  "v_cvt_pk_bf16_f32 v193, v170, v171\n" \
  "v_cvt_pk_bf16_f32 v194, v184, v185\n" \
  "v_cvt_pk_bf16_f32 v195, v186, v187\n" \
  "v_mul_f32 v172, v172, v180\n" \
  "v_mfma_f32_16x16x32_bf16 v[208:211], v[232:235], v[152:155], v[208:211]\n" \
  "ds_read_b64_tr_b16 v[254:255], v15 offset:61952\n" \
  "v_mul_f32 v173, v173, v181\n" \
  "v_mul_f32 v174, v174, v182\n" \
  "v_mul_f32 v175, v175, v183\n" \
  "v_mul_f32 v188, v188, v196\n" \
  "v_mul_f32 v189, v189, v197\n" \
  "s_waitcnt lgkmcnt(4)\n" \
  "v_mfma_f32_16x16x32_bf16 v[216:219], v[240:243], v[152:155], v[216:219]\n" \
  "s_waitcnt vmcnt(10)\n" \
  "s_barrier\n" \
  "v_add_u32 v9, 65536, v9\n" \
  "v_mul_f32 v190, v190, v198\n" \
  "v_mul_f32 v191, v191, v199\n" \
  "v_cvt_pk_bf16_f32 v196, v172, v173\n" \
  "v_cvt_pk_bf16_f32 v197, v174, v175\n" \
  "v_cvt_pk_bf16_f32 v198, v188, v189\n" \
  "v_mfma_f32_16x16x32_bf16 v[216:219], v[244:247], v[160:163], v[216:219]\n" \
  "ds_read_b128 v[232:235], v9\n" \
  "v_cvt_pk_bf16_f32 v199, v190, v191\n" \
  "v_mfma_f32_16x16x32_bf16 v[220:223], v[244:247], v[164:167], v[220:223]\n" \
  "ds_read_b128 v[236:239], v9 offset:2048\n" \
  "buffer_store_dwordx4 v[192:195], v10, s[40:43], s61 offen sc1\n" \
  "v_mfma_f32_16x16x32_bf16 v[220:223], v[240:243], v[156:159], v[220:223]\n" \
  "s_waitcnt lgkmcnt(2)\n" \
  "v_mfma_f32_16x16x32_bf16 v[228:231], v[248:251], v[156:159], v[228:231]\n" \
  "ds_read_b128 v[240:243], v9 offset:1024\n" \
  "v_mfma_f32_16x16x32_bf16 v[228:231], v[252:255], v[164:167], v[228:231]\n" \
  "ds_read_b128 v[244:247], v9 offset:3072\n" \
  "v_mfma_f32_16x16x32_bf16 v[224:227], v[252:255], v[160:163], v[224:227]\n" \
  "v_mfma_f32_16x16x32_bf16 v[224:227], v[248:251], v[152:155], v[224:227]\n" \
  "s_nop 1\n" \
  "s_waitcnt lgkmcnt(2)\n" \
  "v_mfma_f32_16x16x32_bf16 v[168:171], v[232:235], v[24:27], 0\n" \
  "ds_read_b128 v[248:251], v9 offset:4096\n" \
  "v_mfma_f32_16x16x32_bf16 v[168:171], v[236:239], v[32:35], v[168:171]\n" \
  "ds_read_b128 v[252:255], v9 offset:6144\n" \
  "v_mfma_f32_16x16x32_bf16 v[172:175], v[236:239], v[36:39], 0\n" \
  "v_mfma_f32_16x16x32_bf16 v[172:175], v[232:235], v[28:31], v[172:175]\n" \
  "s_waitcnt lgkmcnt(2)\n" \
  "v_mfma_f32_16x16x32_bf16 v[180:183], v[240:243], v[28:31], 0\n" \
  "ds_read_b128 v[232:235], v9 offset:5120\n" \
  "s_nop 3\n" \
  "v_mul_f32 v200, v200, v208\n" \
  "v_mul_f32 v201, v201, v209\n" \
  "v_mul_f32 v202, v202, v210\n" \
  "v_mul_f32 v203, v203, v211\n" \
  "v_mfma_f32_16x16x32_bf16 v[180:183], v[244:247], v[36:39], v[180:183]\n" \
  "ds_read_b128 v[236:239], v9 offset:7168\n" \
  "v_mul_f32 v216, v216, v224\n" \
  "v_mul_f32 v217, v217, v225\n" \
  "v_mul_f32 v218, v218, v226\n" \
  "v_mul_f32 v219, v219, v227\n" \
  "v_cvt_pk_bf16_f32 v224, v200, v201\n" \
  "v_mfma_f32_16x16x32_bf16 v[176:179], v[244:247], v[32:35], 0\n" \
  "v_cvt_pk_bf16_f32 v225, v202, v203\n" \
  "v_cvt_pk_bf16_f32 v226, v216, v217\n" \
  "v_cvt_pk_bf16_f32 v227, v218, v219\n" \
  "v_mul_f32 v204, v204, v212\n" \
  "v_mfma_f32_16x16x32_bf16 v[176:179], v[240:243], v[24:27], v[176:179]\n" \
  "v_mul_f32 v205, v205, v213\n" \
  "v_mul_f32 v206, v206, v214\n" \
  "v_mul_f32 v207, v207, v215\n" \
  "v_mul_f32 v220, v220, v228\n" \
  "v_mul_f32 v221, v221, v229\n" \
  "buffer_store_dwordx4 v[196:199], v10, s[40:43], s61 offen offset:1024 sc1\n" \
  "s_waitcnt lgkmcnt(2)\n" \
  "v_mfma_f32_16x16x32_bf16 v[168:171], v[248:251], v[40:43], v[168:171]\n" \
  "ds_read_b128 v[240:243], v9 offset:8192\n" \
  "v_mul_f32 v222, v222, v230\n" \
  "v_mul_f32 v223, v223, v231\n" \
  "v_cvt_pk_bf16_f32 v228, v204, v205\n" \
  "v_cvt_pk_bf16_f32 v229, v206, v207\n" \
  "v_cvt_pk_bf16_f32 v230, v220, v221\n" \
  "v_mfma_f32_16x16x32_bf16 v[168:171], v[252:255], v[48:51], v[168:171]\n" \
  "ds_read_b128 v[244:247], v9 offset:10240\n" \
  "v_cvt_pk_bf16_f32 v231, v222, v223\n" \
  "v_mfma_f32_16x16x32_bf16 v[172:175], v[252:255], v[52:55], v[172:175]\n" \
  "v_mfma_f32_16x16x32_bf16 v[172:175], v[248:251], v[44:47], v[172:175]\n" \
  "s_waitcnt lgkmcnt(2)\n" \
  "v_mfma_f32_16x16x32_bf16 v[180:183], v[232:235], v[44:47], v[180:183]\n" \
  "ds_read_b128 v[248:251], v9 offset:9216\n" \
  "v_mfma_f32_16x16x32_bf16 v[180:183], v[236:239], v[52:55], v[180:183]\n" \
  "ds_read_b128 v[252:255], v9 offset:11264\n" \
  "v_mfma_f32_16x16x32_bf16 v[176:179], v[236:239], v[48:51], v[176:179]\n" \
  "v_mfma_f32_16x16x32_bf16 v[176:179], v[232:235], v[40:43], v[176:179]\n" \
  "s_waitcnt lgkmcnt(2)\n" \
  "v_mfma_f32_16x16x32_bf16 v[168:171], v[240:243], v[56:59], v[168:171]\n" \
  "ds_read_b128 v[232:235], v9 offset:12288\n" \
  "v_mfma_f32_16x16x32_bf16 v[168:171], v[244:247], v[64:67], v[168:171]\n" \
  "ds_read_b128 v[236:239], v9 offset:14336\n" \
  "v_mfma_f32_16x16x32_bf16 v[172:175], v[244:247], v[68:71], v[172:175]\n" \
  "v_mfma_f32_16x16x32_bf16 v[172:175], v[240:243], v[60:63], v[172:175]\n" \
  "s_waitcnt lgkmcnt(2)\n" \
  "v_mfma_f32_16x16x32_bf16 v[180:183], v[248:251], v[60:63], v[180:183]\n" \
  "ds_read_b128 v[240:243], v9 offset:13312\n" \
  "s_add_u32 s34, s61, 8192\n" \
  "buffer_store_dwordx4 v[224:227], v10, s[40:43], s34 offen sc1\n" \
  "v_mfma_f32_16x16x32_bf16 v[180:183], v[252:255], v[68:71], v[180:183]\n" \
  "ds_read_b128 v[244:247], v9 offset:15360\n" \
  "v_mfma_f32_16x16x32_bf16 v[176:179], v[252:255], v[64:67], v[176:179]\n" \
  "v_mfma_f32_16x16x32_bf16 v[176:179], v[248:251], v[56:59], v[176:179]\n" \
  "s_waitcnt lgkmcnt(2)\n" \
  "v_mfma_f32_16x16x32_bf16 v[168:171], v[232:235], v[72:75], v[168:171]\n" \
  "ds_read_b128 v[248:251], v9 offset:16384\n" \
  "v_mfma_f32_16x16x32_bf16 v[168:171], v[236:239], v[80:83], v[168:171]\n" \
  "ds_read_b128 v[252:255], v9 offset:18432\n" \
  "v_mfma_f32_16x16x32_bf16 v[172:175], v[236:239], v[84:87], v[172:175]\n" \
  "v_mfma_f32_16x16x32_bf16 v[172:175], v[232:235], v[76:79], v[172:175]\n" \
  "s_waitcnt lgkmcnt(2)\n" \
  "v_mfma_f32_16x16x32_bf16 v[180:183], v[240:243], v[76:79], v[180:183]\n" \
  "ds_read_b128 v[232:235], v9 offset:17408\n" \
  "v_mfma_f32_16x16x32_bf16 v[180:183], v[244:247], v[84:87], v[180:183]\n" \
  "ds_read_b128 v[236:239], v9 offset:19456\n" \
  "v_mfma_f32_16x16x32_bf16 v[176:179], v[244:247], v[80:83], v[176:179]\n" \
  "v_mfma_f32_16x16x32_bf16 v[176:179], v[240:243], v[72:75], v[176:179]\n" \
  "s_waitcnt lgkmcnt(2)\n" \
  "v_mfma_f32_16x16x32_bf16 v[168:171], v[248:251], v[88:91], v[168:171]\n" \
  "ds_read_b128 v[240:243], v9 offset:20480\n" \
  "v_mfma_f32_16x16x32_bf16 v[168:171], v[252:255], v[96:99], v[168:171]\n" \
  "ds_read_b128 v[244:247], v9 offset:22528\n" \
  "s_add_u32 s34, s61, 9216\n" \
  "buffer_store_dwordx4 v[228:231], v10, s[40:43], s34 offen sc1\n" \
  "v_mfma_f32_16x16x32_bf16 v[172:175], v[252:255], v[100:103], v[172:175]\n" \
  "v_mfma_f32_16x16x32_bf16 v[172:175], v[248:251], v[92:95], v[172:175]\n" \
  "s_waitcnt lgkmcnt(2)\n" \
  "v_mfma_f32_16x16x32_bf16 v[180:183], v[232:235], v[92:95], v[180:183]\n" \
  "ds_read_b128 v[248:251], v9 offset:21504\n" \
  "v_mfma_f32_16x16x32_bf16 v[180:183], v[236:239], v[100:103], v[180:183]\n" \
  "ds_read_b128 v[252:255], v9 offset:23552\n" \
  "v_mfma_f32_16x16x32_bf16 v[176:179], v[236:239], v[96:99], v[176:179]\n" \
  "v_mfma_f32_16x16x32_bf16 v[176:179], v[232:235], v[88:91], v[176:179]\n" \
  "s_waitcnt lgkmcnt(2)\n" \
  "v_mfma_f32_16x16x32_bf16 v[168:171], v[240:243], v[104:107], v[168:171]\n" \
  "ds_read_b128 v[232:235], v9 offset:24576\n" \
  "v_mfma_f32_16x16x32_bf16 v[168:171], v[244:247], v[112:115], v[168:171]\n" \
  "ds_read_b128 v[236:239], v9 offset:26624\n" \
  "v_mfma_f32_16x16x32_bf16 v[172:175], v[244:247], v[116:119], v[172:175]\n" \
  "v_mfma_f32_16x16x32_bf16 v[172:175], v[240:243], v[108:111], v[172:175]\n" \
  "s_waitcnt lgkmcnt(2)\n" \
  "v_mfma_f32_16x16x32_bf16 v[180:183], v[248:251], v[108:111], v[180:183]\n" \
  "ds_read_b128 v[240:243], v9 offset:25600\n" \
  "v_mfma_f32_16x16x32_bf16 v[180:183], v[252:255], v[116:119], v[180:183]\n" \
  "ds_read_b128 v[244:247], v9 offset:27648\n" \
  "v_mfma_f32_16x16x32_bf16 v[176:179], v[252:255], v[112:115], v[176:179]\n" \
  "s_add_u32 m0, s57, 16384\n" \
  "s_add_u32 s34, s54, 2654336\n" \
  "buffer_load_dwordx4 v8, s[36:39], s34 offen lds\n" \
  "v_mfma_f32_16x16x32_bf16 v[176:179], v[248:251], v[104:107], v[176:179]\n" \
  "s_waitcnt lgkmcnt(2)\n" \
  "v_mfma_f32_16x16x32_bf16 v[168:171], v[232:235], v[120:123], v[168:171]\n" \
  "ds_read_b128 v[248:251], v9 offset:28672\n" \
  "v_mfma_f32_16x16x32_bf16 v[168:171], v[236:239], v[128:131], v[168:171]\n" \
  "ds_read_b128 v[252:255], v9 offset:30720\n" \
  "v_mfma_f32_16x16x32_bf16 v[172:175], v[236:239], v[132:135], v[172:175]\n" \
  "v_mfma_f32_16x16x32_bf16 v[172:175], v[232:235], v[124:127], v[172:175]\n" \
  "s_add_u32 m0, s57, 24576\n" \
  "s_add_u32 s34, s54, 2949248\n" \
  "buffer_load_dwordx4 v8, s[36:39], s34 offen lds\n" \
  "s_waitcnt lgkmcnt(2)\n" \
  "v_mfma_f32_16x16x32_bf16 v[180:183], v[240:243], v[124:127], v[180:183]\n" \
  "ds_read_b128 v[232:235], v9 offset:29696\n" \
  "v_mfma_f32_16x16x32_bf16 v[180:183], v[244:247], v[132:135], v[180:183]\n" \
  "ds_read_b128 v[236:239], v9 offset:31744\n" \
  "v_mfma_f32_16x16x32_bf16 v[176:179], v[244:247], v[128:131], v[176:179]\n" \
  "v_mfma_f32_16x16x32_bf16 v[176:179], v[240:243], v[120:123], v[176:179]\n" \
  "s_waitcnt lgkmcnt(2)\n" \
  "v_mfma_f32_16x16x32_bf16 v[168:171], v[248:251], v[136:139], v[168:171]\n" \
  "s_waitcnt vmcnt(15)\n" \
  "s_barrier\n" \
  "ds_read_b128 v[240:243], v9 offset:32768\n" \
  "s_add_u32 m0, s57, 32768\n" \
  "s_add_u32 s34, s54, 3244160\n" \
  "buffer_load_dwordx4 v8, s[36:39], s34 offen lds\n" \
  "v_mfma_f32_16x16x32_bf16 v[168:171], v[252:255], v[144:147], v[168:171]\n" \
  "ds_read_b128 v[244:247], v9 offset:34816\n" \
  "v_mfma_f32_16x16x32_bf16 v[172:175], v[252:255], v[148:151], v[172:175]\n" \
  "v_mfma_f32_16x16x32_bf16 v[172:175], v[248:251], v[140:143], v[172:175]\n" \
  "s_waitcnt lgkmcnt(2)\n" \
  "v_mfma_f32_16x16x32_bf16 v[180:183], v[232:235], v[140:143], v[180:183]\n" \
  "ds_read_b128 v[248:251], v9 offset:33792\n" \
  "v_mfma_f32_16x16x32_bf16 v[180:183], v[236:239], v[148:151], v[180:183]\n" \
  "ds_read_b128 v[252:255], v9 offset:35840\n" \
  "s_add_u32 m0, s57, 40960\n" \
  "s_add_u32 s34, s54, 3539072\n" \
  "buffer_load_dwordx4 v8, s[36:39], s34 offen lds\n" \
  "v_mfma_f32_16x16x32_bf16 v[176:179], v[236:239], v[144:147], v[176:179]\n" \
  "v_mfma_f32_16x16x32_bf16 v[176:179], v[232:235], v[136:139], v[176:179]\n" \
  "s_waitcnt lgkmcnt(2)\n" \
  "v_mfma_f32_16x16x32_bf16 v[168:171], v[240:243], v[152:155], v[168:171]\n" \
  "s_waitcnt vmcnt(13)\n" \
  "s_barrier\n" \
  "v_add_u32 v12, 106496, v12\n" \
  "ds_read_b64_tr_b16 v[232:233], v12\n" \
  "v_mfma_f32_16x16x32_bf16 v[168:171], v[244:247], v[160:163], v[168:171]\n" \
  "ds_read_b64_tr_b16 v[234:235], v12 offset:512\n" \
  "v_mfma_f32_16x16x32_bf16 v[172:175], v[244:247], v[164:167], v[172:175]\n" \
  "ds_read_b64_tr_b16 v[236:237], v12 offset:8192\n" \
  "s_add_u32 m0, s57, 49152\n" \
  "s_add_u32 s34, s54, 3833984\n" \
  "buffer_load_dwordx4 v8, s[36:39], s34 offen lds\n" \
  "v_mfma_f32_16x16x32_bf16 v[172:175], v[240:243], v[156:159], v[172:175]\n" \
  "ds_read_b64_tr_b16 v[238:239], v12 offset:8704\n" \
  "s_waitcnt lgkmcnt(4)\n" \
  "v_mfma_f32_16x16x32_bf16 v[180:183], v[248:251], v[156:159], v[180:183]\n" \
  "ds_read_b64_tr_b16 v[240:241], v12 offset:4096\n" \
  "v_mfma_f32_16x16x32_bf16 v[180:183], v[252:255], v[164:167], v[180:183]\n" \
  "ds_read_b64_tr_b16 v[242:243], v12 offset:4608\n" \
  "v_mfma_f32_16x16x32_bf16 v[176:179], v[252:255], v[160:163], v[176:179]\n" \
  "ds_read_b64_tr_b16 v[244:245], v12 offset:12288\n" \
  "v_mfma_f32_16x16x32_bf16 v[176:179], v[248:251], v[152:155], v[176:179]\n" \
  "ds_read_b64_tr_b16 v[246:247], v12 offset:12800\n" \
  "s_add_u32 m0, s57, 57344\n" \
  "s_add_u32 s34, s54, 4128896\n" \
  "buffer_load_dwordx4 v8, s[36:39], s34 offen lds\n" \
  "s_nop 1\n" \
  "s_waitcnt lgkmcnt(4)\n" \
  "v_mfma_f32_16x16x32_bf16 v[200:203], v[232:235], v[24:27], 0\n" \
  "v_add_u32 v13, 106496, v13\n" \
  "ds_read_b64_tr_b16 v[248:249], v13\n" \
  "v_mfma_f32_16x16x32_bf16 v[200:203], v[236:239], v[32:35], v[200:203]\n" \
  "ds_read_b64_tr_b16 v[250:251], v13 offset:512\n" \
  "v_mfma_f32_16x16x32_bf16 v[204:207], v[236:239], v[36:39], 0\n" \
  "ds_read_b64_tr_b16 v[252:253], v13 offset:8192\n" \
  "v_mfma_f32_16x16x32_bf16 v[204:207], v[232:235], v[28:31], v[204:207]\n" \
  "ds_read_b64_tr_b16 v[254:255], v13 offset:8704\n" \
  "s_waitcnt lgkmcnt(4)\n" \
  "v_mfma_f32_16x16x32_bf16 v[212:215], v[240:243], v[28:31], 0\n" \
  "ds_read_b64_tr_b16 v[232:233], v13 offset:4096\n" \
  "s_nop 3\n" \
  "v_mul_f32 v168, v168, v176\n" \
  "v_mul_f32 v169, v169, v177\n" \
  "v_mul_f32 v170, v170, v178\n" \
  "v_mul_f32 v171, v171, v179\n" \
  "s_add_u32 m0, s57, 65536\n" \
  "s_add_u32 s34, s54, 4423808\n" \
  "buffer_load_dwordx4 v8, s[36:39], s34 offen lds\n" \
  "v_mfma_f32_16x16x32_bf16 v[212:215], v[244:247], v[36:39], v[212:215]\n" \
  "ds_read_b64_tr_b16 v[234:235], v13 offset:4608\n" \
  "v_cvt_pk_bf16_f32 v0, v168, v169\n" \
  "v_cvt_pk_bf16_f32 v1, v170, v171\n" \
  "v_mul_f32 v172, v172, v180\n" \
  "v_mul_f32 v173, v173, v181\n" \
  "v_mfma_f32_16x16x32_bf16 v[208:211], v[244:247], v[32:35], 0\n" \
  "ds_read_b64_tr_b16 v[236:237], v13 offset:12288\n" \
  "v_mul_f32 v174, v174, v182\n" \
  "v_mul_f32 v175, v175, v183\n" \
  "v_cvt_pk_bf16_f32 v2, v172, v173\n" \
  "v_cvt_pk_bf16_f32 v3, v174, v175\n" \
  "v_mfma_f32_16x16x32_bf16 v[208:211], v[240:243], v[24:27], v[208:211]\n" \
  "ds_read_b64_tr_b16 v[238:239], v13 offset:12800\n" \
  "s_waitcnt lgkmcnt(4)\n" \
  "v_mfma_f32_16x16x32_bf16 v[216:219], v[248:251], v[24:27], 0\n" \
  "v_add_u32 v14, 106496, v14\n" \
  "ds_read_b64_tr_b16 v[240:241], v14\n" \
  "v_mfma_f32_16x16x32_bf16 v[216:219], v[252:255], v[32:35], v[216:219]\n" \
  "ds_read_b64_tr_b16 v[242:243], v14 offset:512\n" \
  "buffer_store_dwordx2 v[0:1], v11, s[40:43], s62 offen sc1\n" \
  "v_mfma_f32_16x16x32_bf16 v[220:223], v[252:255], v[36:39], 0\n" \
  "ds_read_b64_tr_b16 v[244:245], v14 offset:8192\n" \
  "v_mfma_f32_16x16x32_bf16 v[220:223], v[248:251], v[28:31], v[220:223]\n" \
  "ds_read_b64_tr_b16 v[246:247], v14 offset:8704\n" \
  "s_waitcnt lgkmcnt(4)\n" \
  "v_mfma_f32_16x16x32_bf16 v[228:231], v[232:235], v[28:31], 0\n" \
  "ds_read_b64_tr_b16 v[248:249], v14 offset:4096\n" \
  "v_mfma_f32_16x16x32_bf16 v[228:231], v[236:239], v[36:39], v[228:231]\n" \
  "ds_read_b64_tr_b16 v[250:251], v14 offset:4608\n" \
  "v_mfma_f32_16x16x32_bf16 v[224:227], v[236:239], v[32:35], 0\n" \
  "ds_read_b64_tr_b16 v[252:253], v14 offset:12288\n" \
  "v_mfma_f32_16x16x32_bf16 v[224:227], v[232:235], v[24:27], v[224:227]\n" \
  "ds_read_b64_tr_b16 v[254:255], v14 offset:12800\n" \
  "buffer_store_dwordx2 v[2:3], v11, s[40:43], s62 offen offset:1024 sc1\n" \
  "s_nop 1\n" \
  "s_waitcnt lgkmcnt(4)\n" \
  "v_mfma_f32_16x16x32_bf16 v[168:171], v[240:243], v[24:27], 0\n" \
  "v_add_u32 v15, 106496, v15\n" \
  "ds_read_b64_tr_b16 v[232:233], v15\n" \
  "v_mfma_f32_16x16x32_bf16 v[168:171], v[244:247], v[32:35], v[168:171]\n" \
  "ds_read_b64_tr_b16 v[234:235], v15 offset:512\n" \
  "v_mfma_f32_16x16x32_bf16 v[172:175], v[244:247], v[36:39], 0\n" \
  "ds_read_b64_tr_b16 v[236:237], v15 offset:8192\n" \
  "v_mfma_f32_16x16x32_bf16 v[172:175], v[240:243], v[28:31], v[172:175]\n" \
  "ds_read_b64_tr_b16 v[238:239], v15 offset:8704\n" \
  "s_waitcnt lgkmcnt(4)\n" \
  "v_mfma_f32_16x16x32_bf16 v[180:183], v[248:251], v[28:31], 0\n" \
  "ds_read_b64_tr_b16 v[240:241], v15 offset:4096\n" \
  "v_mfma_f32_16x16x32_bf16 v[180:183], v[252:255], v[36:39], v[180:183]\n" \
  "ds_read_b64_tr_b16 v[242:243], v15 offset:4608\n" \
  "v_mfma_f32_16x16x32_bf16 v[176:179], v[252:255], v[32:35], 0\n" \
  "ds_read_b64_tr_b16 v[244:245], v15 offset:12288\n" \
  "s_add_u32 m0, s57, 73728\n" \
  "s_add_u32 s34, s54, 4718720\n" \
  "buffer_load_dwordx4 v8, s[36:39], s34 offen lds\n" \
  "v_mfma_f32_16x16x32_bf16 v[176:179], v[248:251], v[24:27], v[176:179]\n" \
  "ds_read_b64_tr_b16 v[246:247], v15 offset:12800\n" \
  "s_waitcnt lgkmcnt(4)\n" \
  "v_mfma_f32_16x16x32_bf16 v[184:187], v[232:235], v[24:27], 0\n" \
  "ds_read_b64_tr_b16 v[248:249], v12 offset:16384\n" \
  "s_waitcnt vmcnt(10)\n" \
  "s_mov_b64 exec, 1\n" \
  "v_mov_b32 v5, 1\n" \
  "buffer_atomic_add v5, off, s[40:43], s66\n" \
  "s_mov_b64 exec, -1\n" \
  "v_mfma_f32_16x16x32_bf16 v[184:187], v[236:239], v[32:35], v[184:187]\n" \
  "ds_read_b64_tr_b16 v[250:251], v12 offset:16896\n" \
  "v_mfma_f32_16x16x32_bf16 v[188:191], v[236:239], v[36:39], 0\n" \
  "ds_read_b64_tr_b16 v[252:253], v12 offset:24576\n" \
  "v_mfma_f32_16x16x32_bf16 v[188:191], v[232:235], v[28:31], v[188:191]\n" \
  "ds_read_b64_tr_b16 v[254:255], v12 offset:25088\n" \
  "s_add_u32 m0, s57, 81920\n" \
  "s_add_u32 s34, s54, 5013632\n" \
  "buffer_load_dwordx4 v8, s[36:39], s34 offen lds\n" \
  "s_waitcnt lgkmcnt(4)\n" \
  "v_mfma_f32_16x16x32_bf16 v[196:199], v[240:243], v[28:31], 0\n" \
  "ds_read_b64_tr_b16 v[232:233], v12 offset:20480\n" \
  "v_mfma_f32_16x16x32_bf16 v[196:199], v[244:247], v[36:39], v[196:199]\n" \
  "ds_read_b64_tr_b16 v[234:235], v12 offset:20992\n" \
  "v_mfma_f32_16x16x32_bf16 v[192:195], v[244:247], v[32:35], 0\n" \
  "ds_read_b64_tr_b16 v[236:237], v12 offset:28672\n" \
  "v_mfma_f32_16x16x32_bf16 v[192:195], v[240:243], v[24:27], v[192:195]\n" \
  "ds_read_b64_tr_b16 v[238:239], v12 offset:29184\n" \
  "s_waitcnt lgkmcnt(4)\n" \
  "v_mfma_f32_16x16x32_bf16 v[200:203], v[248:251], v[40:43], v[200:203]\n" \
  "ds_read_b64_tr_b16 v[240:241], v13 offset:16384\n" \
  "v_mfma_f32_16x16x32_bf16 v[200:203], v[252:255], v[48:51], v[200:203]\n" \
  "ds_read_b64_tr_b16 v[242:243], v13 offset:16896\n" \
  "v_mfma_f32_16x16x32_bf16 v[204:207], v[252:255], v[52:55], v[204:207]\n" \
  "ds_read_b64_tr_b16 v[244:245], v13 offset:24576\n" \
  "v_mfma_f32_16x16x32_bf16 v[204:207], v[248:251], v[44:47], v[204:207]\n" \
  "ds_read_b64_tr_b16 v[246:247], v13 offset:25088\n" \
  "s_waitcnt lgkmcnt(4)\n" \
  "v_mfma_f32_16x16x32_bf16 v[212:215], v[232:235], v[44:47], v[212:215]\n" \
  "ds_read_b64_tr_b16 v[248:249], v13 offset:20480\n" \
  "v_mfma_f32_16x16x32_bf16 v[212:215], v[236:239], v[52:55], v[212:215]\n" \
  "ds_read_b64_tr_b16 v[250:251], v13 offset:20992\n" \
  "v_mfma_f32_16x16x32_bf16 v[208:211], v[236:239], v[48:51], v[208:211]\n" \
  "ds_read_b64_tr_b16 v[252:253], v13 offset:28672\n" \
  "v_mfma_f32_16x16x32_bf16 v[208:211], v[232:235], v[40:43], v[208:211]\n" \
  "ds_read_b64_tr_b16 v[254:255], v13 offset:29184\n" \
  "s_waitcnt lgkmcnt(4)\n" \
  "v_mfma_f32_16x16x32_bf16 v[216:219], v[240:243], v[40:43], v[216:219]\n" \
  "ds_read_b64_tr_b16 v[232:233], v14 offset:16384\n" \
  "v_mfma_f32_16x16x32_bf16 v[216:219], v[244:247], v[48:51], v[216:219]\n" \
  "ds_read_b64_tr_b16 v[234:235], v14 offset:16896\n" \
  "v_mfma_f32_16x16x32_bf16 v[220:223], v[244:247], v[52:55], v[220:223]\n" \
  "ds_read_b64_tr_b16 v[236:237], v14 offset:24576\n" \
  "v_mfma_f32_16x16x32_bf16 v[220:223], v[240:243], v[44:47], v[220:223]\n" \
  "ds_read_b64_tr_b16 v[238:239], v14 offset:25088\n" \
  "s_waitcnt lgkmcnt(4)\n" \
  "v_mfma_f32_16x16x32_bf16 v[228:231], v[248:251], v[44:47], v[228:231]\n" \
  "ds_read_b64_tr_b16 v[240:241], v14 offset:20480\n" \
  "v_mfma_f32_16x16x32_bf16 v[228:231], v[252:255], v[52:55], v[228:231]\n" \
  "ds_read_b64_tr_b16 v[242:243], v14 offset:20992\n" \
  "v_mfma_f32_16x16x32_bf16 v[224:227], v[252:255], v[48:51], v[224:227]\n" \
  "ds_read_b64_tr_b16 v[244:245], v14 offset:28672\n" \
  "v_mfma_f32_16x16x32_bf16 v[224:227], v[248:251], v[40:43], v[224:227]\n" \
  "ds_read_b64_tr_b16 v[246:247], v14 offset:29184\n" \
  "s_waitcnt lgkmcnt(4)\n" \
  "v_mfma_f32_16x16x32_bf16 v[168:171], v[232:235], v[40:43], v[168:171]\n" \
  "ds_read_b64_tr_b16 v[248:249], v15 offset:16384\n" \
  "v_mfma_f32_16x16x32_bf16 v[168:171], v[236:239], v[48:51], v[168:171]\n" \
  "ds_read_b64_tr_b16 v[250:251], v15 offset:16896\n" \
  "v_mfma_f32_16x16x32_bf16 v[172:175], v[236:239], v[52:55], v[172:175]\n" \
  "ds_read_b64_tr_b16 v[252:253], v15 offset:24576\n" \
  "v_mfma_f32_16x16x32_bf16 v[172:175], v[232:235], v[44:47], v[172:175]\n" \
  "ds_read_b64_tr_b16 v[254:255], v15 offset:25088\n" \
  "s_waitcnt lgkmcnt(4)\n" \
  "v_mfma_f32_16x16x32_bf16 v[180:183], v[240:243], v[44:47], v[180:183]\n" \
  "ds_read_b64_tr_b16 v[232:233], v15 offset:20480\n" \
  "s_mov_b64 exec, 1\n" \
  "v_mov_b32 v4, 0\n" \
  "buffer_atomic_add v4, off, s[40:43], s67 sc0\n" \
  "s_mov_b64 exec, -1\n" \
  "v_mfma_f32_16x16x32_bf16 v[180:183], v[244:247], v[52:55], v[180:183]\n" \
  "ds_read_b64_tr_b16 v[234:235], v15 offset:20992\n" \
  "v_mfma_f32_16x16x32_bf16 v[176:179], v[244:247], v[48:51], v[176:179]\n" \
  "ds_read_b64_tr_b16 v[236:237], v15 offset:28672\n" \
  "v_mfma_f32_16x16x32_bf16 v[176:179], v[240:243], v[40:43], v[176:179]\n" \
  "ds_read_b64_tr_b16 v[238:239], v15 offset:29184\n" \
  "s_waitcnt lgkmcnt(4)\n" \
  "v_mfma_f32_16x16x32_bf16 v[184:187], v[248:251], v[40:43], v[184:187]\n" \
  "s_barrier\n" \
  "ds_read_b64_tr_b16 v[240:241], v12 offset:32768\n" \
  "s_add_u32 m0, s57, 90112\n" \
  "s_add_u32 s34, s65, 0\n" \
  "buffer_load_dwordx4 v10, s[40:43], s34 offen sc1 lds\n" \
  "v_mfma_f32_16x16x32_bf16 v[184:187], v[252:255], v[48:51], v[184:187]\n" \
  "ds_read_b64_tr_b16 v[242:243], v12 offset:33280\n" \
  "v_mfma_f32_16x16x32_bf16 v[188:191], v[252:255], v[52:55], v[188:191]\n" \
  "ds_read_b64_tr_b16 v[244:245], v12 offset:40960\n" \
  "v_mfma_f32_16x16x32_bf16 v[188:191], v[248:251], v[44:47], v[188:191]\n" \
  "ds_read_b64_tr_b16 v[246:247], v12 offset:41472\n" \
  "s_waitcnt lgkmcnt(4)\n" \
  "v_mfma_f32_16x16x32_bf16 v[196:199], v[232:235], v[44:47], v[196:199]\n" \
  "ds_read_b64_tr_b16 v[248:249], v12 offset:36864\n" \
  "v_mfma_f32_16x16x32_bf16 v[196:199], v[236:239], v[52:55], v[196:199]\n" \
  "ds_read_b64_tr_b16 v[250:251], v12 offset:37376\n" \
  "s_add_u32 m0, s57, 98304\n" \
  "s_add_u32 s34, s65, 8192\n" \
  "buffer_load_dwordx4 v10, s[40:43], s34 offen sc1 lds\n" \
  "v_mfma_f32_16x16x32_bf16 v[192:195], v[236:239], v[48:51], v[192:195]\n" \
  "ds_read_b64_tr_b16 v[252:253], v12 offset:45056\n" \
  "v_mfma_f32_16x16x32_bf16 v[192:195], v[232:235], v[40:43], v[192:195]\n" \
  "ds_read_b64_tr_b16 v[254:255], v12 offset:45568\n" \
  "s_waitcnt lgkmcnt(4)\n" \
  "v_mfma_f32_16x16x32_bf16 v[200:203], v[240:243], v[56:59], v[200:203]\n" \
  "ds_read_b64_tr_b16 v[232:233], v13 offset:32768\n" \
  "v_mfma_f32_16x16x32_bf16 v[200:203], v[244:247], v[64:67], v[200:203]\n" \
  "ds_read_b64_tr_b16 v[234:235], v13 offset:33280\n" \
  "v_mfma_f32_16x16x32_bf16 v[204:207], v[244:247], v[68:71], v[204:207]\n" \
  "ds_read_b64_tr_b16 v[236:237], v13 offset:40960\n" \
  "s_add_u32 m0, s57, 106496\n" \
  "s_add_u32 s34, s65, 16384\n" \
  "buffer_load_dwordx4 v10, s[40:43], s34 offen sc1 lds\n" \
  "v_mfma_f32_16x16x32_bf16 v[204:207], v[240:243], v[60:63], v[204:207]\n" \
  "ds_read_b64_tr_b16 v[238:239], v13 offset:41472\n" \
  "s_waitcnt lgkmcnt(4)\n" \
  "v_mfma_f32_16x16x32_bf16 v[212:215], v[248:251], v[60:63], v[212:215]\n" \
  "ds_read_b64_tr_b16 v[240:241], v13 offset:36864\n" \
  "s_waitcnt vmcnt(7)\n" \
  "s_mov_b64 exec, 1\n" \
  "v_mov_b32 v5, 256\n" \
  "buffer_atomic_add v5, off, s[40:43], s66\n" \
  "s_mov_b64 exec, -1\n" \
  "v_mfma_f32_16x16x32_bf16 v[212:215], v[252:255], v[68:71], v[212:215]\n" \
  "ds_read_b64_tr_b16 v[242:243], v13 offset:37376\n" \
  "v_mfma_f32_16x16x32_bf16 v[208:211], v[252:255], v[64:67], v[208:211]\n" \
  "ds_read_b64_tr_b16 v[244:245], v13 offset:45056\n" \
  "v_mfma_f32_16x16x32_bf16 v[208:211], v[248:251], v[56:59], v[208:211]\n" \
  "ds_read_b64_tr_b16 v[246:247], v13 offset:45568\n" \
  "s_add_u32 m0, s57, 114688\n" \
  "s_add_u32 s34, s65, 24576\n" \
  "buffer_load_dwordx4 v10, s[40:43], s34 offen sc1 lds\n" \
  "s_waitcnt lgkmcnt(4)\n" \
  "v_mfma_f32_16x16x32_bf16 v[216:219], v[232:235], v[56:59], v[216:219]\n" \
  "ds_read_b64_tr_b16 v[248:249], v14 offset:32768\n" \
  "s_waitcnt vmcnt(5)\n" \
  "s_mov_b32 s35, 0x40000\n" \
  ".Lpoll3_%=:\n" \
  "v_readfirstlane_b32 s34, v4\n" \
  "s_and_b32 s34, s34, 255\n" \
  "s_cmp_eq_u32 s34, 4\n" \
  "s_cbranch_scc1 .Lok3_%=\n" \
  "s_mov_b64 exec, 1\n" \
  "v_mov_b32 v4, 0\n" \
  "buffer_atomic_add v4, off, s[40:43], s67 sc0\n" \
  "s_mov_b64 exec, -1\n" \
  "s_sub_u32 s35, s35, 1\n" \
  "s_waitcnt vmcnt(0)\n" \
  "s_cbranch_scc0 .Lpoll3_%=\n" \
  ".Lok3_%=:\n" \
  "v_mfma_f32_16x16x32_bf16 v[216:219], v[236:239], v[64:67], v[216:219]\n" \
  "ds_read_b64_tr_b16 v[250:251], v14 offset:33280\n" \
  "v_mfma_f32_16x16x32_bf16 v[220:223], v[236:239], v[68:71], v[220:223]\n" \
  "ds_read_b64_tr_b16 v[252:253], v14 offset:40960\n" \
  "v_mfma_f32_16x16x32_bf16 v[220:223], v[232:235], v[60:63], v[220:223]\n" \
  "ds_read_b64_tr_b16 v[254:255], v14 offset:41472\n" \
  "s_waitcnt lgkmcnt(4)\n" \
  "v_mfma_f32_16x16x32_bf16 v[228:231], v[240:243], v[60:63], v[228:231]\n" \
  "ds_read_b64_tr_b16 v[232:233], v14 offset:36864\n" \
  "buffer_load_dwordx4 v[24:27], v10, s[40:43], s63 offen sc1\n" \
  "v_mfma_f32_16x16x32_bf16 v[228:231], v[244:247], v[68:71], v[228:231]\n" \
  "ds_read_b64_tr_b16 v[234:235], v14 offset:37376\n" \
  "v_mfma_f32_16x16x32_bf16 v[224:227], v[244:247], v[64:67], v[224:227]\n" \
  "ds_read_b64_tr_b16 v[236:237], v14 offset:45056\n" \
  "v_mfma_f32_16x16x32_bf16 v[224:227], v[240:243], v[56:59], v[224:227]\n" \
  "ds_read_b64_tr_b16 v[238:239], v14 offset:45568\n" \
  "s_waitcnt lgkmcnt(4)\n" \
  "v_mfma_f32_16x16x32_bf16 v[168:171], v[248:251], v[56:59], v[168:171]\n" \
  "ds_read_b64_tr_b16 v[240:241], v15 offset:32768\n" \
  "v_mfma_f32_16x16x32_bf16 v[168:171], v[252:255], v[64:67], v[168:171]\n" \
  "ds_read_b64_tr_b16 v[242:243], v15 offset:33280\n" \
  "buffer_load_dwordx4 v[28:31], v10, s[40:43], s64 offen sc1\n" \
  "v_mfma_f32_16x16x32_bf16 v[172:175], v[252:255], v[68:71], v[172:175]\n" \
  "ds_read_b64_tr_b16 v[244:245], v15 offset:40960\n" \
  "v_mfma_f32_16x16x32_bf16 v[172:175], v[248:251], v[60:63], v[172:175]\n" \
  "ds_read_b64_tr_b16 v[246:247], v15 offset:41472\n" \
  "s_waitcnt lgkmcnt(4)\n" \
  "v_mfma_f32_16x16x32_bf16 v[180:183], v[232:235], v[60:63], v[180:183]\n" \
  "ds_read_b64_tr_b16 v[248:249], v15 offset:36864\n" \
  "v_mfma_f32_16x16x32_bf16 v[180:183], v[236:239], v[68:71], v[180:183]\n" \
  "ds_read_b64_tr_b16 v[250:251], v15 offset:37376\n" \
  "v_mfma_f32_16x16x32_bf16 v[176:179], v[236:239], v[64:67], v[176:179]\n" \
  "ds_read_b64_tr_b16 v[252:253], v15 offset:45056\n" \
  "buffer_load_dwordx4 v[32:35], v10, s[40:43], s63 offen offset:2048 sc1\n" \
  "v_mfma_f32_16x16x32_bf16 v[176:179], v[232:235], v[56:59], v[176:179]\n" \
  "ds_read_b64_tr_b16 v[254:255], v15 offset:45568\n" \
  "s_waitcnt lgkmcnt(4)\n" \
  "v_mfma_f32_16x16x32_bf16 v[184:187], v[240:243], v[56:59], v[184:187]\n" \
  "ds_read_b64_tr_b16 v[232:233], v12 offset:49152\n" \
  "ds_read_b64_tr_b16 v[234:235], v12 offset:49664\n" \
  "v_mfma_f32_16x16x32_bf16 v[184:187], v[244:247], v[64:67], v[184:187]\n" \
  "v_add_u32 v12, 4294860800, v12\n" \
  "v_mfma_f32_16x16x32_bf16 v[188:191], v[244:247], v[68:71], v[188:191]\n" \
  "ds_read_b64_tr_b16 v[236:237], v12\n" \
  "v_mfma_f32_16x16x32_bf16 v[188:191], v[240:243], v[60:63], v[188:191]\n" \
  "ds_read_b64_tr_b16 v[238:239], v12 offset:512\n" \
  "buffer_load_dwordx4 v[36:39], v10, s[40:43], s64 offen offset:2048 sc1\n" \
  "s_waitcnt lgkmcnt(4)\n" \
  "v_mfma_f32_16x16x32_bf16 v[196:199], v[248:251], v[60:63], v[196:199]\n" \
  "v_add_u32 v12, 155648, v12\n" \
  "ds_read_b64_tr_b16 v[240:241], v12 offset:4096\n" \
  "v_mfma_f32_16x16x32_bf16 v[196:199], v[252:255], v[68:71], v[196:199]\n" \
  "ds_read_b64_tr_b16 v[242:243], v12 offset:4608\n" \
  "v_add_u32 v12, 4294811648, v12\n" \
  "v_mfma_f32_16x16x32_bf16 v[192:195], v[252:255], v[64:67], v[192:195]\n" \
  "ds_read_b64_tr_b16 v[244:245], v12 offset:4096\n" \
  "v_mfma_f32_16x16x32_bf16 v[192:195], v[248:251], v[56:59], v[192:195]\n" \
  "ds_read_b64_tr_b16 v[246:247], v12 offset:4608\n" \
  "s_waitcnt lgkmcnt(4)\n" \
  "v_mfma_f32_16x16x32_bf16 v[200:203], v[232:235], v[72:75], v[200:203]\n" \
  "ds_read_b64_tr_b16 v[248:249], v13 offset:49152\n" \
  "ds_read_b64_tr_b16 v[250:251], v13 offset:49664\n" \
  "s_add_u32 s34, s63, 4096\n" \
  "buffer_load_dwordx4 v[40:43], v10, s[40:43], s34 offen sc1\n" \
  "v_mfma_f32_16x16x32_bf16 v[200:203], v[236:239], v[80:83], v[200:203]\n" \
  "v_add_u32 v13, 4294860800, v13\n" \
  "v_mfma_f32_16x16x32_bf16 v[204:207], v[236:239], v[84:87], v[204:207]\n" \
  "ds_read_b64_tr_b16 v[252:253], v13\n" \
  "v_mfma_f32_16x16x32_bf16 v[204:207], v[232:235], v[76:79], v[204:207]\n" \
  "ds_read_b64_tr_b16 v[254:255], v13 offset:512\n" \
  "s_waitcnt lgkmcnt(4)\n" \
  "v_mfma_f32_16x16x32_bf16 v[212:215], v[240:243], v[76:79], v[212:215]\n" \
  "v_add_u32 v13, 155648, v13\n" \
  "ds_read_b64_tr_b16 v[232:233], v13 offset:4096\n" \
  "v_mfma_f32_16x16x32_bf16 v[212:215], v[244:247], v[84:87], v[212:215]\n" \
  "ds_read_b64_tr_b16 v[234:235], v13 offset:4608\n" \
  "v_add_u32 v13, 4294811648, v13\n" \
  "s_add_u32 s34, s64, 4096\n" \
  "buffer_load_dwordx4 v[44:47], v10, s[40:43], s34 offen sc1\n" \
  "v_mfma_f32_16x16x32_bf16 v[208:211], v[244:247], v[80:83], v[208:211]\n" \
  "ds_read_b64_tr_b16 v[236:237], v13 offset:4096\n" \
  "v_mfma_f32_16x16x32_bf16 v[208:211], v[240:243], v[72:75], v[208:211]\n" \
  "ds_read_b64_tr_b16 v[238:239], v13 offset:4608\n" \
  "s_waitcnt lgkmcnt(4)\n" \
  "v_mfma_f32_16x16x32_bf16 v[216:219], v[248:251], v[72:75], v[216:219]\n" \
  "ds_read_b64_tr_b16 v[240:241], v14 offset:49152\n" \
  "ds_read_b64_tr_b16 v[242:243], v14 offset:49664\n" \
  "v_mfma_f32_16x16x32_bf16 v[216:219], v[252:255], v[80:83], v[216:219]\n" \
  "v_add_u32 v14, 4294860800, v14\n" \
  "v_mfma_f32_16x16x32_bf16 v[220:223], v[252:255], v[84:87], v[220:223]\n" \
  "ds_read_b64_tr_b16 v[244:245], v14\n" \
  "s_add_u32 s34, s63, 6144\n" \
  "buffer_load_dwordx4 v[48:51], v10, s[40:43], s34 offen sc1\n" \
  "v_mfma_f32_16x16x32_bf16 v[220:223], v[248:251], v[76:79], v[220:223]\n" \
  "ds_read_b64_tr_b16 v[246:247], v14 offset:512\n" \
  "s_waitcnt lgkmcnt(4)\n" \
  "v_mfma_f32_16x16x32_bf16 v[228:231], v[232:235], v[76:79], v[228:231]\n" \
  "v_add_u32 v14, 155648, v14\n" \
  "ds_read_b64_tr_b16 v[248:249], v14 offset:4096\n" \
  "v_mfma_f32_16x16x32_bf16 v[228:231], v[236:239], v[84:87], v[228:231]\n" \
  "ds_read_b64_tr_b16 v[250:251], v14 offset:4608\n" \
  "v_add_u32 v14, 4294811648, v14\n" \
  "v_mfma_f32_16x16x32_bf16 v[224:227], v[236:239], v[80:83], v[224:227]\n" \
  "ds_read_b64_tr_b16 v[252:253], v14 offset:4096\n" \
  "v_mfma_f32_16x16x32_bf16 v[224:227], v[232:235], v[72:75], v[224:227]\n" \
  "ds_read_b64_tr_b16 v[254:255], v14 offset:4608\n" \
  "s_add_u32 s34, s64, 6144\n" \
  "buffer_load_dwordx4 v[52:55], v10, s[40:43], s34 offen sc1\n" \
  "s_waitcnt lgkmcnt(4)\n" \
  "v_mfma_f32_16x16x32_bf16 v[168:171], v[240:243], v[72:75], v[168:171]\n" \
  "ds_read_b64_tr_b16 v[232:233], v15 offset:49152\n" \
  "ds_read_b64_tr_b16 v[234:235], v15 offset:49664\n" \
  "v_mfma_f32_16x16x32_bf16 v[168:171], v[244:247], v[80:83], v[168:171]\n" \
  "v_add_u32 v15, 4294860800, v15\n" \
  "v_mfma_f32_16x16x32_bf16 v[172:175], v[244:247], v[84:87], v[172:175]\n" \
  "ds_read_b64_tr_b16 v[236:237], v15\n" \
  "v_mfma_f32_16x16x32_bf16 v[172:175], v[240:243], v[76:79], v[172:175]\n" \
  "ds_read_b64_tr_b16 v[238:239], v15 offset:512\n" \
  "s_waitcnt lgkmcnt(4)\n" \
  "v_mfma_f32_16x16x32_bf16 v[180:183], v[248:251], v[76:79], v[180:183]\n" \
  "v_add_u32 v15, 155648, v15\n" \
  "ds_read_b64_tr_b16 v[240:241], v15 offset:4096\n" \
  "s_add_u32 s34, s63, 8192\n" \
  "buffer_load_dwordx4 v[56:59], v10, s[40:43], s34 offen sc1\n" \
  "v_mfma_f32_16x16x32_bf16 v[180:183], v[252:255], v[84:87], v[180:183]\n" \
  "ds_read_b64_tr_b16 v[242:243], v15 offset:4608\n" \
  "v_add_u32 v15, 4294811648, v15\n" \
  "v_mfma_f32_16x16x32_bf16 v[176:179], v[252:255], v[80:83], v[176:179]\n" \
  "ds_read_b64_tr_b16 v[244:245], v15 offset:4096\n" \
  "v_mfma_f32_16x16x32_bf16 v[176:179], v[248:251], v[72:75], v[176:179]\n" \
  "ds_read_b64_tr_b16 v[246:247], v15 offset:4608\n" \
  "s_waitcnt lgkmcnt(4)\n" \
  "v_mfma_f32_16x16x32_bf16 v[184:187], v[232:235], v[72:75], v[184:187]\n" \
  "s_barrier\n" \
  "ds_read_b64_tr_b16 v[248:249], v12 offset:8192\n" \
  "v_mfma_f32_16x16x32_bf16 v[184:187], v[236:239], v[80:83], v[184:187]\n" \
  "ds_read_b64_tr_b16 v[250:251], v12 offset:8704\n" \
  "s_add_u32 m0, s57, 122880\n" \
  "s_add_u32 s34, s65, 32768\n" \
  "buffer_load_dwordx4 v10, s[40:43], s34 offen sc1 lds\n" \
  "v_mfma_f32_16x16x32_bf16 v[188:191], v[236:239], v[84:87], v[188:191]\n" \
  "ds_read_b64_tr_b16 v[252:253], v12 offset:16384\n" \
  "v_mfma_f32_16x16x32_bf16 v[188:191], v[232:235], v[76:79], v[188:191]\n" \
  "ds_read_b64_tr_b16 v[254:255], v12 offset:16896\n" \
  "s_waitcnt lgkmcnt(4)\n" \
  "v_mfma_f32_16x16x32_bf16 v[196:199], v[240:243], v[76:79], v[196:199]\n" \
  "ds_read_b64_tr_b16 v[232:233], v12 offset:12288\n" \
  "v_mfma_f32_16x16x32_bf16 v[196:199], v[244:247], v[84:87], v[196:199]\n" \
  "ds_read_b64_tr_b16 v[234:235], v12 offset:12800\n" \
  "v_mfma_f32_16x16x32_bf16 v[192:195], v[244:247], v[80:83], v[192:195]\n" \
  "ds_read_b64_tr_b16 v[236:237], v12 offset:20480\n" \
  "s_add_u32 m0, s57, 131072\n" \
  "s_add_u32 s34, s65, 40960\n" \
  "buffer_load_dwordx4 v10, s[40:43], s34 offen sc1 lds\n" \
  "v_mfma_f32_16x16x32_bf16 v[192:195], v[240:243], v[72:75], v[192:195]\n" \
  "ds_read_b64_tr_b16 v[238:239], v12 offset:20992\n" \
  "s_waitcnt lgkmcnt(4)\n" \
  "v_mfma_f32_16x16x32_bf16 v[200:203], v[248:251], v[88:91], v[200:203]\n" \
  "ds_read_b64_tr_b16 v[240:241], v13 offset:8192\n" \
  "v_mfma_f32_16x16x32_bf16 v[200:203], v[252:255], v[96:99], v[200:203]\n" \
  "ds_read_b64_tr_b16 v[242:243], v13 offset:8704\n" \
  "v_mfma_f32_16x16x32_bf16 v[204:207], v[252:255], v[100:103], v[204:207]\n" \
  "ds_read_b64_tr_b16 v[244:245], v13 offset:16384\n" \
  "v_mfma_f32_16x16x32_bf16 v[204:207], v[248:251], v[92:95], v[204:207]\n" \
  "ds_read_b64_tr_b16 v[246:247], v13 offset:16896\n" \
  "s_add_u32 m0, s57, 139264\n" \
  "s_add_u32 s34, s65, 49152\n" \
  "buffer_load_dwordx4 v10, s[40:43], s34 offen sc1 lds\n" \
  "s_waitcnt lgkmcnt(4)\n" \
  "v_mfma_f32_16x16x32_bf16 v[212:215], v[232:235], v[92:95], v[212:215]\n" \
  "ds_read_b64_tr_b16 v[248:249], v13 offset:12288\n" \
  "s_mov_b64 exec, 1\n" \
  "v_mov_b32 v4, 0\n" \
  "buffer_atomic_add v4, off, s[40:43], s67 sc0\n" \
  "s_mov_b64 exec, -1\n" \
  "v_mfma_f32_16x16x32_bf16 v[212:215], v[236:239], v[100:103], v[212:215]\n" \
  "ds_read_b64_tr_b16 v[250:251], v13 offset:12800\n" \
  "v_mfma_f32_16x16x32_bf16 v[208:211], v[236:239], v[96:99], v[208:211]\n" \
  "ds_read_b64_tr_b16 v[252:253], v13 offset:20480\n" \
  "v_mfma_f32_16x16x32_bf16 v[208:211], v[232:235], v[88:91], v[208:211]\n" \
  "ds_read_b64_tr_b16 v[254:255], v13 offset:20992\n" \
  "s_waitcnt lgkmcnt(4)\n" \
  "v_mfma_f32_16x16x32_bf16 v[216:219], v[240:243], v[88:91], v[216:219]\n" \
  "ds_read_b64_tr_b16 v[232:233], v14 offset:8192\n" \
  "s_add_u32 m0, s57, 147456\n" \
  "s_add_u32 s34, s65, 57344\n" \
  "buffer_load_dwordx4 v10, s[40:43], s34 offen sc1 lds\n" \
  "v_mfma_f32_16x16x32_bf16 v[216:219], v[244:247], v[96:99], v[216:219]\n" \
  "ds_read_b64_tr_b16 v[234:235], v14 offset:8704\n" \
  "v_mfma_f32_16x16x32_bf16 v[220:223], v[244:247], v[100:103], v[220:223]\n" \
  "ds_read_b64_tr_b16 v[236:237], v14 offset:16384\n" \
  "v_mfma_f32_16x16x32_bf16 v[220:223], v[240:243], v[92:95], v[220:223]\n" \
  "ds_read_b64_tr_b16 v[238:239], v14 offset:16896\n" \
  "s_waitcnt lgkmcnt(4)\n" \
  "v_mfma_f32_16x16x32_bf16 v[228:231], v[248:251], v[92:95], v[228:231]\n" \
  "ds_read_b64_tr_b16 v[240:241], v14 offset:12288\n" \
  "v_mfma_f32_16x16x32_bf16 v[228:231], v[252:255], v[100:103], v[228:231]\n" \
  "ds_read_b64_tr_b16 v[242:243], v14 offset:12800\n" \
  "s_add_u32 s34, s64, 8192\n" \
  "buffer_load_dwordx4 v[60:63], v10, s[40:43], s34 offen sc1\n" \
  "v_mfma_f32_16x16x32_bf16 v[224:227], v[252:255], v[96:99], v[224:227]\n" \
  "ds_read_b64_tr_b16 v[244:245], v14 offset:20480\n" \
  "v_mfma_f32_16x16x32_bf16 v[224:227], v[248:251], v[88:91], v[224:227]\n" \
  "ds_read_b64_tr_b16 v[246:247], v14 offset:20992\n" \
  "s_waitcnt lgkmcnt(4)\n" \
  "v_mfma_f32_16x16x32_bf16 v[168:171], v[232:235], v[88:91], v[168:171]\n" \
  "ds_read_b64_tr_b16 v[248:249], v15 offset:8192\n" \
  "v_mfma_f32_16x16x32_bf16 v[168:171], v[236:239], v[96:99], v[168:171]\n" \
  "ds_read_b64_tr_b16 v[250:251], v15 offset:8704\n" \
  "v_mfma_f32_16x16x32_bf16 v[172:175], v[236:239], v[100:103], v[172:175]\n" \
  "ds_read_b64_tr_b16 v[252:253], v15 offset:16384\n" \
  "s_add_u32 s34, s63, 10240\n" \
  "buffer_load_dwordx4 v[64:67], v10, s[40:43], s34 offen sc1\n" \
  "v_mfma_f32_16x16x32_bf16 v[172:175], v[232:235], v[92:95], v[172:175]\n" \
  "ds_read_b64_tr_b16 v[254:255], v15 offset:16896\n" \
  "s_waitcnt lgkmcnt(4)\n" \
  "v_mfma_f32_16x16x32_bf16 v[180:183], v[240:243], v[92:95], v[180:183]\n" \
  "ds_read_b64_tr_b16 v[232:233], v15 offset:12288\n" \
  "v_mfma_f32_16x16x32_bf16 v[180:183], v[244:247], v[100:103], v[180:183]\n" \
  "ds_read_b64_tr_b16 v[234:235], v15 offset:12800\n" \
  "v_mfma_f32_16x16x32_bf16 v[176:179], v[244:247], v[96:99], v[176:179]\n" \
  "ds_read_b64_tr_b16 v[236:237], v15 offset:20480\n" \
  "v_mfma_f32_16x16x32_bf16 v[176:179], v[240:243], v[88:91], v[176:179]\n" \
  "ds_read_b64_tr_b16 v[238:239], v15 offset:20992\n" \
  "s_add_u32 s34, s64, 10240\n" \
  "buffer_load_dwordx4 v[68:71], v10, s[40:43], s34 offen sc1\n" \
  "s_waitcnt lgkmcnt(4)\n" \
  "v_mfma_f32_16x16x32_bf16 v[184:187], v[248:251], v[88:91], v[184:187]\n" \
  "ds_read_b64_tr_b16 v[240:241], v12 offset:24576\n" \
  "v_mfma_f32_16x16x32_bf16 v[184:187], v[252:255], v[96:99], v[184:187]\n" \
  "ds_read_b64_tr_b16 v[242:243], v12 offset:25088\n" \
  "v_mfma_f32_16x16x32_bf16 v[188:191], v[252:255], v[100:103], v[188:191]\n" \
  "ds_read_b64_tr_b16 v[244:245], v12 offset:32768\n" \
  "v_mfma_f32_16x16x32_bf16 v[188:191], v[248:251], v[92:95], v[188:191]\n" \
  "ds_read_b64_tr_b16 v[246:247], v12 offset:33280\n" \
  "s_waitcnt lgkmcnt(4)\n" \
  "v_mfma_f32_16x16x32_bf16 v[196:199], v[232:235], v[92:95], v[196:199]\n" \
  "ds_read_b64_tr_b16 v[248:249], v12 offset:28672\n" \
  "s_add_u32 s34, s63, 12288\n" \
  "buffer_load_dwordx4 v[72:75], v10, s[40:43], s34 offen sc1\n" \
  "v_mfma_f32_16x16x32_bf16 v[196:199], v[236:239], v[100:103], v[196:199]\n" \
  "ds_read_b64_tr_b16 v[250:251], v12 offset:29184\n" \
  "v_mfma_f32_16x16x32_bf16 v[192:195], v[236:239], v[96:99], v[192:195]\n" \
  "ds_read_b64_tr_b16 v[252:253], v12 offset:36864\n" \
  "v_mfma_f32_16x16x32_bf16 v[192:195], v[232:235], v[88:91], v[192:195]\n" \
  "ds_read_b64_tr_b16 v[254:255], v12 offset:37376\n" \
  "s_waitcnt lgkmcnt(4)\n" \
  "v_mfma_f32_16x16x32_bf16 v[200:203], v[240:243], v[104:107], v[200:203]\n" \
  "ds_read_b64_tr_b16 v[232:233], v13 offset:24576\n" \
  "v_mfma_f32_16x16x32_bf16 v[200:203], v[244:247], v[112:115], v[200:203]\n" \
  "ds_read_b64_tr_b16 v[234:235], v13 offset:25088\n" \
  "s_add_u32 s34, s64, 12288\n" \
  "buffer_load_dwordx4 v[76:79], v10, s[40:43], s34 offen sc1\n" \
  "v_mfma_f32_16x16x32_bf16 v[204:207], v[244:247], v[116:119], v[204:207]\n" \
  "ds_read_b64_tr_b16 v[236:237], v13 offset:32768\n" \
  "v_mfma_f32_16x16x32_bf16 v[204:207], v[240:243], v[108:111], v[204:207]\n" \
  "ds_read_b64_tr_b16 v[238:239], v13 offset:33280\n" \
  "s_waitcnt lgkmcnt(4)\n" \
  "v_mfma_f32_16x16x32_bf16 v[212:215], v[248:251], v[108:111], v[212:215]\n" \
  "ds_read_b64_tr_b16 v[240:241], v13 offset:28672\n" \
  "s_waitcnt vmcnt(6)\n" \
  "s_mov_b32 s35, 0x40000\n" \
  ".Lpoll4_%=:\n" \
  "v_readfirstlane_b32 s34, v4\n" \
  "s_and_b32 s34, s34, 65280\n" \
  "s_cmp_eq_u32 s34, 1024\n" \
  "s_cbranch_scc1 .Lok4_%=\n" \
  "s_mov_b64 exec, 1\n" \
  "v_mov_b32 v4, 0\n" \
  "buffer_atomic_add v4, off, s[40:43], s67 sc0\n" \
  "s_mov_b64 exec, -1\n" \
  "s_sub_u32 s35, s35, 1\n" \
  "s_waitcnt vmcnt(0)\n" \
  "s_cbranch_scc0 .Lpoll4_%=\n" \
  ".Lok4_%=:\n" \
  "v_mfma_f32_16x16x32_bf16 v[212:215], v[252:255], v[116:119], v[212:215]\n" \
  "ds_read_b64_tr_b16 v[242:243], v13 offset:29184\n" \
  "v_mfma_f32_16x16x32_bf16 v[208:211], v[252:255], v[112:115], v[208:211]\n" \
  "ds_read_b64_tr_b16 v[244:245], v13 offset:36864\n" \
  "s_add_u32 s34, s63, 14336\n" \
  "buffer_load_dwordx4 v[80:83], v10, s[40:43], s34 offen sc1\n" \
  "v_mfma_f32_16x16x32_bf16 v[208:211], v[248:251], v[104:107], v[208:211]\n" \
  "ds_read_b64_tr_b16 v[246:247], v13 offset:37376\n" \
  "s_waitcnt lgkmcnt(4)\n" \
  "v_mfma_f32_16x16x32_bf16 v[216:219], v[232:235], v[104:107], v[216:219]\n" \
  "ds_read_b64_tr_b16 v[248:249], v14 offset:24576\n" \
  "v_mfma_f32_16x16x32_bf16 v[216:219], v[236:239], v[112:115], v[216:219]\n" \
  "ds_read_b64_tr_b16 v[250:251], v14 offset:25088\n" \
  "v_mfma_f32_16x16x32_bf16 v[220:223], v[236:239], v[116:119], v[220:223]\n" \
  "ds_read_b64_tr_b16 v[252:253], v14 offset:32768\n" \
  "v_mfma_f32_16x16x32_bf16 v[220:223], v[232:235], v[108:111], v[220:223]\n" \
  "ds_read_b64_tr_b16 v[254:255], v14 offset:33280\n" \
  "s_add_u32 s34, s64, 14336\n" \
  "buffer_load_dwordx4 v[84:87], v10, s[40:43], s34 offen sc1\n" \
  "s_waitcnt lgkmcnt(4)\n" \
  "v_mfma_f32_16x16x32_bf16 v[228:231], v[240:243], v[108:111], v[228:231]\n" \
  "ds_read_b64_tr_b16 v[232:233], v14 offset:28672\n" \
  "v_mfma_f32_16x16x32_bf16 v[228:231], v[244:247], v[116:119], v[228:231]\n" \
  "ds_read_b64_tr_b16 v[234:235], v14 offset:29184\n" \
  "v_mfma_f32_16x16x32_bf16 v[224:227], v[244:247], v[112:115], v[224:227]\n" \
  "ds_read_b64_tr_b16 v[236:237], v14 offset:36864\n" \
  "v_mfma_f32_16x16x32_bf16 v[224:227], v[240:243], v[104:107], v[224:227]\n" \
  "ds_read_b64_tr_b16 v[238:239], v14 offset:37376\n" \
  "s_waitcnt lgkmcnt(4)\n" \
  "v_mfma_f32_16x16x32_bf16 v[168:171], v[248:251], v[104:107], v[168:171]\n" \
  "ds_read_b64_tr_b16 v[240:241], v15 offset:24576\n" \
  "s_add_u32 s34, s63, 16384\n" \
  "buffer_load_dwordx2 v[88:89], v11, s[40:43], s34 offen sc1\n" \
  "v_mfma_f32_16x16x32_bf16 v[168:171], v[252:255], v[112:115], v[168:171]\n" \
  "ds_read_b64_tr_b16 v[242:243], v15 offset:25088\n" \
  "v_mfma_f32_16x16x32_bf16 v[172:175], v[252:255], v[116:119], v[172:175]\n" \
  "ds_read_b64_tr_b16 v[244:245], v15 offset:32768\n" \
  "s_add_u32 s34, s63, 16896\n" \
  "buffer_load_dwordx2 v[90:91], v11, s[40:43], s34 offen sc1\n" \
  "v_mfma_f32_16x16x32_bf16 v[172:175], v[248:251], v[108:111], v[172:175]\n" \
  "ds_read_b64_tr_b16 v[246:247], v15 offset:33280\n" \
  "s_waitcnt lgkmcnt(4)\n" \
  "v_mfma_f32_16x16x32_bf16 v[180:183], v[232:235], v[108:111], v[180:183]\n" \
  "ds_read_b64_tr_b16 v[248:249], v15 offset:28672\n" \
  "v_mfma_f32_16x16x32_bf16 v[180:183], v[236:239], v[116:119], v[180:183]\n" \
  "ds_read_b64_tr_b16 v[250:251], v15 offset:29184\n" \
  "s_add_u32 s34, s64, 16384\n" \
  "buffer_load_dwordx2 v[92:93], v11, s[40:43], s34 offen sc1\n" \
  "v_mfma_f32_16x16x32_bf16 v[176:179], v[236:239], v[112:115], v[176:179]\n" \
  "ds_read_b64_tr_b16 v[252:253], v15 offset:36864\n" \
  "v_mfma_f32_16x16x32_bf16 v[176:179], v[232:235], v[104:107], v[176:179]\n" \
  "ds_read_b64_tr_b16 v[254:255], v15 offset:37376\n" \
  "s_add_u32 s34, s64, 16896\n" \
  "buffer_load_dwordx2 v[94:95], v11, s[40:43], s34 offen sc1\n" \
  "s_waitcnt lgkmcnt(4)\n" \
  "v_mfma_f32_16x16x32_bf16 v[184:187], v[240:243], v[104:107], v[184:187]\n" \
  "s_barrier\n" \
  "ds_read_b64_tr_b16 v[232:233], v12 offset:40960\n" \
  "v_mfma_f32_16x16x32_bf16 v[184:187], v[244:247], v[112:115], v[184:187]\n" \
  "ds_read_b64_tr_b16 v[234:235], v12 offset:41472\n" \
  "v_mfma_f32_16x16x32_bf16 v[188:191], v[244:247], v[116:119], v[188:191]\n" \
  "ds_read_b64_tr_b16 v[236:237], v12 offset:49152\n" \
  "s_add_u32 m0, s57, 155648\n" \
  "s_add_u32 s34, s65, 65536\n" \
  "buffer_load_dwordx4 v10, s[40:43], s34 offen sc1 lds\n" \
  "v_mfma_f32_16x16x32_bf16 v[188:191], v[240:243], v[108:111], v[188:191]\n" \
  "ds_read_b64_tr_b16 v[238:239], v12 offset:49664\n" \
  "s_waitcnt lgkmcnt(4)\n" \
  "v_mfma_f32_16x16x32_bf16 v[196:199], v[248:251], v[108:111], v[196:199]\n" \
  "ds_read_b64_tr_b16 v[240:241], v12 offset:45056\n" \
  "v_mfma_f32_16x16x32_bf16 v[196:199], v[252:255], v[116:119], v[196:199]\n" \
  "ds_read_b64_tr_b16 v[242:243], v12 offset:45568\n" \
  "v_mfma_f32_16x16x32_bf16 v[192:195], v[252:255], v[112:115], v[192:195]\n" \
  "ds_read_b64_tr_b16 v[244:245], v12 offset:53248\n" \
  "v_mfma_f32_16x16x32_bf16 v[192:195], v[248:251], v[104:107], v[192:195]\n" \
  "ds_read_b64_tr_b16 v[246:247], v12 offset:53760\n" \
  "s_add_u32 m0, s57, 0\n" \
  "s_add_u32 s34, s65, 73728\n" \
  "buffer_load_dwordx4 v10, s[40:43], s34 offen sc1 lds\n" \
  "s_waitcnt lgkmcnt(4)\n" \
  "v_mfma_f32_16x16x32_bf16 v[200:203], v[232:235], v[120:123], v[200:203]\n" \
  "ds_read_b64_tr_b16 v[248:249], v13 offset:40960\n" \
  "v_mfma_f32_16x16x32_bf16 v[200:203], v[236:239], v[128:131], v[200:203]\n" \
  "ds_read_b64_tr_b16 v[250:251], v13 offset:41472\n" \
  "v_mfma_f32_16x16x32_bf16 v[204:207], v[236:239], v[132:135], v[204:207]\n" \
  "ds_read_b64_tr_b16 v[252:253], v13 offset:49152\n" \
  "v_mfma_f32_16x16x32_bf16 v[204:207], v[232:235], v[124:127], v[204:207]\n" \
  "ds_read_b64_tr_b16 v[254:255], v13 offset:49664\n" \
  "s_waitcnt lgkmcnt(4)\n" \
  "v_mfma_f32_16x16x32_bf16 v[212:215], v[240:243], v[124:127], v[212:215]\n" \
  "ds_read_b64_tr_b16 v[232:233], v13 offset:45056\n" \
  "s_add_u32 m0, s57, 8192\n" \
  "s_add_u32 s34, s65, 81920\n" \
  "buffer_load_dwordx4 v10, s[40:43], s34 offen sc1 lds\n" \
  "v_mfma_f32_16x16x32_bf16 v[212:215], v[244:247], v[132:135], v[212:215]\n" \
  "ds_read_b64_tr_b16 v[234:235], v13 offset:45568\n" \
  "v_mfma_f32_16x16x32_bf16 v[208:211], v[244:247], v[128:131], v[208:211]\n" \
  "ds_read_b64_tr_b16 v[236:237], v13 offset:53248\n" \
  "v_mfma_f32_16x16x32_bf16 v[208:211], v[240:243], v[120:123], v[208:211]\n" \
  "ds_read_b64_tr_b16 v[238:239], v13 offset:53760\n" \
  "s_waitcnt lgkmcnt(4)\n" \
  "v_mfma_f32_16x16x32_bf16 v[216:219], v[248:251], v[120:123], v[216:219]\n" \
  "ds_read_b64_tr_b16 v[240:241], v12 offset:57344\n" \
  "ds_read_b64_tr_b16 v[242:243], v12 offset:57856\n" \
  "v_mfma_f32_16x16x32_bf16 v[216:219], v[252:255], v[128:131], v[216:219]\n" \
  "v_add_u32 v12, 65536, v12\n" \
  "s_add_u32 m0, s57, 16384\n" \
  "s_add_u32 s34, s65, 90112\n" \
  "buffer_load_dwordx4 v10, s[40:43], s34 offen sc1 lds\n" \
  "v_mfma_f32_16x16x32_bf16 v[220:223], v[252:255], v[132:135], v[220:223]\n" \
  "ds_read_b64_tr_b16 v[244:245], v12\n" \
  "v_mfma_f32_16x16x32_bf16 v[220:223], v[248:251], v[124:127], v[220:223]\n" \
  "ds_read_b64_tr_b16 v[246:247], v12 offset:512\n" \
  "s_waitcnt lgkmcnt(4)\n" \
  "v_mfma_f32_16x16x32_bf16 v[228:231], v[232:235], v[124:127], v[228:231]\n" \
  "v_add_u32 v12, 4294959104, v12\n" \
  "ds_read_b64_tr_b16 v[248:249], v12 offset:4096\n" \
  "v_mfma_f32_16x16x32_bf16 v[228:231], v[236:239], v[132:135], v[228:231]\n" \
  "ds_read_b64_tr_b16 v[250:251], v12 offset:4608\n" \
  "v_mfma_f32_16x16x32_bf16 v[224:227], v[236:239], v[128:131], v[224:227]\n" \
  "ds_read_b64_tr_b16 v[252:253], v12 offset:12288\n" \
  "s_add_u32 s34, s63, 18432\n" \
  "buffer_load_dwordx2 v[96:97], v11, s[40:43], s34 offen sc1\n" \
  "v_mfma_f32_16x16x32_bf16 v[224:227], v[232:235], v[120:123], v[224:227]\n" \
  "ds_read_b64_tr_b16 v[254:255], v12 offset:12800\n" \
  "s_waitcnt lgkmcnt(4)\n" \
  "v_mfma_f32_16x16x32_bf16 v[200:203], v[240:243], v[136:139], v[200:203]\n" \
  "ds_read_b64_tr_b16 v[232:233], v13 offset:57344\n" \
  "ds_read_b64_tr_b16 v[234:235], v13 offset:57856\n" \
  "s_add_u32 s34, s63, 18944\n" \
  "buffer_load_dwordx2 v[98:99], v11, s[40:43], s34 offen sc1\n" \
  "v_mfma_f32_16x16x32_bf16 v[200:203], v[244:247], v[144:147], v[200:203]\n" \
  "v_add_u32 v13, 65536, v13\n" \
  "v_mfma_f32_16x16x32_bf16 v[204:207], v[244:247], v[148:151], v[204:207]\n" \
  "ds_read_b64_tr_b16 v[236:237], v13\n" \
  "v_mfma_f32_16x16x32_bf16 v[204:207], v[240:243], v[140:143], v[204:207]\n" \
  "ds_read_b64_tr_b16 v[238:239], v13 offset:512\n" \
  "s_add_u32 s34, s64, 18432\n" \
  "buffer_load_dwordx2 v[100:101], v11, s[40:43], s34 offen sc1\n" \
  "s_waitcnt lgkmcnt(4)\n" \
  "v_mfma_f32_16x16x32_bf16 v[212:215], v[248:251], v[140:143], v[212:215]\n" \
  "v_add_u32 v13, 4294959104, v13\n" \
  "ds_read_b64_tr_b16 v[240:241], v13 offset:4096\n" \
  "v_mfma_f32_16x16x32_bf16 v[212:215], v[252:255], v[148:151], v[212:215]\n" \
  "ds_read_b64_tr_b16 v[242:243], v13 offset:4608\n" \
  "s_add_u32 s34, s64, 18944\n" \
  "buffer_load_dwordx2 v[102:103], v11, s[40:43], s34 offen sc1\n" \
  "v_mfma_f32_16x16x32_bf16 v[208:211], v[252:255], v[144:147], v[208:211]\n" \
  "ds_read_b64_tr_b16 v[244:245], v13 offset:12288\n" \
  "v_mfma_f32_16x16x32_bf16 v[208:211], v[248:251], v[136:139], v[208:211]\n" \
  "ds_read_b64_tr_b16 v[246:247], v13 offset:12800\n" \
  "s_waitcnt lgkmcnt(4)\n" \
  "v_mfma_f32_16x16x32_bf16 v[216:219], v[232:235], v[136:139], v[216:219]\n" \
  "s_barrier\n" \
  "ds_read_b64_tr_b16 v[248:249], v12 offset:16384\n" \
  "s_add_u32 m0, s57, 24576\n" \
  "s_add_u32 s34, s65, 98304\n" \
  "buffer_load_dwordx4 v10, s[40:43], s34 offen sc1 lds\n" \
  "v_mfma_f32_16x16x32_bf16 v[216:219], v[236:239], v[144:147], v[216:219]\n" \
  "ds_read_b64_tr_b16 v[250:251], v12 offset:16896\n" \
  "v_mfma_f32_16x16x32_bf16 v[220:223], v[236:239], v[148:151], v[220:223]\n" \
  "ds_read_b64_tr_b16 v[252:253], v12 offset:24576\n" \
  "v_mfma_f32_16x16x32_bf16 v[220:223], v[232:235], v[140:143], v[220:223]\n" \
  "ds_read_b64_tr_b16 v[254:255], v12 offset:25088\n" \
  "s_waitcnt lgkmcnt(4)\n" \
  "v_mfma_f32_16x16x32_bf16 v[228:231], v[240:243], v[140:143], v[228:231]\n" \
  "ds_read_b64_tr_b16 v[232:233], v12 offset:20480\n" \
  "v_mfma_f32_16x16x32_bf16 v[228:231], v[244:247], v[148:151], v[228:231]\n" \
  "ds_read_b64_tr_b16 v[234:235], v12 offset:20992\n" \
  "s_add_u32 m0, s57, 32768\n" \
  "s_add_u32 s34, s65, 106496\n" \
  "buffer_load_dwordx4 v10, s[40:43], s34 offen sc1 lds\n" \
  "v_mfma_f32_16x16x32_bf16 v[224:227], v[244:247], v[144:147], v[224:227]\n" \
  "ds_read_b64_tr_b16 v[236:237], v12 offset:28672\n" \
  "v_mfma_f32_16x16x32_bf16 v[224:227], v[240:243], v[136:139], v[224:227]\n" \
  "ds_read_b64_tr_b16 v[238:239], v12 offset:29184\n" \
  "s_waitcnt lgkmcnt(4)\n" \
  "v_mfma_f32_16x16x32_bf16 v[200:203], v[248:251], v[152:155], v[200:203]\n" \
  "ds_read_b64_tr_b16 v[240:241], v13 offset:16384\n" \
  "v_mfma_f32_16x16x32_bf16 v[200:203], v[252:255], v[160:163], v[200:203]\n" \
  "ds_read_b64_tr_b16 v[242:243], v13 offset:16896\n" \
  "v_mfma_f32_16x16x32_bf16 v[204:207], v[252:255], v[164:167], v[204:207]\n" \
  "ds_read_b64_tr_b16 v[244:245], v13 offset:24576\n" \
  "v_mfma_f32_16x16x32_bf16 v[204:207], v[248:251], v[156:159], v[204:207]\n" \
  "ds_read_b64_tr_b16 v[246:247], v13 offset:25088\n" \
  "s_waitcnt lgkmcnt(4)\n" \
  "v_mfma_f32_16x16x32_bf16 v[212:215], v[232:235], v[156:159], v[212:215]\n" \
  "ds_read_b64_tr_b16 v[248:249], v13 offset:20480\n" \
  "v_mfma_f32_16x16x32_bf16 v[212:215], v[236:239], v[164:167], v[212:215]\n" \
  "ds_read_b64_tr_b16 v[250:251], v13 offset:20992\n" \
  "v_mfma_f32_16x16x32_bf16 v[208:211], v[236:239], v[160:163], v[208:211]\n" \
  "ds_read_b64_tr_b16 v[252:253], v13 offset:28672\n" \
  "v_mfma_f32_16x16x32_bf16 v[208:211], v[232:235], v[152:155], v[208:211]\n" \
  "ds_read_b64_tr_b16 v[254:255], v13 offset:29184\n" \
  "s_waitcnt lgkmcnt(4)\n" \
  "v_mfma_f32_16x16x32_bf16 v[216:219], v[240:243], v[152:155], v[216:219]\n" \
  "ds_read_b64_tr_b16 v[232:233], v14 offset:40960\n" \
  "v_mfma_f32_16x16x32_bf16 v[216:219], v[244:247], v[160:163], v[216:219]\n" \
  "ds_read_b64_tr_b16 v[234:235], v14 offset:41472\n" \
  "v_mfma_f32_16x16x32_bf16 v[220:223], v[244:247], v[164:167], v[220:223]\n" \
  "ds_read_b64_tr_b16 v[236:237], v14 offset:49152\n" \
  "v_mfma_f32_16x16x32_bf16 v[220:223], v[240:243], v[156:159], v[220:223]\n" \
  "ds_read_b64_tr_b16 v[238:239], v14 offset:49664\n" \
  "s_waitcnt lgkmcnt(4)\n" \
  "v_mfma_f32_16x16x32_bf16 v[228:231], v[248:251], v[156:159], v[228:231]\n" \
  "ds_read_b64_tr_b16 v[240:241], v14 offset:45056\n" \
  "v_mfma_f32_16x16x32_bf16 v[228:231], v[252:255], v[164:167], v[228:231]\n" \
  "ds_read_b64_tr_b16 v[242:243], v14 offset:45568\n" \
  "v_mfma_f32_16x16x32_bf16 v[224:227], v[252:255], v[160:163], v[224:227]\n" \
  "ds_read_b64_tr_b16 v[244:245], v14 offset:53248\n" \
  "v_mfma_f32_16x16x32_bf16 v[224:227], v[248:251], v[152:155], v[224:227]\n" \
  "ds_read_b64_tr_b16 v[246:247], v14 offset:53760\n" \
  "s_waitcnt lgkmcnt(4)\n" \
  "v_mfma_f32_16x16x32_bf16 v[168:171], v[232:235], v[120:123], v[168:171]\n" \
  "ds_read_b64_tr_b16 v[248:249], v15 offset:40960\n" \
  "v_mfma_f32_16x16x32_bf16 v[168:171], v[236:239], v[128:131], v[168:171]\n" \
  "ds_read_b64_tr_b16 v[250:251], v15 offset:41472\n" \
  "v_mfma_f32_16x16x32_bf16 v[172:175], v[236:239], v[132:135], v[172:175]\n" \
  "ds_read_b64_tr_b16 v[252:253], v15 offset:49152\n" \
  "v_mfma_f32_16x16x32_bf16 v[172:175], v[232:235], v[124:127], v[172:175]\n" \
  "ds_read_b64_tr_b16 v[254:255], v15 offset:49664\n" \
  "s_waitcnt lgkmcnt(4)\n" \
  "v_mfma_f32_16x16x32_bf16 v[180:183], v[240:243], v[124:127], v[180:183]\n" \
  "ds_read_b64_tr_b16 v[232:233], v15 offset:45056\n" \
  "s_nop 3\n" \
  "v_mul_f32 v200, v200, v208\n" \
  "v_mul_f32 v201, v201, v209\n" \
  "v_mul_f32 v202, v202, v210\n" \
  "v_mul_f32 v203, v203, v211\n" \
  "v_mfma_f32_16x16x32_bf16 v[180:183], v[244:247], v[132:135], v[180:183]\n" \
  "ds_read_b64_tr_b16 v[234:235], v15 offset:45568\n" \
  "v_mul_f32 v216, v216, v224\n" \
  "v_mul_f32 v217, v217, v225\n" \
  "v_mul_f32 v218, v218, v226\n" \
  "v_mul_f32 v219, v219, v227\n" \
  "v_cvt_pk_bf16_f32 v224, v200, v201\n" \
  "v_mfma_f32_16x16x32_bf16 v[176:179], v[244:247], v[128:131], v[176:179]\n" \
  "ds_read_b64_tr_b16 v[236:237], v15 offset:53248\n" \
  "v_cvt_pk_bf16_f32 v225, v202, v203\n" \
  "v_cvt_pk_bf16_f32 v226, v216, v217\n" \
  "v_cvt_pk_bf16_f32 v227, v218, v219\n" \
  "v_mul_f32 v204, v204, v212\n" \
  "s_add_u32 s34, s61, 20480\n" \
  "buffer_store_dwordx4 v[224:227], v10, s[40:43], s34 offen sc1\n" \
  "v_mfma_f32_16x16x32_bf16 v[176:179], v[240:243], v[120:123], v[176:179]\n" \
  "ds_read_b64_tr_b16 v[238:239], v15 offset:53760\n" \
  "v_mul_f32 v205, v205, v213\n" \
  "v_mul_f32 v206, v206, v214\n" \
  "v_mul_f32 v207, v207, v215\n" \
  "v_mul_f32 v220, v220, v228\n" \
  "v_mul_f32 v221, v221, v229\n" \
  "s_waitcnt lgkmcnt(4)\n" \
  "v_mfma_f32_16x16x32_bf16 v[184:187], v[248:251], v[120:123], v[184:187]\n" \
  "s_barrier\n" \
  "ds_read_b64_tr_b16 v[240:241], v14 offset:57344\n" \
  "ds_read_b64_tr_b16 v[242:243], v14 offset:57856\n" \
  "v_mul_f32 v222, v222, v230\n" \
  "v_mul_f32 v223, v223, v231\n" \
  "v_cvt_pk_bf16_f32 v228, v204, v205\n" \
  "v_cvt_pk_bf16_f32 v229, v206, v207\n" \
  "v_cvt_pk_bf16_f32 v230, v220, v221\n" \
  "v_mfma_f32_16x16x32_bf16 v[184:187], v[252:255], v[128:131], v[184:187]\n" \
  "v_add_u32 v14, 65536, v14\n" \
  "v_cvt_pk_bf16_f32 v231, v222, v223\n" \
  "v_mfma_f32_16x16x32_bf16 v[188:191], v[252:255], v[132:135], v[188:191]\n" \
  "ds_read_b64_tr_b16 v[244:245], v14\n" \
  "v_mfma_f32_16x16x32_bf16 v[188:191], v[248:251], v[124:127], v[188:191]\n" \
  "ds_read_b64_tr_b16 v[246:247], v14 offset:512\n" \
  "s_waitcnt lgkmcnt(4)\n" \
  "v_mfma_f32_16x16x32_bf16 v[196:199], v[232:235], v[124:127], v[196:199]\n" \
  "v_add_u32 v14, 4294959104, v14\n" \
  "ds_read_b64_tr_b16 v[248:249], v14 offset:4096\n" \
  "v_mfma_f32_16x16x32_bf16 v[196:199], v[236:239], v[132:135], v[196:199]\n" \
  "ds_read_b64_tr_b16 v[250:251], v14 offset:4608\n" \
  "v_mfma_f32_16x16x32_bf16 v[192:195], v[236:239], v[128:131], v[192:195]\n" \
  "ds_read_b64_tr_b16 v[252:253], v14 offset:12288\n" \
  "v_mfma_f32_16x16x32_bf16 v[192:195], v[232:235], v[120:123], v[192:195]\n" \
  "ds_read_b64_tr_b16 v[254:255], v14 offset:12800\n" \
  "s_waitcnt lgkmcnt(4)\n" \
  "v_mfma_f32_16x16x32_bf16 v[168:171], v[240:243], v[136:139], v[168:171]\n" \
  "ds_read_b64_tr_b16 v[232:233], v15 offset:57344\n" \
  "ds_read_b64_tr_b16 v[234:235], v15 offset:57856\n" \
  "v_mfma_f32_16x16x32_bf16 v[168:171], v[244:247], v[144:147], v[168:171]\n" \
  "v_add_u32 v15, 65536, v15\n" \
  "v_mfma_f32_16x16x32_bf16 v[172:175], v[244:247], v[148:151], v[172:175]\n" \
  "ds_read_b64_tr_b16 v[236:237], v15\n" \
  "v_mfma_f32_16x16x32_bf16 v[172:175], v[240:243], v[140:143], v[172:175]\n" \
  "ds_read_b64_tr_b16 v[238:239], v15 offset:512\n" \
  "s_add_u32 s34, s61, 21504\n" \
  "buffer_store_dwordx4 v[228:231], v10, s[40:43], s34 offen sc1\n" \
  "s_waitcnt lgkmcnt(4)\n" \
  "v_mfma_f32_16x16x32_bf16 v[180:183], v[248:251], v[140:143], v[180:183]\n" \
  "v_add_u32 v15, 4294959104, v15\n" \
  "ds_read_b64_tr_b16 v[240:241], v15 offset:4096\n" \
  "v_mfma_f32_16x16x32_bf16 v[180:183], v[252:255], v[148:151], v[180:183]\n" \
  "ds_read_b64_tr_b16 v[242:243], v15 offset:4608\n" \
  "v_mfma_f32_16x16x32_bf16 v[176:179], v[252:255], v[144:147], v[176:179]\n" \
  "ds_read_b64_tr_b16 v[244:245], v15 offset:12288\n" \
  "v_mfma_f32_16x16x32_bf16 v[176:179], v[248:251], v[136:139], v[176:179]\n" \
  "ds_read_b64_tr_b16 v[246:247], v15 offset:12800\n" \
  "s_waitcnt lgkmcnt(4)\n" \
  "v_mfma_f32_16x16x32_bf16 v[184:187], v[232:235], v[136:139], v[184:187]\n" \
  "s_barrier\n" \
  "ds_read_b64_tr_b16 v[248:249], v14 offset:16384\n" \
  "v_mfma_f32_16x16x32_bf16 v[184:187], v[236:239], v[144:147], v[184:187]\n" \
  "ds_read_b64_tr_b16 v[250:251], v14 offset:16896\n" \
  "v_mfma_f32_16x16x32_bf16 v[188:191], v[236:239], v[148:151], v[188:191]\n" \
  "ds_read_b64_tr_b16 v[252:253], v14 offset:24576\n" \
  "v_mfma_f32_16x16x32_bf16 v[188:191], v[232:235], v[140:143], v[188:191]\n" \
  "ds_read_b64_tr_b16 v[254:255], v14 offset:25088\n" \
  "s_waitcnt lgkmcnt(4)\n" \
  "v_mfma_f32_16x16x32_bf16 v[196:199], v[240:243], v[140:143], v[196:199]\n" \
  "ds_read_b64_tr_b16 v[232:233], v14 offset:20480\n" \
  "v_mfma_f32_16x16x32_bf16 v[196:199], v[244:247], v[148:151], v[196:199]\n" \
  "ds_read_b64_tr_b16 v[234:235], v14 offset:20992\n" \
  "v_mfma_f32_16x16x32_bf16 v[192:195], v[244:247], v[144:147], v[192:195]\n" \
  "ds_read_b64_tr_b16 v[236:237], v14 offset:28672\n" \
  "v_mfma_f32_16x16x32_bf16 v[192:195], v[240:243], v[136:139], v[192:195]\n" \
  "ds_read_b64_tr_b16 v[238:239], v14 offset:29184\n" \
  "s_waitcnt lgkmcnt(4)\n" \
  "v_mfma_f32_16x16x32_bf16 v[168:171], v[248:251], v[152:155], v[168:171]\n" \
  "ds_read_b64_tr_b16 v[240:241], v15 offset:16384\n" \
  "s_add_u32 m0, s57, 40960\n" \
  "s_add_u32 s34, s65, 114688\n" \
  "buffer_load_dwordx4 v10, s[40:43], s34 offen sc1 lds\n" \
  "v_mfma_f32_16x16x32_bf16 v[168:171], v[252:255], v[160:163], v[168:171]\n" \
  "ds_read_b64_tr_b16 v[242:243], v15 offset:16896\n" \
  "v_mfma_f32_16x16x32_bf16 v[172:175], v[252:255], v[164:167], v[172:175]\n" \
  "ds_read_b64_tr_b16 v[244:245], v15 offset:24576\n" \
  "v_mfma_f32_16x16x32_bf16 v[172:175], v[248:251], v[156:159], v[172:175]\n" \
  "ds_read_b64_tr_b16 v[246:247], v15 offset:25088\n" \
  "s_waitcnt lgkmcnt(4)\n" \
  "v_mfma_f32_16x16x32_bf16 v[180:183], v[232:235], v[156:159], v[180:183]\n" \
  "ds_read_b64_tr_b16 v[248:249], v15 offset:20480\n" \
  "v_mfma_f32_16x16x32_bf16 v[180:183], v[236:239], v[164:167], v[180:183]\n" \
  "ds_read_b64_tr_b16 v[250:251], v15 offset:20992\n" \
  "s_add_u32 m0, s57, 49152\n" \
  "s_add_u32 s34, s65, 122880\n" \
  "buffer_load_dwordx4 v10, s[40:43], s34 offen sc1 lds\n" \
  "v_mfma_f32_16x16x32_bf16 v[176:179], v[236:239], v[160:163], v[176:179]\n" \
  "ds_read_b64_tr_b16 v[252:253], v15 offset:28672\n" \
  "v_mfma_f32_16x16x32_bf16 v[176:179], v[232:235], v[152:155], v[176:179]\n" \
  "ds_read_b64_tr_b16 v[254:255], v15 offset:29184\n" \
  "s_waitcnt lgkmcnt(4)\n" \
  "v_mfma_f32_16x16x32_bf16 v[184:187], v[240:243], v[152:155], v[184:187]\n" \
  "s_barrier\n" \
  "v_add_u32 v17, 90112, v17\n" \
  "v_mfma_f32_16x16x32_bf16 v[184:187], v[244:247], v[160:163], v[184:187]\n" \
  "ds_read_b128 v[232:235], v17\n" \
  "v_mfma_f32_16x16x32_bf16 v[188:191], v[244:247], v[164:167], v[188:191]\n" \
  "ds_read_b128 v[236:239], v17 offset:1024\n" \
  "s_add_u32 m0, s57, 57344\n" \
  "s_add_u32 s34, s65, 131072\n" \
  "buffer_load_dwordx4 v10, s[40:43], s34 offen sc1 lds\n" \
  "v_mfma_f32_16x16x32_bf16 v[188:191], v[240:243], v[156:159], v[188:191]\n" \
  "s_waitcnt lgkmcnt(2)\n" \
  "v_mfma_f32_16x16x32_bf16 v[196:199], v[248:251], v[156:159], v[196:199]\n" \
  "ds_read_b128 v[240:243], v17 offset:2048\n" \
  "v_mfma_f32_16x16x32_bf16 v[196:199], v[252:255], v[164:167], v[196:199]\n" \
  "ds_read_b128 v[244:247], v17 offset:3072\n" \
  "v_mfma_f32_16x16x32_bf16 v[192:195], v[252:255], v[160:163], v[192:195]\n" \
  "v_mfma_f32_16x16x32_bf16 v[192:195], v[248:251], v[152:155], v[192:195]\n" \
  "s_add_u32 m0, s57, 65536\n" \
  "s_add_u32 s34, s65, 139264\n" \
  "buffer_load_dwordx4 v10, s[40:43], s34 offen sc1 lds\n" \
  "s_nop 1\n" \
  "s_waitcnt lgkmcnt(2)\n" \
  "v_mfma_f32_16x16x32_bf16 v[200:203], v[232:235], v[24:27], 0\n" \
  "ds_read_b128 v[248:251], v17 offset:8192\n" \
  "v_mfma_f32_16x16x32_bf16 v[200:203], v[236:239], v[32:35], v[200:203]\n" \
  "ds_read_b128 v[252:255], v17 offset:9216\n" \
  "v_mfma_f32_16x16x32_bf16 v[204:207], v[236:239], v[36:39], 0\n" \
  "v_mfma_f32_16x16x32_bf16 v[204:207], v[232:235], v[28:31], v[204:207]\n" \
  "s_waitcnt lgkmcnt(2)\n" \
  "v_mfma_f32_16x16x32_bf16 v[212:215], v[240:243], v[28:31], 0\n" \
  "ds_read_b128 v[232:235], v17 offset:10240\n" \
  "s_nop 3\n" \
  "v_mul_f32 v168, v168, v176\n" \
  "v_mul_f32 v169, v169, v177\n" \
  "v_mul_f32 v170, v170, v178\n" \
  "v_mul_f32 v171, v171, v179\n" \
  "v_mfma_f32_16x16x32_bf16 v[212:215], v[244:247], v[36:39], v[212:215]\n" \
  "ds_read_b128 v[236:239], v17 offset:11264\n" \
  "v_mul_f32 v184, v184, v192\n" \
  "v_mul_f32 v185, v185, v193\n" \
  "v_mul_f32 v186, v186, v194\n" \
  "v_mul_f32 v187, v187, v195\n" \
  "v_cvt_pk_bf16_f32 v192, v168, v169\n" \
  "v_mfma_f32_16x16x32_bf16 v[208:211], v[244:247], v[32:35], 0\n" \
  "v_cvt_pk_bf16_f32 v193, v170, v171\n" \
  "v_cvt_pk_bf16_f32 v194, v184, v185\n" \
  "v_cvt_pk_bf16_f32 v195, v186, v187\n" \
  "v_mul_f32 v172, v172, v180\n" \
  "s_add_u32 s34, s61, 28672\n" \
  "buffer_store_dwordx4 v[192:195], v10, s[40:43], s34 offen sc1\n" \
  "v_mfma_f32_16x16x32_bf16 v[208:211], v[240:243], v[24:27], v[208:211]\n" \
  "v_mul_f32 v173, v173, v181\n" \
  "v_mul_f32 v174, v174, v182\n" \
  "v_mul_f32 v175, v175, v183\n" \
  "v_mul_f32 v188, v188, v196\n" \
  "v_mul_f32 v189, v189, v197\n" \
  "s_waitcnt lgkmcnt(2)\n" \
  "v_mfma_f32_16x16x32_bf16 v[216:219], v[248:251], v[24:27], 0\n" \
  "ds_read_b128 v[240:243], v17 offset:16384\n" \
  "v_mul_f32 v190, v190, v198\n" \
  "v_mul_f32 v191, v191, v199\n" \
  "v_cvt_pk_bf16_f32 v196, v172, v173\n" \
  "v_cvt_pk_bf16_f32 v197, v174, v175\n" \
  "v_cvt_pk_bf16_f32 v198, v188, v189\n" \
  "v_mfma_f32_16x16x32_bf16 v[216:219], v[252:255], v[32:35], v[216:219]\n" \
  "ds_read_b128 v[244:247], v17 offset:17408\n" \
  "v_cvt_pk_bf16_f32 v199, v190, v191\n" \
  "s_waitcnt vmcnt(5)\n" \
  "s_mov_b64 exec, 1\n" \
  "v_mov_b32 v5, 65536\n" \
  "buffer_atomic_add v5, off, s[40:43], s66\n" \
  "s_mov_b64 exec, -1\n" \
  "v_mfma_f32_16x16x32_bf16 v[220:223], v[252:255], v[36:39], 0\n" \
  "v_mfma_f32_16x16x32_bf16 v[220:223], v[248:251], v[28:31], v[220:223]\n" \
  "s_nop 1\n" \
  "s_waitcnt lgkmcnt(2)\n" \
  "v_mfma_f32_16x16x32_bf16 v[228:231], v[232:235], v[28:31], 0\n" \
  "ds_read_b128 v[248:251], v17 offset:18432\n" \
  "v_mfma_f32_16x16x32_bf16 v[228:231], v[236:239], v[36:39], v[228:231]\n" \
  "ds_read_b128 v[252:255], v17 offset:19456\n" \
  "v_mfma_f32_16x16x32_bf16 v[224:227], v[236:239], v[32:35], 0\n" \
  "v_mfma_f32_16x16x32_bf16 v[224:227], v[232:235], v[24:27], v[224:227]\n" \
  "s_nop 1\n" \
  "s_waitcnt lgkmcnt(2)\n" \
  "v_mfma_f32_16x16x32_bf16 v[168:171], v[240:243], v[24:27], 0\n" \
  "ds_read_b128 v[232:235], v17 offset:24576\n" \
  "v_mfma_f32_16x16x32_bf16 v[168:171], v[244:247], v[32:35], v[168:171]\n" \
  "ds_read_b128 v[236:239], v17 offset:25600\n" \
  "v_mfma_f32_16x16x32_bf16 v[172:175], v[244:247], v[36:39], 0\n" \
  "v_mfma_f32_16x16x32_bf16 v[172:175], v[240:243], v[28:31], v[172:175]\n" \
  "s_add_u32 s34, s61, 29696\n" \
  "buffer_store_dwordx4 v[196:199], v10, s[40:43], s34 offen sc1\n" \
  "s_waitcnt lgkmcnt(2)\n" \
  "v_mfma_f32_16x16x32_bf16 v[180:183], v[248:251], v[28:31], 0\n" \
  "ds_read_b128 v[240:243], v17 offset:26624\n" \
  "v_mfma_f32_16x16x32_bf16 v[180:183], v[252:255], v[36:39], v[180:183]\n" \
  "ds_read_b128 v[244:247], v17 offset:27648\n" \
  "v_mfma_f32_16x16x32_bf16 v[176:179], v[252:255], v[32:35], 0\n" \
  "v_mfma_f32_16x16x32_bf16 v[176:179], v[248:251], v[24:27], v[176:179]\n" \
  "s_waitcnt lgkmcnt(2)\n" \
  "v_mfma_f32_16x16x32_bf16 v[184:187], v[232:235], v[24:27], 0\n" \
  "s_barrier\n" \
  "ds_read_b128 v[248:251], v17 offset:32768\n" \
  "v_mfma_f32_16x16x32_bf16 v[184:187], v[236:239], v[32:35], v[184:187]\n" \
  "ds_read_b128 v[252:255], v17 offset:33792\n" \
  "v_mfma_f32_16x16x32_bf16 v[188:191], v[236:239], v[36:39], 0\n" \
  "v_mfma_f32_16x16x32_bf16 v[188:191], v[232:235], v[28:31], v[188:191]\n" \
  "s_waitcnt lgkmcnt(2)\n" \
  "v_mfma_f32_16x16x32_bf16 v[204:207], v[240:243], v[44:47], v[204:207]\n" \
  "ds_read_b128 v[232:235], v17 offset:34816\n" \
  "v_mfma_f32_16x16x32_bf16 v[204:207], v[244:247], v[52:55], v[204:207]\n" \
  "ds_read_b128 v[236:239], v17 offset:35840\n" \
  "v_mfma_f32_16x16x32_bf16 v[200:203], v[244:247], v[48:51], v[200:203]\n" \
  "v_mfma_f32_16x16x32_bf16 v[200:203], v[240:243], v[40:43], v[200:203]\n" \
  "s_waitcnt lgkmcnt(2)\n" \
  "v_mfma_f32_16x16x32_bf16 v[208:211], v[248:251], v[40:43], v[208:211]\n" \
  "ds_read_b128 v[240:243], v17 offset:40960\n" \
  "s_mov_b64 exec, 1\n" \
  "v_mov_b32 v4, 0\n" \
  "buffer_atomic_add v4, off, s[40:43], s67 sc0\n" \
  "s_mov_b64 exec, -1\n" \
  "s_add_u32 m0, s57, 73728\n" \
  "s_add_u32 s34, s65, 147456\n" \
  "buffer_load_dwordx4 v10, s[40:43], s34 offen sc1 lds\n" \
  "v_mfma_f32_16x16x32_bf16 v[208:211], v[252:255], v[48:51], v[208:211]\n" \
  "ds_read_b128 v[244:247], v17 offset:41984\n" \
  "v_mfma_f32_16x16x32_bf16 v[212:215], v[252:255], v[52:55], v[212:215]\n" \
  "v_mfma_f32_16x16x32_bf16 v[212:215], v[248:251], v[44:47], v[212:215]\n" \
  "s_waitcnt lgkmcnt(2)\n" \
  "v_mfma_f32_16x16x32_bf16 v[220:223], v[232:235], v[44:47], v[220:223]\n" \
  "ds_read_b128 v[248:251], v17 offset:43008\n" \
  "v_mfma_f32_16x16x32_bf16 v[220:223], v[236:239], v[52:55], v[220:223]\n" \
  "ds_read_b128 v[252:255], v17 offset:44032\n" \
  "s_add_u32 m0, s57, 81920\n" \
  "s_add_u32 s34, s65, 155648\n" \
  "buffer_load_dwordx4 v10, s[40:43], s34 offen sc1 lds\n" \
  "v_mfma_f32_16x16x32_bf16 v[216:219], v[236:239], v[48:51], v[216:219]\n" \
  "v_mfma_f32_16x16x32_bf16 v[216:219], v[232:235], v[40:43], v[216:219]\n" \
  "s_waitcnt lgkmcnt(2)\n" \
  "v_mfma_f32_16x16x32_bf16 v[224:227], v[240:243], v[40:43], v[224:227]\n" \
  "ds_read_b128 v[232:235], v17 offset:49152\n" \
  "v_mfma_f32_16x16x32_bf16 v[224:227], v[244:247], v[48:51], v[224:227]\n" \
  "ds_read_b128 v[236:239], v17 offset:50176\n" \
  "v_mfma_f32_16x16x32_bf16 v[228:231], v[244:247], v[52:55], v[228:231]\n" \
  "s_add_u32 m0, s57, 90112\n" \
  "s_add_u32 s34, s65, 163840\n" \
  "buffer_load_dwordx4 v10, s[40:43], s34 offen sc1 lds\n" \
  "v_mfma_f32_16x16x32_bf16 v[228:231], v[240:243], v[44:47], v[228:231]\n" \
  "s_waitcnt lgkmcnt(2)\n" \
  "v_mfma_f32_16x16x32_bf16 v[172:175], v[248:251], v[44:47], v[172:175]\n" \
  "ds_read_b128 v[240:243], v17 offset:51200\n" \
  "v_mfma_f32_16x16x32_bf16 v[172:175], v[252:255], v[52:55], v[172:175]\n" \
  "ds_read_b128 v[244:247], v17 offset:52224\n" \
  "v_mfma_f32_16x16x32_bf16 v[168:171], v[252:255], v[48:51], v[168:171]\n" \
  "v_mfma_f32_16x16x32_bf16 v[168:171], v[248:251], v[40:43], v[168:171]\n" \
  "s_add_u32 m0, s57, 98304\n" \
  "s_add_u32 s34, s65, 172032\n" \
  "buffer_load_dwordx4 v10, s[40:43], s34 offen sc1 lds\n" \
  "s_waitcnt lgkmcnt(2)\n" \
  "v_mfma_f32_16x16x32_bf16 v[176:179], v[232:235], v[40:43], v[176:179]\n" \
  "ds_read_b128 v[248:251], v17 offset:57344\n" \
  "s_waitcnt vmcnt(4)\n" \
  "s_mov_b32 s35, 0x40000\n" \
  ".Lpoll5_%=:\n" \
  "v_readfirstlane_b32 s34, v4\n" \
  "s_and_b32 s34, s34, 16711680\n" \
  "s_cmp_eq_u32 s34, 262144\n" \
  "s_cbranch_scc1 .Lok5_%=\n" \
  "s_mov_b64 exec, 1\n" \
  "v_mov_b32 v4, 0\n" \
  "buffer_atomic_add v4, off, s[40:43], s67 sc0\n" \
  "s_mov_b64 exec, -1\n" \
  "s_sub_u32 s35, s35, 1\n" \
  "s_waitcnt vmcnt(0)\n" \
  "s_cbranch_scc0 .Lpoll5_%=\n" \
  ".Lok5_%=:\n" \
  "v_mfma_f32_16x16x32_bf16 v[176:179], v[236:239], v[48:51], v[176:179]\n" \
  "ds_read_b128 v[252:255], v17 offset:58368\n" \
  "v_mfma_f32_16x16x32_bf16 v[180:183], v[236:239], v[52:55], v[180:183]\n" \
  "v_mfma_f32_16x16x32_bf16 v[180:183], v[232:235], v[44:47], v[180:183]\n" \
  "s_waitcnt lgkmcnt(2)\n" \
  "v_mfma_f32_16x16x32_bf16 v[188:191], v[240:243], v[44:47], v[188:191]\n" \
  "ds_read_b128 v[232:235], v17 offset:59392\n" \
  "s_add_u32 s34, s63, 20480\n" \
  "buffer_load_dwordx4 v[104:107], v10, s[40:43], s34 offen sc1\n" \
  "v_mfma_f32_16x16x32_bf16 v[188:191], v[244:247], v[52:55], v[188:191]\n" \
  "ds_read_b128 v[236:239], v17 offset:60416\n" \
  "v_mfma_f32_16x16x32_bf16 v[184:187], v[244:247], v[48:51], v[184:187]\n" \
  "v_mfma_f32_16x16x32_bf16 v[184:187], v[240:243], v[40:43], v[184:187]\n" \
  "s_waitcnt lgkmcnt(2)\n" \
  "v_mfma_f32_16x16x32_bf16 v[200:203], v[248:251], v[56:59], v[200:203]\n" \
  "s_barrier\n" \
  "v_add_u32 v17, 65536, v17\n" \
  "v_mfma_f32_16x16x32_bf16 v[200:203], v[252:255], v[64:67], v[200:203]\n" \
  "ds_read_b128 v[240:243], v17\n" \
  "s_add_u32 s34, s64, 20480\n" \
  "buffer_load_dwordx4 v[108:111], v10, s[40:43], s34 offen sc1\n" \
  "v_mfma_f32_16x16x32_bf16 v[204:207], v[252:255], v[68:71], v[204:207]\n" \
  "ds_read_b128 v[244:247], v17 offset:1024\n" \
  "v_mfma_f32_16x16x32_bf16 v[204:207], v[248:251], v[60:63], v[204:207]\n" \
  "s_waitcnt lgkmcnt(2)\n" \
  "v_mfma_f32_16x16x32_bf16 v[212:215], v[232:235], v[60:63], v[212:215]\n" \
  "ds_read_b128 v[248:251], v17 offset:2048\n" \
  "s_mov_b64 exec, 1\n" \
  "v_mov_b32 v5, 16777216\n" \
  "buffer_atomic_add v5, off, s[40:43], s66\n" \
  "s_mov_b64 exec, -1\n" \
  "v_mfma_f32_16x16x32_bf16 v[212:215], v[236:239], v[68:71], v[212:215]\n" \
  "ds_read_b128 v[252:255], v17 offset:3072\n" \
  "v_mfma_f32_16x16x32_bf16 v[208:211], v[236:239], v[64:67], v[208:211]\n" \
  "s_add_u32 s34, s63, 22528\n" \
  "buffer_load_dwordx4 v[112:115], v10, s[40:43], s34 offen sc1\n" \
  "v_mfma_f32_16x16x32_bf16 v[208:211], v[232:235], v[56:59], v[208:211]\n" \
  "s_waitcnt lgkmcnt(2)\n" \
  "v_mfma_f32_16x16x32_bf16 v[216:219], v[240:243], v[56:59], v[216:219]\n" \
  "v_add_u32 v17, 4294811648, v17\n" \
  "v_mfma_f32_16x16x32_bf16 v[216:219], v[244:247], v[64:67], v[216:219]\n" \
  "ds_read_b128 v[232:235], v17\n" \
  "v_mfma_f32_16x16x32_bf16 v[220:223], v[244:247], v[68:71], v[220:223]\n" \
  "ds_read_b128 v[236:239], v17 offset:1024\n" \
  "v_mfma_f32_16x16x32_bf16 v[220:223], v[240:243], v[60:63], v[220:223]\n" \
  "s_add_u32 s34, s64, 22528\n" \
  "buffer_load_dwordx4 v[116:119], v10, s[40:43], s34 offen sc1\n" \
  "s_waitcnt lgkmcnt(2)\n" \
  "v_mfma_f32_16x16x32_bf16 v[228:231], v[248:251], v[60:63], v[228:231]\n" \
  "ds_read_b128 v[240:243], v17 offset:2048\n" \
  "v_mfma_f32_16x16x32_bf16 v[228:231], v[252:255], v[68:71], v[228:231]\n" \
  "ds_read_b128 v[244:247], v17 offset:3072\n" \
  "v_mfma_f32_16x16x32_bf16 v[224:227], v[252:255], v[64:67], v[224:227]\n" \
  "v_mfma_f32_16x16x32_bf16 v[224:227], v[248:251], v[56:59], v[224:227]\n" \
  "s_waitcnt lgkmcnt(2)\n" \
  "v_mfma_f32_16x16x32_bf16 v[168:171], v[232:235], v[56:59], v[168:171]\n" \
  "ds_read_b128 v[248:251], v17 offset:8192\n" \
  "s_add_u32 m0, s57, 106496\n" \
  "s_add_u32 s34, s65, 180224\n" \
  "buffer_load_dwordx4 v10, s[40:43], s34 offen sc1 lds\n" \
  "v_mfma_f32_16x16x32_bf16 v[168:171], v[236:239], v[64:67], v[168:171]\n" \
  "ds_read_b128 v[252:255], v17 offset:9216\n" \
  "v_mfma_f32_16x16x32_bf16 v[172:175], v[236:239], v[68:71], v[172:175]\n" \
  "v_mfma_f32_16x16x32_bf16 v[172:175], v[232:235], v[60:63], v[172:175]\n" \
  "s_waitcnt lgkmcnt(2)\n" \
  "v_mfma_f32_16x16x32_bf16 v[180:183], v[240:243], v[60:63], v[180:183]\n" \
  "ds_read_b128 v[232:235], v17 offset:10240\n" \
  "v_mfma_f32_16x16x32_bf16 v[180:183], v[244:247], v[68:71], v[180:183]\n" \
  "ds_read_b128 v[236:239], v17 offset:11264\n" \
  "s_add_u32 m0, s57, 114688\n" \
  "s_add_u32 s34, s65, 188416\n" \
  "buffer_load_dwordx4 v10, s[40:43], s34 offen sc1 lds\n" \
  "v_mfma_f32_16x16x32_bf16 v[176:179], v[244:247], v[64:67], v[176:179]\n" \
  "v_mfma_f32_16x16x32_bf16 v[176:179], v[240:243], v[56:59], v[176:179]\n" \
  "s_waitcnt lgkmcnt(2)\n" \
  "v_mfma_f32_16x16x32_bf16 v[184:187], v[248:251], v[56:59], v[184:187]\n" \
  "ds_read_b128 v[240:243], v17 offset:16384\n" \
  "v_mfma_f32_16x16x32_bf16 v[184:187], v[252:255], v[64:67], v[184:187]\n" \
  "ds_read_b128 v[244:247], v17 offset:17408\n" \
  "v_mfma_f32_16x16x32_bf16 v[188:191], v[252:255], v[68:71], v[188:191]\n" \
  "s_add_u32 s34, s63, 24576\n" \
  "buffer_load_dwordx4 v[120:123], v10, s[40:43], s34 offen sc1\n" \
  "v_mfma_f32_16x16x32_bf16 v[188:191], v[248:251], v[60:63], v[188:191]\n" \
  "s_waitcnt lgkmcnt(2)\n" \
  "v_mfma_f32_16x16x32_bf16 v[204:207], v[232:235], v[76:79], v[204:207]\n" \
  "ds_read_b128 v[248:251], v17 offset:18432\n" \
  "v_mfma_f32_16x16x32_bf16 v[204:207], v[236:239], v[84:87], v[204:207]\n" \
  "ds_read_b128 v[252:255], v17 offset:19456\n" \
  "v_mfma_f32_16x16x32_bf16 v[200:203], v[236:239], v[80:83], v[200:203]\n" \
  "v_mfma_f32_16x16x32_bf16 v[200:203], v[232:235], v[72:75], v[200:203]\n" \
  "s_add_u32 s34, s64, 24576\n" \
  "buffer_load_dwordx4 v[124:127], v10, s[40:43], s34 offen sc1\n" \
  "s_waitcnt lgkmcnt(2)\n" \
  "v_mfma_f32_16x16x32_bf16 v[208:211], v[240:243], v[72:75], v[208:211]\n" \
  "s_barrier\n" \
  "ds_read_b128 v[232:235], v17 offset:24576\n" \
  "s_mov_b64 exec, 1\n" \
  "v_mov_b32 v4, 0\n" \
  "buffer_atomic_add v4, off, s[40:43], s67 sc0\n" \
  "s_mov_b64 exec, -1\n" \
  "v_mfma_f32_16x16x32_bf16 v[208:211], v[244:247], v[80:83], v[208:211]\n" \
  "ds_read_b128 v[236:239], v17 offset:25600\n" \
  "v_mfma_f32_16x16x32_bf16 v[212:215], v[244:247], v[84:87], v[212:215]\n" \
  "v_mfma_f32_16x16x32_bf16 v[212:215], v[240:243], v[76:79], v[212:215]\n" \
  "s_waitcnt lgkmcnt(2)\n" \
  "v_mfma_f32_16x16x32_bf16 v[220:223], v[248:251], v[76:79], v[220:223]\n" \
  "ds_read_b128 v[240:243], v17 offset:26624\n" \
  "s_add_u32 s34, s63, 26624\n" \
  "buffer_load_dwordx4 v[128:131], v10, s[40:43], s34 offen sc1\n" \
  "v_mfma_f32_16x16x32_bf16 v[220:223], v[252:255], v[84:87], v[220:223]\n" \
  "ds_read_b128 v[244:247], v17 offset:27648\n" \
  "v_mfma_f32_16x16x32_bf16 v[216:219], v[252:255], v[80:83], v[216:219]\n" \
  "v_mfma_f32_16x16x32_bf16 v[216:219], v[248:251], v[72:75], v[216:219]\n" \
  "s_waitcnt lgkmcnt(2)\n" \
  "v_mfma_f32_16x16x32_bf16 v[224:227], v[232:235], v[72:75], v[224:227]\n" \
  "ds_read_b128 v[248:251], v17 offset:32768\n" \
  "v_mfma_f32_16x16x32_bf16 v[224:227], v[236:239], v[80:83], v[224:227]\n" \
  "ds_read_b128 v[252:255], v17 offset:33792\n" \
  "s_add_u32 s34, s64, 26624\n" \
  "buffer_load_dwordx4 v[132:135], v10, s[40:43], s34 offen sc1\n" \
  "v_mfma_f32_16x16x32_bf16 v[228:231], v[236:239], v[84:87], v[228:231]\n" \
  "v_mfma_f32_16x16x32_bf16 v[228:231], v[232:235], v[76:79], v[228:231]\n" \
  "s_waitcnt lgkmcnt(2)\n" \
  "v_mfma_f32_16x16x32_bf16 v[172:175], v[240:243], v[76:79], v[172:175]\n" \
  "ds_read_b128 v[232:235], v17 offset:34816\n" \
  "v_mfma_f32_16x16x32_bf16 v[172:175], v[244:247], v[84:87], v[172:175]\n" \
  "ds_read_b128 v[236:239], v17 offset:35840\n" \
  "v_mfma_f32_16x16x32_bf16 v[168:171], v[244:247], v[80:83], v[168:171]\n" \
  "s_add_u32 m0, s57, 122880\n" \
  "s_add_u32 s34, s65, 196608\n" \
  "buffer_load_dwordx4 v10, s[40:43], s34 offen sc1 lds\n" \
  "v_mfma_f32_16x16x32_bf16 v[168:171], v[240:243], v[72:75], v[168:171]\n" \
  "s_waitcnt lgkmcnt(2)\n" \
  "v_mfma_f32_16x16x32_bf16 v[176:179], v[248:251], v[72:75], v[176:179]\n" \
  "ds_read_b128 v[240:243], v17 offset:40960\n" \
  "s_waitcnt vmcnt(3)\n" \
  "s_mov_b32 s35, 0x40000\n" \
  ".Lpoll6_%=:\n" \
  "v_readfirstlane_b32 s34, v4\n" \
  "s_and_b32 s34, s34, 4278190080\n" \
  "s_cmp_eq_u32 s34, 67108864\n" \
  "s_cbranch_scc1 .Lok6_%=\n" \
  "s_mov_b64 exec, 1\n" \
  "v_mov_b32 v4, 0\n" \
  "buffer_atomic_add v4, off, s[40:43], s67 sc0\n" \
  "s_mov_b64 exec, -1\n" \
  "s_sub_u32 s35, s35, 1\n" \
  "s_waitcnt vmcnt(0)\n" \
  "s_cbranch_scc0 .Lpoll6_%=\n" \
  ".Lok6_%=:\n" \
  "v_mfma_f32_16x16x32_bf16 v[176:179], v[252:255], v[80:83], v[176:179]\n" \
  "ds_read_b128 v[244:247], v17 offset:41984\n" \
  "v_mfma_f32_16x16x32_bf16 v[180:183], v[252:255], v[84:87], v[180:183]\n" \
  "v_mfma_f32_16x16x32_bf16 v[180:183], v[248:251], v[76:79], v[180:183]\n" \
  "s_add_u32 s34, s63, 28672\n" \
  "buffer_load_dwordx4 v[136:139], v10, s[40:43], s34 offen sc1\n" \
  "s_waitcnt lgkmcnt(2)\n" \
  "v_mfma_f32_16x16x32_bf16 v[188:191], v[232:235], v[76:79], v[188:191]\n" \
  "ds_read_b128 v[248:251], v17 offset:43008\n" \
  "v_mfma_f32_16x16x32_bf16 v[188:191], v[236:239], v[84:87], v[188:191]\n" \
  "ds_read_b128 v[252:255], v17 offset:44032\n" \
  "v_mfma_f32_16x16x32_bf16 v[184:187], v[236:239], v[80:83], v[184:187]\n" \
  "v_mfma_f32_16x16x32_bf16 v[184:187], v[232:235], v[72:75], v[184:187]\n" \
  "s_waitcnt lgkmcnt(2)\n" \
  "v_mfma_f32_16x16x32_bf16 v[200:203], v[240:243], v[88:91], v[200:203]\n" \
  "ds_read_b128 v[232:235], v17 offset:49152\n" \
  "s_add_u32 s34, s64, 28672\n" \
  "buffer_load_dwordx4 v[140:143], v10, s[40:43], s34 offen sc1\n" \
  "v_mfma_f32_16x16x32_bf16 v[200:203], v[244:247], v[96:99], v[200:203]\n" \
  "ds_read_b128 v[236:239], v17 offset:50176\n" \
  "v_mfma_f32_16x16x32_bf16 v[204:207], v[244:247], v[100:103], v[204:207]\n" \
  "v_mfma_f32_16x16x32_bf16 v[204:207], v[240:243], v[92:95], v[204:207]\n" \
  "s_waitcnt lgkmcnt(2)\n" \
  "v_mfma_f32_16x16x32_bf16 v[212:215], v[248:251], v[92:95], v[212:215]\n" \
  "ds_read_b128 v[240:243], v17 offset:51200\n" \
  "v_mfma_f32_16x16x32_bf16 v[212:215], v[252:255], v[100:103], v[212:215]\n" \
  "ds_read_b128 v[244:247], v17 offset:52224\n" \
  "s_add_u32 s34, s63, 30720\n" \
  "buffer_load_dwordx4 v[144:147], v10, s[40:43], s34 offen sc1\n" \
  "v_mfma_f32_16x16x32_bf16 v[208:211], v[252:255], v[96:99], v[208:211]\n" \
  "v_mfma_f32_16x16x32_bf16 v[208:211], v[248:251], v[88:91], v[208:211]\n" \
  "s_waitcnt lgkmcnt(2)\n" \
  "v_mfma_f32_16x16x32_bf16 v[216:219], v[232:235], v[88:91], v[216:219]\n" \
  "s_barrier\n" \
  "ds_read_b128 v[248:251], v17 offset:57344\n" \
  "v_mfma_f32_16x16x32_bf16 v[216:219], v[236:239], v[96:99], v[216:219]\n" \
  "ds_read_b128 v[252:255], v17 offset:58368\n" \
  "v_mfma_f32_16x16x32_bf16 v[220:223], v[236:239], v[100:103], v[220:223]\n" \
  "s_add_u32 s34, s64, 30720\n" \
  "buffer_load_dwordx4 v[148:151], v10, s[40:43], s34 offen sc1\n" \
  "v_mfma_f32_16x16x32_bf16 v[220:223], v[232:235], v[92:95], v[220:223]\n" \
  "s_waitcnt lgkmcnt(2)\n" \
  "v_mfma_f32_16x16x32_bf16 v[228:231], v[240:243], v[92:95], v[228:231]\n" \
  "ds_read_b128 v[232:235], v17 offset:59392\n" \
  "v_mfma_f32_16x16x32_bf16 v[228:231], v[244:247], v[100:103], v[228:231]\n" \
  "ds_read_b128 v[236:239], v17 offset:60416\n" \
  "v_mfma_f32_16x16x32_bf16 v[224:227], v[244:247], v[96:99], v[224:227]\n" \
  "v_mfma_f32_16x16x32_bf16 v[224:227], v[240:243], v[88:91], v[224:227]\n" \
  "s_add_u32 m0, s57, 131072\n" \
  "s_add_u32 s34, s65, 204800\n" \
  "buffer_load_dwordx4 v10, s[40:43], s34 offen sc1 lds\n" \
  "s_waitcnt lgkmcnt(2)\n" \
  "v_mfma_f32_16x16x32_bf16 v[168:171], v[248:251], v[88:91], v[168:171]\n" \
  "v_add_u32 v17, 65536, v17\n" \
  "v_mfma_f32_16x16x32_bf16 v[168:171], v[252:255], v[96:99], v[168:171]\n" \
  "ds_read_b128 v[240:243], v17\n" \
  "v_mfma_f32_16x16x32_bf16 v[172:175], v[252:255], v[100:103], v[172:175]\n" \
  "ds_read_b128 v[244:247], v17 offset:1024\n" \
  "v_mfma_f32_16x16x32_bf16 v[172:175], v[248:251], v[92:95], v[172:175]\n" \
  "s_waitcnt lgkmcnt(2)\n" \
  "v_mfma_f32_16x16x32_bf16 v[180:183], v[232:235], v[92:95], v[180:183]\n" \
  "ds_read_b128 v[248:251], v17 offset:2048\n" \
  "s_add_u32 m0, s57, 139264\n" \
  "s_add_u32 s34, s65, 212992\n" \
  "buffer_load_dwordx4 v10, s[40:43], s34 offen sc1 lds\n" \
  "v_mfma_f32_16x16x32_bf16 v[180:183], v[236:239], v[100:103], v[180:183]\n" \
  "ds_read_b128 v[252:255], v17 offset:3072\n" \
  "v_mfma_f32_16x16x32_bf16 v[176:179], v[236:239], v[96:99], v[176:179]\n" \
  "v_mfma_f32_16x16x32_bf16 v[176:179], v[232:235], v[88:91], v[176:179]\n" \
  "s_waitcnt lgkmcnt(2)\n" \
  "v_mfma_f32_16x16x32_bf16 v[184:187], v[240:243], v[88:91], v[184:187]\n" \
  "ds_read_b128 v[232:235], v17 offset:8192\n" \
  "v_mfma_f32_16x16x32_bf16 v[184:187], v[244:247], v[96:99], v[184:187]\n" \
  "ds_read_b128 v[236:239], v17 offset:9216\n" \
  "s_add_u32 m0, s57, 147456\n" \
  "s_add_u32 s34, s65, 221184\n" \
  "buffer_load_dwordx4 v10, s[40:43], s34 offen sc1 lds\n" \
  "v_mfma_f32_16x16x32_bf16 v[188:191], v[244:247], v[100:103], v[188:191]\n" \
  "v_mfma_f32_16x16x32_bf16 v[188:191], v[240:243], v[92:95], v[188:191]\n" \
  "s_waitcnt lgkmcnt(2)\n" \
  "v_mfma_f32_16x16x32_bf16 v[204:207], v[248:251], v[108:111], v[204:207]\n" \
  "ds_read_b128 v[240:243], v17 offset:10240\n" \
  "v_mfma_f32_16x16x32_bf16 v[204:207], v[252:255], v[116:119], v[204:207]\n" \
  "ds_read_b128 v[244:247], v17 offset:11264\n" \
  "v_mfma_f32_16x16x32_bf16 v[200:203], v[252:255], v[112:115], v[200:203]\n" \
  "s_add_u32 s34, s63, 32768\n" \
  "buffer_load_dwordx4 v[152:155], v10, s[40:43], s34 offen sc1\n" \
  "v_mfma_f32_16x16x32_bf16 v[200:203], v[248:251], v[104:107], v[200:203]\n" \
  "s_waitcnt lgkmcnt(2)\n" \
  "v_mfma_f32_16x16x32_bf16 v[208:211], v[232:235], v[104:107], v[208:211]\n" \
  "ds_read_b128 v[248:251], v17 offset:16384\n" \
  "v_mfma_f32_16x16x32_bf16 v[208:211], v[236:239], v[112:115], v[208:211]\n" \
  "ds_read_b128 v[252:255], v17 offset:17408\n" \
  "v_mfma_f32_16x16x32_bf16 v[212:215], v[236:239], v[116:119], v[212:215]\n" \
  "v_mfma_f32_16x16x32_bf16 v[212:215], v[232:235], v[108:111], v[212:215]\n" \
  "s_add_u32 s34, s64, 32768\n" \
  "buffer_load_dwordx4 v[156:159], v10, s[40:43], s34 offen sc1\n" \
  "s_waitcnt lgkmcnt(2)\n" \
  "v_mfma_f32_16x16x32_bf16 v[220:223], v[240:243], v[108:111], v[220:223]\n" \
  "ds_read_b128 v[232:235], v17 offset:18432\n" \
  "v_mfma_f32_16x16x32_bf16 v[220:223], v[244:247], v[116:119], v[220:223]\n" \
  "ds_read_b128 v[236:239], v17 offset:19456\n" \
  "v_mfma_f32_16x16x32_bf16 v[216:219], v[244:247], v[112:115], v[216:219]\n" \
  "v_mfma_f32_16x16x32_bf16 v[216:219], v[240:243], v[104:107], v[216:219]\n" \
  "s_waitcnt lgkmcnt(2)\n" \
  "v_mfma_f32_16x16x32_bf16 v[224:227], v[248:251], v[104:107], v[224:227]\n" \
  "s_barrier\n" \
  "ds_read_b128 v[240:243], v17 offset:24576\n" \
  "s_add_u32 s34, s63, 34816\n" \
  "buffer_load_dwordx4 v[160:163], v10, s[40:43], s34 offen sc1\n" \
  "v_mfma_f32_16x16x32_bf16 v[224:227], v[252:255], v[112:115], v[224:227]\n" \
  "ds_read_b128 v[244:247], v17 offset:25600\n" \
  "v_mfma_f32_16x16x32_bf16 v[228:231], v[252:255], v[116:119], v[228:231]\n" \
  "v_mfma_f32_16x16x32_bf16 v[228:231], v[248:251], v[108:111], v[228:231]\n" \
  "s_waitcnt lgkmcnt(2)\n" \
  "v_mfma_f32_16x16x32_bf16 v[172:175], v[232:235], v[108:111], v[172:175]\n" \
  "ds_read_b128 v[248:251], v17 offset:26624\n" \
  "v_mfma_f32_16x16x32_bf16 v[172:175], v[236:239], v[116:119], v[172:175]\n" \
  "ds_read_b128 v[252:255], v17 offset:27648\n" \
  "s_add_u32 s34, s64, 34816\n" \
  "buffer_load_dwordx4 v[164:167], v10, s[40:43], s34 offen sc1\n" \
  "v_mfma_f32_16x16x32_bf16 v[168:171], v[236:239], v[112:115], v[168:171]\n" \
  "v_mfma_f32_16x16x32_bf16 v[168:171], v[232:235], v[104:107], v[168:171]\n" \
  "s_waitcnt vmcnt(12)\n" \
  "s_waitcnt lgkmcnt(2)\n" \
  "v_mfma_f32_16x16x32_bf16 v[200:203], v[240:243], v[120:123], v[200:203]\n" \
  "ds_read_b128 v[232:235], v17 offset:32768\n" \
  "v_mfma_f32_16x16x32_bf16 v[200:203], v[244:247], v[128:131], v[200:203]\n" \
  "ds_read_b128 v[236:239], v17 offset:33792\n" \
  "v_mfma_f32_16x16x32_bf16 v[204:207], v[244:247], v[132:135], v[204:207]\n" \
  "s_add_u32 m0, s57, 155648\n" \
  "s_add_u32 s34, s65, 229376\n" \
  "buffer_load_dwordx4 v10, s[40:43], s34 offen sc1 lds\n" \
  "v_mfma_f32_16x16x32_bf16 v[204:207], v[240:243], v[124:127], v[204:207]\n" \
  "s_waitcnt lgkmcnt(2)\n" \
  "v_mfma_f32_16x16x32_bf16 v[212:215], v[248:251], v[124:127], v[212:215]\n" \
  "ds_read_b128 v[240:243], v17 offset:34816\n" \
  "v_mfma_f32_16x16x32_bf16 v[212:215], v[252:255], v[132:135], v[212:215]\n" \
  "ds_read_b128 v[244:247], v17 offset:35840\n" \
  "v_mfma_f32_16x16x32_bf16 v[208:211], v[252:255], v[128:131], v[208:211]\n" \
  "v_mfma_f32_16x16x32_bf16 v[208:211], v[248:251], v[120:123], v[208:211]\n" \
  "s_add_u32 m0, s57, 0\n" \
  "s_add_u32 s34, s65, 237568\n" \
  "buffer_load_dwordx4 v10, s[40:43], s34 offen sc1 lds\n" \
  "s_waitcnt lgkmcnt(2)\n" \
  "v_mfma_f32_16x16x32_bf16 v[216:219], v[232:235], v[120:123], v[216:219]\n" \
  "ds_read_b128 v[248:251], v17 offset:40960\n" \
  "v_mfma_f32_16x16x32_bf16 v[216:219], v[236:239], v[128:131], v[216:219]\n" \
  "ds_read_b128 v[252:255], v17 offset:41984\n" \
  "v_mfma_f32_16x16x32_bf16 v[220:223], v[236:239], v[132:135], v[220:223]\n" \
  "v_mfma_f32_16x16x32_bf16 v[220:223], v[232:235], v[124:127], v[220:223]\n" \
  "s_waitcnt lgkmcnt(2)\n" \
  "v_mfma_f32_16x16x32_bf16 v[228:231], v[240:243], v[124:127], v[228:231]\n" \
  "ds_read_b128 v[232:235], v17 offset:43008\n" \
  "s_add_u32 m0, s57, 8192\n" \
  "s_add_u32 s34, s65, 245760\n" \
  "buffer_load_dwordx4 v10, s[40:43], s34 offen sc1 lds\n" \
  "v_mfma_f32_16x16x32_bf16 v[228:231], v[244:247], v[132:135], v[228:231]\n" \
  "ds_read_b128 v[236:239], v17 offset:44032\n" \
  "v_mfma_f32_16x16x32_bf16 v[224:227], v[244:247], v[128:131], v[224:227]\n" \
  "v_mfma_f32_16x16x32_bf16 v[224:227], v[240:243], v[120:123], v[224:227]\n" \
  "s_waitcnt lgkmcnt(2)\n" \
  "v_mfma_f32_16x16x32_bf16 v[168:171], v[248:251], v[120:123], v[168:171]\n" \
  "ds_read_b128 v[240:243], v17 offset:49152\n" \
  "v_mfma_f32_16x16x32_bf16 v[168:171], v[252:255], v[128:131], v[168:171]\n" \
  "ds_read_b128 v[244:247], v17 offset:50176\n" \
  "s_add_u32 m0, s57, 16384\n" \
  "s_add_u32 s34, s65, 253952\n" \
  "buffer_load_dwordx4 v10, s[40:43], s34 offen sc1 lds\n" \
  "v_mfma_f32_16x16x32_bf16 v[172:175], v[252:255], v[132:135], v[172:175]\n" \
  "v_mfma_f32_16x16x32_bf16 v[172:175], v[248:251], v[124:127], v[172:175]\n" \
  "s_waitcnt vmcnt(11)\n" \
  "s_waitcnt lgkmcnt(2)\n" \
  "v_mfma_f32_16x16x32_bf16 v[204:207], v[232:235], v[140:143], v[204:207]\n" \
  "ds_read_b128 v[248:251], v17 offset:51200\n" \
  "v_mfma_f32_16x16x32_bf16 v[204:207], v[236:239], v[148:151], v[204:207]\n" \
  "ds_read_b128 v[252:255], v17 offset:52224\n" \
  "v_mfma_f32_16x16x32_bf16 v[200:203], v[236:239], v[144:147], v[200:203]\n" \
  "s_add_u32 m0, s57, 24576\n" \
  "s_add_u32 s34, s65, 262144\n" \
  "buffer_load_dwordx4 v10, s[40:43], s34 offen sc1 lds\n" \
  "v_mfma_f32_16x16x32_bf16 v[200:203], v[232:235], v[136:139], v[200:203]\n" \
  "s_waitcnt lgkmcnt(2)\n" \
  "v_mfma_f32_16x16x32_bf16 v[208:211], v[240:243], v[136:139], v[208:211]\n" \
  "s_waitcnt vmcnt(9)\n" \
  "s_barrier\n" \
  "ds_read_b128 v[232:235], v17 offset:57344\n" \
  "v_mfma_f32_16x16x32_bf16 v[208:211], v[244:247], v[144:147], v[208:211]\n" \
  "ds_read_b128 v[236:239], v17 offset:58368\n" \
  "v_mfma_f32_16x16x32_bf16 v[212:215], v[244:247], v[148:151], v[212:215]\n" \
  "v_mfma_f32_16x16x32_bf16 v[212:215], v[240:243], v[140:143], v[212:215]\n" \
  "s_add_u32 m0, s57, 32768\n" \
  "s_add_u32 s34, s65, 270336\n" \
  "buffer_load_dwordx4 v10, s[40:43], s34 offen sc1 lds\n" \
  "s_waitcnt lgkmcnt(2)\n" \
  "v_mfma_f32_16x16x32_bf16 v[220:223], v[248:251], v[140:143], v[220:223]\n" \
  "ds_read_b128 v[240:243], v17 offset:59392\n" \
  "v_mfma_f32_16x16x32_bf16 v[220:223], v[252:255], v[148:151], v[220:223]\n" \
  "ds_read_b128 v[244:247], v17 offset:60416\n" \
  "v_mfma_f32_16x16x32_bf16 v[216:219], v[252:255], v[144:147], v[216:219]\n" \
  "v_mfma_f32_16x16x32_bf16 v[216:219], v[248:251], v[136:139], v[216:219]\n" \
  "s_waitcnt lgkmcnt(2)\n" \
  "v_mfma_f32_16x16x32_bf16 v[224:227], v[232:235], v[136:139], v[224:227]\n" \
  "v_add_u32 v17, 65536, v17\n" \
  "s_add_u32 m0, s57, 40960\n" \
  "s_add_u32 s34, s65, 278528\n" \
  "buffer_load_dwordx4 v10, s[40:43], s34 offen sc1 lds\n" \
  "v_mfma_f32_16x16x32_bf16 v[224:227], v[236:239], v[144:147], v[224:227]\n" \
  "ds_read_b128 v[248:251], v17\n" \
  "v_mfma_f32_16x16x32_bf16 v[228:231], v[236:239], v[148:151], v[228:231]\n" \
  "ds_read_b128 v[252:255], v17 offset:1024\n" \
  "v_mfma_f32_16x16x32_bf16 v[228:231], v[232:235], v[140:143], v[228:231]\n" \
  "s_waitcnt lgkmcnt(2)\n" \
  "v_mfma_f32_16x16x32_bf16 v[172:175], v[240:243], v[140:143], v[172:175]\n" \
  "ds_read_b128 v[232:235], v17 offset:2048\n" \
  "v_mfma_f32_16x16x32_bf16 v[172:175], v[244:247], v[148:151], v[172:175]\n" \
  "ds_read_b128 v[236:239], v17 offset:3072\n" \
  "s_add_u32 m0, s57, 49152\n" \
  "s_add_u32 s34, s65, 286720\n" \
  "buffer_load_dwordx4 v10, s[40:43], s34 offen sc1 lds\n" \
  "v_mfma_f32_16x16x32_bf16 v[168:171], v[244:247], v[144:147], v[168:171]\n" \
  "v_mfma_f32_16x16x32_bf16 v[168:171], v[240:243], v[136:139], v[168:171]\n" \
  "s_waitcnt vmcnt(8)\n" \
  "s_waitcnt lgkmcnt(2)\n" \
  "v_mfma_f32_16x16x32_bf16 v[200:203], v[248:251], v[152:155], v[200:203]\n" \
  "ds_read_b128 v[240:243], v17 offset:8192\n" \
  "v_mfma_f32_16x16x32_bf16 v[200:203], v[252:255], v[160:163], v[200:203]\n" \
  "ds_read_b128 v[244:247], v17 offset:9216\n" \
  "v_mfma_f32_16x16x32_bf16 v[204:207], v[252:255], v[164:167], v[204:207]\n" \
  "s_add_u32 m0, s57, 57344\n" \
  "s_add_u32 s34, s65, 294912\n" \
  "buffer_load_dwordx4 v10, s[40:43], s34 offen sc1 lds\n" \
  "v_mfma_f32_16x16x32_bf16 v[204:207], v[248:251], v[156:159], v[204:207]\n" \
  "s_waitcnt lgkmcnt(2)\n" \
  "v_mfma_f32_16x16x32_bf16 v[212:215], v[232:235], v[156:159], v[212:215]\n" \
  "ds_read_b128 v[248:251], v17 offset:10240\n" \
  "v_mfma_f32_16x16x32_bf16 v[212:215], v[236:239], v[164:167], v[212:215]\n" \
  "ds_read_b128 v[252:255], v17 offset:11264\n" \
  "v_mfma_f32_16x16x32_bf16 v[208:211], v[236:239], v[160:163], v[208:211]\n" \
  "v_mfma_f32_16x16x32_bf16 v[208:211], v[232:235], v[152:155], v[208:211]\n" \
  "s_add_u32 m0, s57, 65536\n" \
  "s_add_u32 s34, s65, 303104\n" \
  "buffer_load_dwordx4 v10, s[40:43], s34 offen sc1 lds\n" \
  "s_waitcnt lgkmcnt(2)\n" \
  "v_mfma_f32_16x16x32_bf16 v[216:219], v[240:243], v[152:155], v[216:219]\n" \
  "ds_read_b128 v[232:235], v17 offset:16384\n" \
  "v_mfma_f32_16x16x32_bf16 v[216:219], v[244:247], v[160:163], v[216:219]\n" \
  "ds_read_b128 v[236:239], v17 offset:17408\n" \
  "v_mfma_f32_16x16x32_bf16 v[220:223], v[244:247], v[164:167], v[220:223]\n" \
  "v_mfma_f32_16x16x32_bf16 v[220:223], v[240:243], v[156:159], v[220:223]\n" \
  "s_waitcnt lgkmcnt(2)\n" \
  "v_mfma_f32_16x16x32_bf16 v[228:231], v[248:251], v[156:159], v[228:231]\n" \
  "ds_read_b128 v[240:243], v17 offset:18432\n" \
  "s_add_u32 m0, s57, 73728\n" \
  "s_add_u32 s34, s65, 311296\n" \
  "buffer_load_dwordx4 v10, s[40:43], s34 offen sc1 lds\n" \
  "v_mfma_f32_16x16x32_bf16 v[228:231], v[252:255], v[164:167], v[228:231]\n" \
  "ds_read_b128 v[244:247], v17 offset:19456\n" \
  "v_mfma_f32_16x16x32_bf16 v[224:227], v[252:255], v[160:163], v[224:227]\n" \
  "v_mfma_f32_16x16x32_bf16 v[224:227], v[248:251], v[152:155], v[224:227]\n" \
  "s_waitcnt lgkmcnt(2)\n" \
  "v_mfma_f32_16x16x32_bf16 v[168:171], v[232:235], v[152:155], v[168:171]\n" \
  "s_waitcnt vmcnt(7)\n" \
  "s_barrier\n" \
  "ds_read_b128 v[248:251], v17 offset:24576\n" \
  "v_mfma_f32_16x16x32_bf16 v[168:171], v[236:239], v[160:163], v[168:171]\n" \
  "ds_read_b128 v[252:255], v17 offset:25600\n" \
  "s_add_u32 m0, s57, 81920\n" \
  "s_add_u32 s34, s65, 319488\n" \
  "buffer_load_dwordx4 v10, s[40:43], s34 offen sc1 lds\n" \
  "v_mfma_f32_16x16x32_bf16 v[172:175], v[236:239], v[164:167], v[172:175]\n" \
  "v_mfma_f32_16x16x32_bf16 v[172:175], v[232:235], v[156:159], v[172:175]\n" \
  "s_waitcnt lgkmcnt(2)\n" \
  "v_mfma_f32_16x16x32_bf16 v[180:183], v[240:243], v[108:111], v[180:183]\n" \
  "ds_read_b128 v[232:235], v17 offset:26624\n" \
  "v_mfma_f32_16x16x32_bf16 v[180:183], v[244:247], v[116:119], v[180:183]\n" \
  "ds_read_b128 v[236:239], v17 offset:27648\n" \
  "v_mfma_f32_16x16x32_bf16 v[176:179], v[244:247], v[112:115], v[176:179]\n" \
  "s_add_u32 m0, s57, 90112\n" \
  "s_add_u32 s34, s65, 327680\n" \
  "buffer_load_dwordx4 v10, s[40:43], s34 offen sc1 lds\n" \
  "v_mfma_f32_16x16x32_bf16 v[176:179], v[240:243], v[104:107], v[176:179]\n" \
  "s_waitcnt lgkmcnt(2)\n" \
  "v_mfma_f32_16x16x32_bf16 v[184:187], v[248:251], v[104:107], v[184:187]\n" \
  "v_add_u32 v17, 4294836224, v17\n" \
  "s_nop 3\n" \
  "ds_write_b128 v18, v[204:207]\n" \
  "v_mfma_f32_16x16x32_bf16 v[184:187], v[252:255], v[112:115], v[184:187]\n" \
  "ds_read_b128 v[240:243], v17\n" \
  "ds_write_b128 v18, v[212:215] offset:1024\n" \
  "v_mfma_f32_16x16x32_bf16 v[188:191], v[252:255], v[116:119], v[188:191]\n" \
  "ds_read_b128 v[244:247], v17 offset:1024\n" \
  "ds_write_b128 v18, v[220:223] offset:2048\n" \
  "v_mfma_f32_16x16x32_bf16 v[188:191], v[248:251], v[108:111], v[188:191]\n" \
  "ds_write_b128 v18, v[228:231] offset:3072\n" \
  "s_waitcnt lgkmcnt(6)\n" \
  "v_mfma_f32_16x16x32_bf16 v[180:183], v[232:235], v[124:127], v[180:183]\n" \
  "ds_read_b128 v[248:251], v17 offset:2048\n" \
  "ds_write_b128 v18, v[172:175] offset:4096\n" \
  "v_mfma_f32_16x16x32_bf16 v[180:183], v[236:239], v[132:135], v[180:183]\n" \
  "ds_read_b128 v[252:255], v17 offset:3072\n" \
  "v_mfma_f32_16x16x32_bf16 v[176:179], v[236:239], v[128:131], v[176:179]\n" \
  "v_mfma_f32_16x16x32_bf16 v[176:179], v[232:235], v[120:123], v[176:179]\n" \
  "s_waitcnt lgkmcnt(5)\n" \
  "v_mfma_f32_16x16x32_bf16 v[184:187], v[240:243], v[120:123], v[184:187]\n" \
  "ds_read_b128 v[232:235], v17 offset:8192\n" \
  "v_mfma_f32_16x16x32_bf16 v[184:187], v[244:247], v[128:131], v[184:187]\n" \
  "ds_read_b128 v[236:239], v17 offset:9216\n" \
  "v_mfma_f32_16x16x32_bf16 v[188:191], v[244:247], v[132:135], v[188:191]\n" \
  "v_mfma_f32_16x16x32_bf16 v[188:191], v[240:243], v[124:127], v[188:191]\n" \
  "s_waitcnt lgkmcnt(3)\n" \
  "s_barrier\n" \
  "s_waitcnt lgkmcnt(2)\n" \
  "v_mfma_f32_16x16x32_bf16 v[180:183], v[248:251], v[140:143], v[180:183]\n" \
  "ds_read_b128 v[240:243], v17 offset:10240\n" \
  "ds_read_b128 v[204:207], v19\n" \
  "ds_read_b128 v[212:215], v19 offset:1024\n" \
  "ds_read_b128 v[220:223], v19 offset:2048\n" \
  "ds_read_b128 v[228:231], v19 offset:3072\n" \
  "ds_read_b128 v[172:175], v19 offset:4096\n" \
  "v_mfma_f32_16x16x32_bf16 v[180:183], v[252:255], v[148:151], v[180:183]\n" \
  "ds_read_b128 v[244:247], v17 offset:11264\n" \
  "v_mfma_f32_16x16x32_bf16 v[176:179], v[252:255], v[144:147], v[176:179]\n" \
  "s_waitcnt lgkmcnt(5)\n" \
  "v_add_f32 v200, v200, v204\n" \
  "v_add_f32 v201, v201, v205\n" \
  "v_add_f32 v202, v202, v206\n" \
  "v_add_f32 v203, v203, v207\n" \
  "v_mfma_f32_16x16x32_bf16 v[176:179], v[248:251], v[136:139], v[176:179]\n" \
  "s_waitcnt lgkmcnt(4)\n" \
  "v_add_f32 v208, v208, v212\n" \
  "v_add_f32 v209, v209, v213\n" \
  "v_add_f32 v210, v210, v214\n" \
  "v_add_f32 v211, v211, v215\n" \
  "v_mfma_f32_16x16x32_bf16 v[184:187], v[232:235], v[136:139], v[184:187]\n" \
  "ds_read_b128 v[248:251], v17 offset:16384\n" \
  "v_cvt_pk_bf16_f32 v204, v200, v201\n" \
  "v_cvt_pk_bf16_f32 v205, v202, v203\n" \
  "v_cvt_pk_bf16_f32 v206, v208, v209\n" \
  "v_cvt_pk_bf16_f32 v207, v210, v211\n" \
  "buffer_store_dwordx4 v[204:207], v10, s[40:43], s68 offen sc1\n" \
  "v_mfma_f32_16x16x32_bf16 v[184:187], v[236:239], v[144:147], v[184:187]\n" \
  "ds_read_b128 v[252:255], v17 offset:17408\n" \
  "s_waitcnt lgkmcnt(5)\n" \
  "v_add_f32 v216, v216, v220\n" \
  "v_add_f32 v217, v217, v221\n" \
  "v_add_f32 v218, v218, v222\n" \
  "v_mfma_f32_16x16x32_bf16 v[188:191], v[236:239], v[148:151], v[188:191]\n" \
  "v_add_f32 v219, v219, v223\n" \
  "s_waitcnt lgkmcnt(4)\n" \
  "v_add_f32 v224, v224, v228\n" \
  "v_add_f32 v225, v225, v229\n" \
  "v_add_f32 v226, v226, v230\n" \
  "v_mfma_f32_16x16x32_bf16 v[188:191], v[232:235], v[140:143], v[188:191]\n" \
  "v_add_f32 v227, v227, v231\n" \
  "v_cvt_pk_bf16_f32 v220, v216, v217\n" \
  "v_cvt_pk_bf16_f32 v221, v218, v219\n" \
  "v_cvt_pk_bf16_f32 v222, v224, v225\n" \
  "v_cvt_pk_bf16_f32 v223, v226, v227\n" \
  "s_waitcnt lgkmcnt(2)\n" \
  "v_mfma_f32_16x16x32_bf16 v[180:183], v[240:243], v[156:159], v[180:183]\n" \
  "ds_read_b128 v[232:235], v17 offset:18432\n" \
  "buffer_store_dwordx4 v[220:223], v10, s[40:43], s69 offen offset:1024 sc1\n" \
  "v_add_f32 v168, v168, v172\n" \
  "v_add_f32 v169, v169, v173\n" \
  "v_mfma_f32_16x16x32_bf16 v[180:183], v[244:247], v[164:167], v[180:183]\n" \
  "ds_read_b128 v[236:239], v17 offset:19456\n" \
  "v_add_f32 v170, v170, v174\n" \
  "v_add_f32 v171, v171, v175\n" \
  "v_cvt_pk_bf16_f32 v172, v168, v169\n" \
  "v_cvt_pk_bf16_f32 v173, v170, v171\n" \
  "v_mfma_f32_16x16x32_bf16 v[176:179], v[244:247], v[160:163], v[176:179]\n" \
  "buffer_store_dwordx2 v[172:173], v11, s[40:43], s71 offen sc1\n" \
  "v_mfma_f32_16x16x32_bf16 v[176:179], v[240:243], v[152:155], v[176:179]\n" \
  "s_waitcnt lgkmcnt(2)\n" \
  "v_mfma_f32_16x16x32_bf16 v[184:187], v[248:251], v[152:155], v[184:187]\n" \
  "s_waitcnt vmcnt(8)\n" \
  "s_barrier\n" \
  "ds_read_b128 v[240:243], v17 offset:24576\n" \
  "v_mfma_f32_16x16x32_bf16 v[184:187], v[252:255], v[160:163], v[184:187]\n" \
  "ds_read_b128 v[244:247], v17 offset:25600\n" \
  "v_mfma_f32_16x16x32_bf16 v[188:191], v[252:255], v[164:167], v[188:191]\n" \
  "v_mfma_f32_16x16x32_bf16 v[188:191], v[248:251], v[156:159], v[188:191]\n" \
  "s_nop 7\n" \
  "ds_write_b128 v18, v[180:183]\n" \
  "ds_write_b128 v18, v[188:191] offset:1024\n" \
  "s_waitcnt lgkmcnt(0)\n" \
  "s_barrier\n" \
  "ds_read_b128 v[180:183], v19\n" \
  "ds_read_b128 v[188:191], v19 offset:1024\n" \
  "v_mfma_f32_16x16x32_bf16 v[196:199], v[232:235], v[28:31], 0\n" \
  "ds_read_b128 v[248:251], v17 offset:26624\n" \
  "s_waitcnt lgkmcnt(2)\n" \
  "v_add_f32 v176, v176, v180\n" \
  "v_add_f32 v177, v177, v181\n" \
  "v_add_f32 v178, v178, v182\n" \
  "v_add_f32 v179, v179, v183\n" \
  "v_mfma_f32_16x16x32_bf16 v[196:199], v[236:239], v[36:39], v[196:199]\n" \
  "ds_read_b128 v[252:255], v17 offset:27648\n" \
  "s_waitcnt lgkmcnt(2)\n" \
  "v_add_f32 v184, v184, v188\n" \
  "v_add_f32 v185, v185, v189\n" \
  "v_add_f32 v186, v186, v190\n" \
  "v_add_f32 v187, v187, v191\n" \
  "v_mfma_f32_16x16x32_bf16 v[192:195], v[236:239], v[32:35], 0\n" \
  "v_cvt_pk_bf16_f32 v180, v176, v177\n" \
  "v_cvt_pk_bf16_f32 v181, v178, v179\n" \
  "v_cvt_pk_bf16_f32 v182, v184, v185\n" \
  "v_cvt_pk_bf16_f32 v183, v186, v187\n" \
  "v_mfma_f32_16x16x32_bf16 v[192:195], v[232:235], v[24:27], v[192:195]\n" \
  "buffer_store_dwordx4 v[180:183], v10, s[40:43], s70 offen offset:2048 sc1\n" \
  "v_mfma_f32_16x16x32_bf16 v[0:3], v[240:243], v[24:27], 0\n" \
  "ds_read_b128 v[232:235], v17 offset:32768\n" \
  "v_mfma_f32_16x16x32_bf16 v[0:3], v[244:247], v[32:35], v[0:3]\n" \
  "ds_read_b128 v[236:239], v17 offset:33792\n" \
  "v_mfma_f32_16x16x32_bf16 v[12:15], v[244:247], v[36:39], 0\n" \
  "v_mfma_f32_16x16x32_bf16 v[12:15], v[240:243], v[28:31], v[12:15]\n" \
  "s_waitcnt lgkmcnt(2)\n" \
  "v_mfma_f32_16x16x32_bf16 v[196:199], v[248:251], v[44:47], v[196:199]\n" \
  "ds_read_b128 v[240:243], v17 offset:34816\n" \
  "v_mfma_f32_16x16x32_bf16 v[196:199], v[252:255], v[52:55], v[196:199]\n" \
  "ds_read_b128 v[244:247], v17 offset:35840\n" \
  "v_mfma_f32_16x16x32_bf16 v[192:195], v[252:255], v[48:51], v[192:195]\n" \
  "v_mfma_f32_16x16x32_bf16 v[192:195], v[248:251], v[40:43], v[192:195]\n" \
  "buffer_load_dwordx4 v[200:203], v10, s[40:43], s75 offen sc1\n" \
  "s_waitcnt lgkmcnt(2)\n" \
  "v_mfma_f32_16x16x32_bf16 v[0:3], v[232:235], v[40:43], v[0:3]\n" \
  "ds_read_b128 v[248:251], v17 offset:40960\n" \
  "v_mfma_f32_16x16x32_bf16 v[0:3], v[236:239], v[48:51], v[0:3]\n" \
  "ds_read_b128 v[252:255], v17 offset:41984\n" \
  "v_mfma_f32_16x16x32_bf16 v[12:15], v[236:239], v[52:55], v[12:15]\n" \
  "v_mfma_f32_16x16x32_bf16 v[12:15], v[232:235], v[44:47], v[12:15]\n" \
  "s_waitcnt lgkmcnt(2)\n" \
  "v_mfma_f32_16x16x32_bf16 v[196:199], v[240:243], v[60:63], v[196:199]\n" \
  "ds_read_b128 v[232:235], v17 offset:43008\n" \
  "buffer_load_dwordx4 v[204:207], v22, s[40:43], 0 offen sc1\n" \
  "v_mfma_f32_16x16x32_bf16 v[196:199], v[244:247], v[68:71], v[196:199]\n" \
  "ds_read_b128 v[236:239], v17 offset:44032\n" \
  "v_mfma_f32_16x16x32_bf16 v[192:195], v[244:247], v[64:67], v[192:195]\n" \
  "v_mfma_f32_16x16x32_bf16 v[192:195], v[240:243], v[56:59], v[192:195]\n" \
  "s_waitcnt lgkmcnt(2)\n" \
  "v_mfma_f32_16x16x32_bf16 v[0:3], v[248:251], v[56:59], v[0:3]\n" \
  "ds_read_b128 v[240:243], v17 offset:49152\n" \
  "v_mfma_f32_16x16x32_bf16 v[0:3], v[252:255], v[64:67], v[0:3]\n" \
  "ds_read_b128 v[244:247], v17 offset:50176\n" \
  "buffer_load_dwordx4 v[208:211], v22, s[40:43], 0 offen offset:16 sc1\n" \
  "v_mfma_f32_16x16x32_bf16 v[12:15], v[252:255], v[68:71], v[12:15]\n" \
  "v_mfma_f32_16x16x32_bf16 v[12:15], v[248:251], v[60:63], v[12:15]\n" \
  "s_waitcnt lgkmcnt(2)\n" \
  "v_mfma_f32_16x16x32_bf16 v[196:199], v[232:235], v[76:79], v[196:199]\n" \
  "ds_read_b128 v[248:251], v17 offset:51200\n" \
  "s_waitcnt vmcnt(4)\n" \
  "s_mov_b64 exec, 1\n" \
  "v_mov_b32 v5, 1\n" \
  "buffer_atomic_add v5, off, s[40:43], s78\n" \
  "v_mov_b32 v6, 1\n" \
  "buffer_atomic_add v6, off, s[40:43], s79\n" \
  "v_mov_b32 v7, 65536\n" \
  "buffer_atomic_add v7, off, s[40:43], s72\n" \
  "s_mov_b64 exec, -1\n" \
  "v_mfma_f32_16x16x32_bf16 v[196:199], v[236:239], v[84:87], v[196:199]\n" \
  "ds_read_b128 v[252:255], v17 offset:52224\n" \
  "v_mfma_f32_16x16x32_bf16 v[192:195], v[236:239], v[80:83], v[192:195]\n" \
  "buffer_load_dwordx2 v[230:231], v16, s[40:43], s77 offen sc1\n" \
  "v_mfma_f32_16x16x32_bf16 v[192:195], v[232:235], v[72:75], v[192:195]\n" \
  "s_waitcnt lgkmcnt(2)\n" \
  "v_mfma_f32_16x16x32_bf16 v[0:3], v[240:243], v[72:75], v[0:3]\n" \
  "s_barrier\n" \
  "ds_read_b128 v[232:235], v17 offset:57344\n" \
  "v_mfma_f32_16x16x32_bf16 v[0:3], v[244:247], v[80:83], v[0:3]\n" \
  "ds_read_b128 v[236:239], v17 offset:58368\n" \
  "buffer_load_dwordx4 v[172:175], v23, s[40:43], 0 offen sc1\n" \
  "v_mfma_f32_16x16x32_bf16 v[12:15], v[244:247], v[84:87], v[12:15]\n" \
  "v_mfma_f32_16x16x32_bf16 v[12:15], v[240:243], v[76:79], v[12:15]\n" \
  "s_waitcnt lgkmcnt(2)\n" \
  "v_mfma_f32_16x16x32_bf16 v[196:199], v[248:251], v[92:95], v[196:199]\n" \
  "ds_read_b128 v[240:243], v17 offset:59392\n" \
  "s_waitcnt vmcnt(8)\n" \
  "s_mov_b64 exec, 1\n" \
  "v_mov_b32 v5, 256\n" \
  "buffer_atomic_add v5, off, s[40:43], s59\n" \
  "s_mov_b64 exec, -1\n" \
  "v_mfma_f32_16x16x32_bf16 v[196:199], v[252:255], v[100:103], v[196:199]\n" \
  "ds_read_b128 v[244:247], v17 offset:60416\n" \
  "v_mfma_f32_16x16x32_bf16 v[192:195], v[252:255], v[96:99], v[192:195]\n" \
  "v_mfma_f32_16x16x32_bf16 v[192:195], v[248:251], v[88:91], v[192:195]\n" \
  "s_waitcnt lgkmcnt(2)\n" \
  "v_mfma_f32_16x16x32_bf16 v[0:3], v[232:235], v[88:91], v[0:3]\n" \
  "v_add_u32 v17, 65536, v17\n" \
  "s_mov_b64 exec, 1\n" \
  "v_mov_b32 v4, 0\n" \
  "buffer_atomic_add v4, off, s[40:43], s76 sc0\n" \
  "s_mov_b64 exec, -1\n" \
  "s_lshl_b32 s31, s74, 18\n" \
  "s_add_u32 s31, s31, 0x102\n" \
  "v_mfma_f32_16x16x32_bf16 v[0:3], v[236:239], v[96:99], v[0:3]\n" \
  "ds_read_b128 v[248:251], v17\n" \
  "v_mfma_f32_16x16x32_bf16 v[12:15], v[236:239], v[100:103], v[12:15]\n" \
  "ds_read_b128 v[252:255], v17 offset:1024\n" \
  "v_mfma_f32_16x16x32_bf16 v[12:15], v[232:235], v[92:95], v[12:15]\n" \
  "s_waitcnt lgkmcnt(2)\n" \
  "v_mfma_f32_16x16x32_bf16 v[196:199], v[240:243], v[108:111], v[196:199]\n" \
  "ds_read_b128 v[232:235], v17 offset:2048\n" \
  "v_mfma_f32_16x16x32_bf16 v[196:199], v[244:247], v[116:119], v[196:199]\n" \
  "ds_read_b128 v[236:239], v17 offset:3072\n" \
  "v_mfma_f32_16x16x32_bf16 v[192:195], v[244:247], v[112:115], v[192:195]\n" \
  "v_mfma_f32_16x16x32_bf16 v[192:195], v[240:243], v[104:107], v[192:195]\n" \
  "s_waitcnt lgkmcnt(2)\n" \
  "v_mfma_f32_16x16x32_bf16 v[0:3], v[248:251], v[104:107], v[0:3]\n" \
  "ds_read_b128 v[240:243], v17 offset:8192\n" \
  "v_mfma_f32_16x16x32_bf16 v[0:3], v[252:255], v[112:115], v[0:3]\n" \
  "ds_read_b128 v[244:247], v17 offset:9216\n" \
  "v_mfma_f32_16x16x32_bf16 v[12:15], v[252:255], v[116:119], v[12:15]\n" \
  "v_mfma_f32_16x16x32_bf16 v[12:15], v[248:251], v[108:111], v[12:15]\n" \
  "s_waitcnt lgkmcnt(2)\n" \
  "v_mfma_f32_16x16x32_bf16 v[196:199], v[232:235], v[124:127], v[196:199]\n" \
  "ds_read_b128 v[248:251], v17 offset:10240\n" \
  "v_mfma_f32_16x16x32_bf16 v[196:199], v[236:239], v[132:135], v[196:199]\n" \
  "ds_read_b128 v[252:255], v17 offset:11264\n" \
  "v_mfma_f32_16x16x32_bf16 v[192:195], v[236:239], v[128:131], v[192:195]\n" \
  "v_mfma_f32_16x16x32_bf16 v[192:195], v[232:235], v[120:123], v[192:195]\n" \
  "s_waitcnt lgkmcnt(2)\n" \
  "v_mfma_f32_16x16x32_bf16 v[0:3], v[240:243], v[120:123], v[0:3]\n" \
  "ds_read_b128 v[232:235], v17 offset:16384\n" \
  "v_mfma_f32_16x16x32_bf16 v[0:3], v[244:247], v[128:131], v[0:3]\n" \
  "ds_read_b128 v[236:239], v17 offset:17408\n" \
  "v_mfma_f32_16x16x32_bf16 v[12:15], v[244:247], v[132:135], v[12:15]\n" \
  "v_mfma_f32_16x16x32_bf16 v[12:15], v[240:243], v[124:127], v[12:15]\n" \
  "s_waitcnt lgkmcnt(2)\n" \
  "v_mfma_f32_16x16x32_bf16 v[196:199], v[248:251], v[140:143], v[196:199]\n" \
  "ds_read_b128 v[240:243], v17 offset:18432\n" \
  "s_waitcnt vmcnt(0)\n" \
  "v_readfirstlane_b32 s35, v4\n" \
  "s_sub_u32 s35, s35, s31\n" \
  "s_cmp_eq_u32 s35, 0\n" \
  "s_cbranch_scc0 .Lmiss_%=\n" \
  "buffer_load_dwordx4 v[212:215], v10, s[40:43], s73 offen sc1\n" \
  "buffer_load_dwordx4 v[216:219], v10, s[40:43], s73 offen offset:1024 sc1\n" \
  "buffer_load_dwordx4 v[220:223], v10, s[40:43], s73 offen offset:2048 sc1\n" \
  "s_cmp_eq_u32 s74, 0\n" \
  "s_cbranch_scc1 .Lhit_%=\n" \
  "s_add_u32 s34, s73, 3072\n" \
  "buffer_load_dwordx2 v[224:225], v11, s[40:43], s34 offen sc1\n" \
  "buffer_load_dwordx2 v[226:227], v11, s[40:43], s34 offen offset:512 sc1\n" \
  "buffer_load_dwordx2 v[228:229], v11, s[40:43], s34 offen offset:1024 sc1\n" \
  ".Lhit_%=:\n" \
  "s_branch .Lgo_%=\n" \
  ".Lmiss_%=:\n" \
  "s_mov_b64 exec, 1\n" \
  "v_mov_b32 v4, 0\n" \
  "buffer_atomic_add v4, off, s[40:43], s76 sc0\n" \
  "s_mov_b64 exec, -1\n" \
  ".Lgo_%=:\n" \
  "v_mfma_f32_16x16x32_bf16 v[196:199], v[252:255], v[148:151], v[196:199]\n" \
  "ds_read_b128 v[244:247], v17 offset:19456\n" \
  "v_mfma_f32_16x16x32_bf16 v[192:195], v[252:255], v[144:147], v[192:195]\n" \
  "v_mfma_f32_16x16x32_bf16 v[192:195], v[248:251], v[136:139], v[192:195]\n" \
  "s_waitcnt lgkmcnt(2)\n" \
  "v_mfma_f32_16x16x32_bf16 v[0:3], v[232:235], v[136:139], v[0:3]\n" \
  "s_barrier\n" \
  "ds_read_b128 v[248:251], v17 offset:24576\n" \
  "v_mfma_f32_16x16x32_bf16 v[0:3], v[236:239], v[144:147], v[0:3]\n" \
  "ds_read_b128 v[252:255], v17 offset:25600\n" \
  "v_mfma_f32_16x16x32_bf16 v[12:15], v[236:239], v[148:151], v[12:15]\n" \
  "v_mfma_f32_16x16x32_bf16 v[12:15], v[232:235], v[140:143], v[12:15]\n" \
  "s_waitcnt lgkmcnt(2)\n" \
  "v_mfma_f32_16x16x32_bf16 v[196:199], v[240:243], v[156:159], v[196:199]\n" \
  "v_mfma_f32_16x16x32_bf16 v[196:199], v[244:247], v[164:167], v[196:199]\n" \
  "v_mfma_f32_16x16x32_bf16 v[192:195], v[244:247], v[160:163], v[192:195]\n" \
  "v_mfma_f32_16x16x32_bf16 v[192:195], v[240:243], v[152:155], v[192:195]\n" \
  "s_waitcnt lgkmcnt(0)\n" \
  "v_mfma_f32_16x16x32_bf16 v[0:3], v[248:251], v[152:155], v[0:3]\n" \
  "v_mfma_f32_16x16x32_bf16 v[0:3], v[252:255], v[160:163], v[0:3]\n" \
  "v_mfma_f32_16x16x32_bf16 v[12:15], v[252:255], v[164:167], v[12:15]\n" \
  "v_mfma_f32_16x16x32_bf16 v[12:15], v[248:251], v[156:159], v[12:15]\n" \
  "s_nop 7\n" \
  "ds_write_b128 v18, v[196:199] offset:2048\n" \
  "ds_write_b128 v18, v[12:15] offset:3072\n" \
  "v_lshlrev_b32 v232, 16, v200\n" \
  "v_and_b32 v233, 0xffff0000, v200\n" \
  "v_lshlrev_b32 v234, 16, v201\n" \
  "v_and_b32 v235, 0xffff0000, v201\n" \
  "v_lshlrev_b32 v236, 16, v202\n" \
  "v_and_b32 v237, 0xffff0000, v202\n" \
  "v_lshlrev_b32 v238, 16, v203\n" \
  "v_and_b32 v239, 0xffff0000, v203\n" \
  "s_waitcnt lgkmcnt(0)\n" \
  "s_barrier\n" \
  "ds_read_b128 v[196:199], v19 offset:2048\n" \
  "ds_read_b128 v[12:15], v19 offset:3072\n" \
  "s_cmp_eq_u32 s35, 0\n" \
  "s_cbranch_scc1 .Lfast_%=\n" \
  "s_waitcnt vmcnt(0)\n" \
  "s_mov_b32 s35, 0x40000\n" \
  ".Lpoll7_%=:\n" \
  "v_readfirstlane_b32 s34, v4\n" \
  "s_cmp_eq_u32 s34, s31\n" \
  "s_cbranch_scc1 .Lok7_%=\n" \
  "s_mov_b64 exec, 1\n" \
  "v_mov_b32 v4, 0\n" \
  "buffer_atomic_add v4, off, s[40:43], s76 sc0\n" \
  "s_mov_b64 exec, -1\n" \
  "s_sub_u32 s35, s35, 1\n" \
  "s_waitcnt vmcnt(0)\n" \
  "s_cbranch_scc0 .Lpoll7_%=\n" \
  ".Lok7_%=:\n" \
  "buffer_load_dwordx4 v[212:215], v10, s[40:43], s73 offen sc1\n" \
  "buffer_load_dwordx4 v[216:219], v10, s[40:43], s73 offen offset:1024 sc1\n" \
  "buffer_load_dwordx4 v[220:223], v10, s[40:43], s73 offen offset:2048 sc1\n" \
  "s_cmp_eq_u32 s74, 0\n" \
  "s_cbranch_scc1 .Lhit2_%=\n" \
  "s_add_u32 s34, s73, 3072\n" \
  "buffer_load_dwordx2 v[224:225], v11, s[40:43], s34 offen sc1\n" \
  "buffer_load_dwordx2 v[226:227], v11, s[40:43], s34 offen offset:512 sc1\n" \
  "buffer_load_dwordx2 v[228:229], v11, s[40:43], s34 offen offset:1024 sc1\n" \
  ".Lhit2_%=:\n" \
  ".Lfast_%=:\n" \
  "s_waitcnt lgkmcnt(0)\n" \
  "v_pk_add_f32 v[192:193], v[192:193], v[196:197]\n" \
  "v_pk_add_f32 v[194:195], v[194:195], v[198:199]\n" \
  "v_pk_add_f32 v[0:1], v[0:1], v[12:13]\n" \
  "v_pk_add_f32 v[2:3], v[2:3], v[14:15]\n" \
  "v_pk_fma_f32 v[232:233], v[192:193], v[204:205], v[232:233]\n" \
  "v_pk_fma_f32 v[234:235], v[194:195], v[206:207], v[234:235]\n" \
  "v_pk_fma_f32 v[236:237], v[0:1], v[208:209], v[236:237]\n" \
  "v_pk_fma_f32 v[238:239], v[2:3], v[210:211], v[238:239]\n" \
  "s_waitcnt vmcnt(0)\n" \
  "v_lshlrev_b32 v240, 16, v212\n" \
  "v_and_b32 v241, 0xffff0000, v212\n" \
  "v_lshlrev_b32 v242, 16, v213\n" \
  "v_and_b32 v243, 0xffff0000, v213\n" \
  "v_lshlrev_b32 v244, 16, v214\n" \
  "v_and_b32 v245, 0xffff0000, v214\n" \
  "v_lshlrev_b32 v246, 16, v215\n" \
  "v_and_b32 v247, 0xffff0000, v215\n" \
  "v_lshlrev_b32 v248, 16, v216\n" \
  "v_and_b32 v249, 0xffff0000, v216\n" \
  "v_pk_add_f32 v[240:241], v[240:241], v[248:249]\n" \
  "v_lshlrev_b32 v250, 16, v217\n" \
  "v_and_b32 v251, 0xffff0000, v217\n" \
  "v_pk_add_f32 v[242:243], v[242:243], v[250:251]\n" \
  "v_lshlrev_b32 v248, 16, v218\n" \
  "v_and_b32 v249, 0xffff0000, v218\n" \
  "v_pk_add_f32 v[244:245], v[244:245], v[248:249]\n" \
  "v_lshlrev_b32 v250, 16, v219\n" \
  "v_and_b32 v251, 0xffff0000, v219\n" \
  "v_pk_add_f32 v[246:247], v[246:247], v[250:251]\n" \
  "v_lshlrev_b32 v248, 16, v220\n" \
  "v_and_b32 v249, 0xffff0000, v220\n" \
  "v_pk_add_f32 v[240:241], v[240:241], v[248:249]\n" \
  "v_lshlrev_b32 v250, 16, v221\n" \
  "v_and_b32 v251, 0xffff0000, v221\n" \
  "v_pk_add_f32 v[242:243], v[242:243], v[250:251]\n" \
  "v_lshlrev_b32 v248, 16, v222\n" \
  "v_and_b32 v249, 0xffff0000, v222\n" \
  "v_pk_add_f32 v[244:245], v[244:245], v[248:249]\n" \
  "v_lshlrev_b32 v250, 16, v223\n" \
  "v_and_b32 v251, 0xffff0000, v223\n" \
  "v_pk_add_f32 v[246:247], v[246:247], v[250:251]\n" \
  "v_pk_fma_f32 v[232:233], v[240:241], v[204:205], v[232:233]\n" \
  "v_pk_fma_f32 v[234:235], v[242:243], v[206:207], v[234:235]\n" \
  "v_pk_fma_f32 v[236:237], v[244:245], v[208:209], v[236:237]\n" \
  "v_pk_fma_f32 v[238:239], v[246:247], v[210:211], v[238:239]\n" \
  "v_cvt_pk_bf16_f32 v212, v232, v233\n" \
  "v_cvt_pk_bf16_f32 v213, v234, v235\n" \
  "v_cvt_pk_bf16_f32 v214, v236, v237\n" \
  "v_cvt_pk_bf16_f32 v215, v238, v239\n" \
  "buffer_store_dwordx4 v[212:215], v20, s[44:47], 0 offen\n" \
  "s_cmp_eq_u32 s74, 0\n" \
  "s_cbranch_scc1 .Lend_%=\n" \
  "v_lshlrev_b32 v248, 16, v224\n" \
  "v_and_b32 v249, 0xffff0000, v224\n" \
  "v_pk_add_f32 v[168:169], v[168:169], v[248:249]\n" \
  "v_lshlrev_b32 v250, 16, v225\n" \
  "v_and_b32 v251, 0xffff0000, v225\n" \
  "v_pk_add_f32 v[170:171], v[170:171], v[250:251]\n" \
  "v_lshlrev_b32 v248, 16, v226\n" \
  "v_and_b32 v249, 0xffff0000, v226\n" \
  "v_pk_add_f32 v[168:169], v[168:169], v[248:249]\n" \
  "v_lshlrev_b32 v250, 16, v227\n" \
  "v_and_b32 v251, 0xffff0000, v227\n" \
  "v_pk_add_f32 v[170:171], v[170:171], v[250:251]\n" \
  "v_lshlrev_b32 v248, 16, v228\n" \
  "v_and_b32 v249, 0xffff0000, v228\n" \
  "v_pk_add_f32 v[168:169], v[168:169], v[248:249]\n" \
  "v_lshlrev_b32 v250, 16, v229\n" \
  "v_and_b32 v251, 0xffff0000, v229\n" \
  "v_pk_add_f32 v[170:171], v[170:171], v[250:251]\n" \
  "v_lshlrev_b32 v248, 16, v230\n" \
  "v_and_b32 v249, 0xffff0000, v230\n" \
  "v_pk_fma_f32 v[248:249], v[168:169], v[172:173], v[248:249]\n" \
  "v_cvt_pk_bf16_f32 v224, v248, v249\n" \
  "v_lshlrev_b32 v248, 16, v231\n" \
  "v_and_b32 v249, 0xffff0000, v231\n" \
  "v_pk_fma_f32 v[248:249], v[170:171], v[174:175], v[248:249]\n" \
  "v_cvt_pk_bf16_f32 v225, v248, v249\n" \
  "buffer_store_dwordx2 v[224:225], v21, s[44:47], 0 offen\n" \
  ".Lend_%=:\n" \
  ""
#define SIDE_ASM \
  "s_endpgm\n" \
  ".LM_%=:\n" \
  "s_cmp_ge_i32 s24, 0\n" \
  "s_cbranch_scc1 .LMnf_%=\n" \
  "s_and_b32 s18, s3, 1\n" \
  "s_lshr_b32 s19, s3, 1\n" \
  "v_and_b32 v2, 15, v1\n" \
  "v_lshrrev_b32 v3, 4, v1\n" \
  "s_lshl_b32 s20, s18, 4\n" \
  "v_add_u32 v4, s20, v2\n" \
  "v_lshl_add_u32 v5, v3, 7, s6\n" \
  "v_mul_u32_u24 v5, v5, v4\n" \
  "v_cvt_f32_u32 v5, v5\n" \
  "v_mul_f32 v5, 0xba000000, v5\n" \
  "v_cvt_f32_u32 v6, v4\n" \
  "v_mul_f32 v6, 0xbc800000, v6\n" \
  "v_cmp_eq_u32 s[20:21], 0, v4\n" \
  "v_mov_b32 v152, v5\n" \
  "v_add_f32 v153, v152, v6\n" \
  "v_cos_f32 v154, v152\n" \
  "v_cos_f32 v155, v153\n" \
  "v_sin_f32 v152, v152\n" \
  "v_sin_f32 v153, v153\n" \
  "v_cvt_pk_bf16_f32 v136, v154, v155\n" \
  "v_cvt_pk_bf16_f32 v144, v152, v153\n" \
  "v_fmamk_f32 v152, v6, 0x40000000, v5\n" \
  "v_add_f32 v153, v152, v6\n" \
  "v_cos_f32 v154, v152\n" \
  "v_cos_f32 v155, v153\n" \
  "v_sin_f32 v152, v152\n" \
  "v_sin_f32 v153, v153\n" \
  "v_cvt_pk_bf16_f32 v137, v154, v155\n" \
  "v_cvt_pk_bf16_f32 v145, v152, v153\n" \
  "v_fmamk_f32 v152, v6, 0x41800000, v5\n" \
  "v_add_f32 v153, v152, v6\n" \
  "v_cos_f32 v154, v152\n" \
  "v_cos_f32 v155, v153\n" \
  "v_sin_f32 v152, v152\n" \
  "v_sin_f32 v153, v153\n" \
  "v_cvt_pk_bf16_f32 v138, v154, v155\n" \
  "v_cvt_pk_bf16_f32 v146, v152, v153\n" \
  "v_fmamk_f32 v152, v6, 0x41900000, v5\n" \
  "v_add_f32 v153, v152, v6\n" \
  "v_cos_f32 v154, v152\n" \
  "v_cos_f32 v155, v153\n" \
  "v_sin_f32 v152, v152\n" \
  "v_sin_f32 v153, v153\n" \
  "v_cvt_pk_bf16_f32 v139, v154, v155\n" \
  "v_cvt_pk_bf16_f32 v147, v152, v153\n" \
  "v_fmamk_f32 v152, v6, 0x42000000, v5\n" \
  "v_add_f32 v153, v152, v6\n" \
  "v_cos_f32 v154, v152\n" \
  "v_cos_f32 v155, v153\n" \
  "v_sin_f32 v152, v152\n" \
  "v_sin_f32 v153, v153\n" \
  "v_cvt_pk_bf16_f32 v140, v154, v155\n" \
  "v_cvt_pk_bf16_f32 v148, v152, v153\n" \
  "v_fmamk_f32 v152, v6, 0x42080000, v5\n" \
  "v_add_f32 v153, v152, v6\n" \
  "v_cos_f32 v154, v152\n" \
  "v_cos_f32 v155, v153\n" \
  "v_sin_f32 v152, v152\n" \
  "v_sin_f32 v153, v153\n" \
  "v_cvt_pk_bf16_f32 v141, v154, v155\n" \
  "v_cvt_pk_bf16_f32 v149, v152, v153\n" \
  "v_fmamk_f32 v152, v6, 0x42400000, v5\n" \
  "v_add_f32 v153, v152, v6\n" \
  "v_cos_f32 v154, v152\n" \
  "v_cos_f32 v155, v153\n" \
  "v_sin_f32 v152, v152\n" \
  "v_sin_f32 v153, v153\n" \
  "v_cvt_pk_bf16_f32 v142, v154, v155\n" \
  "v_cvt_pk_bf16_f32 v150, v152, v153\n" \
  "v_fmamk_f32 v152, v6, 0x42480000, v5\n" \
  "v_add_f32 v153, v152, v6\n" \
  "v_cos_f32 v154, v152\n" \
  "v_cos_f32 v155, v153\n" \
  "v_sin_f32 v152, v152\n" \
  "v_sin_f32 v153, v153\n" \
  "v_cvt_pk_bf16_f32 v143, v154, v155\n" \
  "v_cvt_pk_bf16_f32 v151, v152, v153\n" \
  "s_mov_b64 exec, s[20:21]\n" \
  "v_mov_b32 v144, 0xbf803f80\n" \
  "v_mov_b32 v145, 0xbf803f80\n" \
  "v_mov_b32 v146, 0xbf803f80\n" \
  "v_mov_b32 v147, 0xbf803f80\n" \
  "v_mov_b32 v148, 0xbf803f80\n" \
  "v_mov_b32 v149, 0xbf803f80\n" \
  "v_mov_b32 v150, 0xbf803f80\n" \
  "v_mov_b32 v151, 0xbf803f80\n" \
  "s_mov_b64 exec, -1\n" \
  "v_cvt_f32_u32 v4, s6\n" \
  "v_mul_f32 v4, 0xbc800000, v4\n" \
  "v_cos_f32 v164, v4\n" \
  "v_sin_f32 v165, v4\n" \
  "v_lshrrev_b32 v4, 2, v1\n" \
  "v_mul_u32_u24 v4, 288, v4\n" \
  "v_and_b32 v5, 3, v1\n" \
  "v_lshl_add_u32 v4, v5, 3, v4\n" \
  "s_and_b32 s20, s7, 1\n" \
  "s_lshl_b32 s20, s20, 6\n" \
  "s_mul_i32 s21, s19, 96\n" \
  "s_add_u32 s20, s20, s21\n" \
  "v_add_u32 v156, s20, v4\n" \
  "s_mul_i32 s20, s3, 3328\n" \
  "s_add_u32 s20, s20, 28672\n" \
  "v_mul_u32_u24 v4, 0xd0, v2\n" \
  "v_lshl_add_u32 v4, v3, 4, v4\n" \
  "v_add_u32 v157, s20, v4\n" \
  "v_add_u32 v4, 0, v1\n" \
  "v_mul_u32_u24 v5, 0xab, v4\n" \
  "v_lshrrev_b32 v5, 11, v5\n" \
  "v_add_u32 v6, v4, v5\n" \
  "v_lshl_add_u32 v158, v6, 4, s20\n" \
  "v_mul_u32_u24 v5, 73536, v5\n" \
  "v_lshl_add_u32 v161, v4, 4, v5\n" \
  "v_add_u32 v4, 64, v1\n" \
  "v_mul_u32_u24 v5, 0xab, v4\n" \
  "v_lshrrev_b32 v5, 11, v5\n" \
  "v_add_u32 v6, v4, v5\n" \
  "v_lshl_add_u32 v159, v6, 4, s20\n" \
  "v_mul_u32_u24 v5, 73536, v5\n" \
  "v_lshl_add_u32 v162, v4, 4, v5\n" \
  "v_add_u32 v4, 128, v1\n" \
  "v_mul_u32_u24 v5, 0xab, v4\n" \
  "v_lshrrev_b32 v5, 11, v5\n" \
  "v_add_u32 v6, v4, v5\n" \
  "v_lshl_add_u32 v160, v6, 4, s20\n" \
  "v_mul_u32_u24 v5, 73536, v5\n" \
  "v_lshl_add_u32 v163, v4, 4, v5\n" \
  "s_mul_i32 s44, s18, 1179648\n" \
  "s_mul_i32 s20, s6, 2304\n" \
  "s_add_u32 s44, s44, s20\n" \
  "s_mul_i32 s20, s7, 384\n" \
  "s_add_u32 s44, s44, s20\n" \
  "s_lshl_b32 s21, s21, 1\n" \
  "s_add_u32 s44, s44, s21\n" \
  "s_add_u32 s44, s44, 1048576\n" \
  "s_add_u32 s45, s44, 2359296\n" \
  ".LMnf_%=:\n" \
  "s_cmp_eq_u32 s3, 3\n" \
  "s_cbranch_scc1 .LMnfr_%=\n" \
  "v_and_b32 v2, 31, v1\n" \
  "v_lshrrev_b32 v3, 5, v1\n" \
  "v_lshlrev_b32 v5, 2, v3\n" \
  "v_cvt_f32_u32 v7, v5\n" \
  "s_mul_i32 s26, s3, 6\n" \
  "s_add_u32 s26, s26, s7\n" \
  "v_and_b32 v2, 31, v1\n" \
  "v_lshrrev_b32 v3, 5, v1\n" \
  "v_cvt_f32_u32 v4, v2\n" \
  "v_mul_f32 v4, 0x3d000000, v4\n" \
  "v_lshlrev_b32 v5, 2, v3\n" \
  "v_cvt_f32_u32 v7, v5\n" \
  "v_mul_f32 v5, v4, v7\n" \
  "v_mov_b32 v28, v5\n" \
  "v_add_f32 v29, v28, v4\n" \
  "v_cos_f32 v30, v28\n" \
  "v_cos_f32 v31, v29\n" \
  "v_sin_f32 v28, v28\n" \
  "v_sin_f32 v29, v29\n" \
  "v_cvt_pk_bf16_f32 v32, v30, v31\n" \
  "v_cvt_pk_bf16_f32 v40, v28, v29\n" \
  "v_fmamk_f32 v28, v4, 0x40000000, v5\n" \
  "v_add_f32 v29, v28, v4\n" \
  "v_cos_f32 v30, v28\n" \
  "v_cos_f32 v31, v29\n" \
  "v_sin_f32 v28, v28\n" \
  "v_sin_f32 v29, v29\n" \
  "v_cvt_pk_bf16_f32 v33, v30, v31\n" \
  "v_cvt_pk_bf16_f32 v41, v28, v29\n" \
  "v_fmamk_f32 v28, v4, 0x41000000, v5\n" \
  "v_add_f32 v29, v28, v4\n" \
  "v_cos_f32 v30, v28\n" \
  "v_cos_f32 v31, v29\n" \
  "v_sin_f32 v28, v28\n" \
  "v_sin_f32 v29, v29\n" \
  "v_cvt_pk_bf16_f32 v34, v30, v31\n" \
  "v_cvt_pk_bf16_f32 v42, v28, v29\n" \
  "v_fmamk_f32 v28, v4, 0x41200000, v5\n" \
  "v_add_f32 v29, v28, v4\n" \
  "v_cos_f32 v30, v28\n" \
  "v_cos_f32 v31, v29\n" \
  "v_sin_f32 v28, v28\n" \
  "v_sin_f32 v29, v29\n" \
  "v_cvt_pk_bf16_f32 v35, v30, v31\n" \
  "v_cvt_pk_bf16_f32 v43, v28, v29\n" \
  "v_fmamk_f32 v28, v4, 0x41800000, v5\n" \
  "v_add_f32 v29, v28, v4\n" \
  "v_cos_f32 v30, v28\n" \
  "v_cos_f32 v31, v29\n" \
  "v_sin_f32 v28, v28\n" \
  "v_sin_f32 v29, v29\n" \
  "v_cvt_pk_bf16_f32 v36, v30, v31\n" \
  "v_cvt_pk_bf16_f32 v44, v28, v29\n" \
  "v_fmamk_f32 v28, v4, 0x41900000, v5\n" \
  "v_add_f32 v29, v28, v4\n" \
  "v_cos_f32 v30, v28\n" \
  "v_cos_f32 v31, v29\n" \
  "v_sin_f32 v28, v28\n" \
  "v_sin_f32 v29, v29\n" \
  "v_cvt_pk_bf16_f32 v37, v30, v31\n" \
  "v_cvt_pk_bf16_f32 v45, v28, v29\n" \
  "v_fmamk_f32 v28, v4, 0x41c00000, v5\n" \
  "v_add_f32 v29, v28, v4\n" \
  "v_cos_f32 v30, v28\n" \
  "v_cos_f32 v31, v29\n" \
  "v_sin_f32 v28, v28\n" \
  "v_sin_f32 v29, v29\n" \
  "v_cvt_pk_bf16_f32 v38, v30, v31\n" \
  "v_cvt_pk_bf16_f32 v46, v28, v29\n" \
  "v_fmamk_f32 v28, v4, 0x41d00000, v5\n" \
  "v_add_f32 v29, v28, v4\n" \
  "v_cos_f32 v30, v28\n" \
  "v_cos_f32 v31, v29\n" \
  "v_sin_f32 v28, v28\n" \
  "v_sin_f32 v29, v29\n" \
  "v_cvt_pk_bf16_f32 v39, v30, v31\n" \
  "v_cvt_pk_bf16_f32 v47, v28, v29\n" \
  "v_xor_b32 v48, 0x80008000, v40\n" \
  "v_xor_b32 v49, 0x80008000, v41\n" \
  "v_xor_b32 v50, 0x80008000, v42\n" \
  "v_xor_b32 v51, 0x80008000, v43\n" \
  "v_xor_b32 v52, 0x80008000, v44\n" \
  "v_xor_b32 v53, 0x80008000, v45\n" \
  "v_xor_b32 v54, 0x80008000, v46\n" \
  "v_xor_b32 v55, 0x80008000, v47\n" \
  "v_and_b32 v2, 31, v1\n" \
  "v_lshrrev_b32 v3, 5, v1\n" \
  "v_lshlrev_b32 v5, 2, v3\n" \
  "v_cvt_f32_u32 v7, v5\n" \
  "s_mul_i32 s26, s3, 6\n" \
  "s_add_u32 s26, s26, s7\n" \
  "v_mul_u32_u24 v4, 80, v2\n" \
  "v_lshl_add_u32 v4, v3, 4, v4\n" \
  "s_mul_i32 s16, s3, 5120\n" \
  "s_add_u32 s16, s16, 5888\n" \
  "v_add_u32 v80, s16, v4\n" \
  "v_mul_u32_u24 v4, 0x250, v2\n" \
  "v_mul_u32_u24 v5, 0x90, v3\n" \
  "s_and_b32 s16, s6, 7\n" \
  "s_add_u32 s17, s16, s26\n" \
  "s_lshl_b32 s17, s17, 1\n" \
  "s_add_u32 s17, s17, 81920\n" \
  "v_add3_u32 v81, v4, v5, s17\n" \
  "s_sub_u32 s16, 0, s6\n" \
  "s_and_b32 s16, s16, 7\n" \
  "s_sub_u32 s17, s16, s26\n" \
  "s_lshl_b32 s17, s17, 1\n" \
  "s_add_u32 s17, s17, 101044\n" \
  "v_sub_u32 v4, v4, v5\n" \
  "v_add_u32 v86, s17, v4\n" \
  "s_mov_b32 s47, s26\n" \
  "v_mul_u32_u24 v4, 0x90, v2\n" \
  "v_lshl_add_u32 v4, v3, 4, v4\n" \
  "s_mul_i32 s16, s3, 4608\n" \
  "s_add_u32 s16, s16, 37632\n" \
  "s_and_b32 s17, s6, 1\n" \
  "s_lshl_b32 s17, s17, 5\n" \
  "s_add_u32 s18, s16, s17\n" \
  "v_add_u32 v82, s18, v4\n" \
  "s_xor_b32 s17, s17, 32\n" \
  "s_add_u32 s18, s16, s17\n" \
  "v_add_u32 v83, s18, v4\n" \
  "v_lshrrev_b32 v4, 3, v1\n" \
  "v_and_b32 v5, 0x70, v10\n" \
  "v_mul_u32_u24 v6, 0x90, v4\n" \
  "v_add3_u32 v84, v6, v5, s16\n" \
  "v_mul_u32_u24 v6, 76800, v4\n" \
  "v_add_u32 v85, v6, v5\n" \
  "s_add_u32 s16, s26, 10\n" \
  "s_sub_u32 s17, s26, 8\n" \
  "s_cmp_lt_u32 s26, 9\n" \
  "s_cselect_b32 s16, s16, s17\n" \
  "s_bitcmp1_b32 s26, 0\n" \
  "s_cselect_b32 s16, s16, s26\n" \
  "s_bfe_u32 s17, s6, 0x10002\n" \
  "s_xor_b32 s16, s16, s17\n" \
  "s_lshr_b32 s17, s16, 1\n" \
  "s_mul_i32 s17, s17, 8192\n" \
  "s_lshl_b32 s16, s16, 7\n" \
  "s_add_u32 s16, s16, s17\n" \
  "s_lshl_b32 s46, s6, 8\n" \
  "s_add_u32 s46, s46, s16\n" \
  "s_add_u32 s46, s46, 7340032\n" \
  "v_mov_b32 v176, 0\n" \
  "v_mov_b32 v177, 0\n" \
  "v_mov_b32 v178, 0\n" \
  "v_mov_b32 v179, 0\n" \
  "v_mov_b32 v180, 0\n" \
  "v_mov_b32 v181, 0\n" \
  "v_mov_b32 v182, 0\n" \
  "v_mov_b32 v183, 0\n" \
  "v_mov_b32 v184, 0\n" \
  "v_mov_b32 v185, 0\n" \
  "v_mov_b32 v186, 0\n" \
  "v_mov_b32 v187, 0\n" \
  "v_mov_b32 v188, 0\n" \
  "v_mov_b32 v189, 0\n" \
  "v_mov_b32 v190, 0\n" \
  "v_mov_b32 v191, 0\n" \
  "v_mov_b32 v192, 0\n" \
  "v_mov_b32 v193, 0\n" \
  "v_mov_b32 v194, 0\n" \
  "v_mov_b32 v195, 0\n" \
  "v_mov_b32 v196, 0\n" \
  "v_mov_b32 v197, 0\n" \
  "v_mov_b32 v198, 0\n" \
  "v_mov_b32 v199, 0\n" \
  "v_mov_b32 v200, 0\n" \
  "v_mov_b32 v201, 0\n" \
  "v_mov_b32 v202, 0\n" \
  "v_mov_b32 v203, 0\n" \
  "v_mov_b32 v204, 0\n" \
  "v_mov_b32 v205, 0\n" \
  "v_mov_b32 v206, 0\n" \
  "v_mov_b32 v207, 0\n" \
  "v_mov_b32 v208, 0\n" \
  "v_mov_b32 v209, 0\n" \
  "v_mov_b32 v210, 0\n" \
  "v_mov_b32 v211, 0\n" \
  ".LMnfr_%=:\n" \
  "s_waitcnt lgkmcnt(0)\n" \
  "s_mov_b32 s40, s96\n" \
  "s_and_b32 s41, s97, 0xffff\n" \
  "s_mov_b32 s42, 134217728\n" \
  "s_mov_b32 s43, 0x20000\n" \
  "s_cmp_gt_u32 s6, 32\n" \
  "s_cbranch_scc1 .LMnw_%=\n" \
  "s_mov_b32 s8, s82\n" \
  "s_and_b32 s9, s83, 0xffff\n" \
  "s_mov_b32 s10, 1183744\n" \
  "s_mov_b32 s11, 0x20000\n" \
  "s_mov_b32 s12, s84\n" \
  "s_and_b32 s13, s85, 0xffff\n" \
  "s_mov_b32 s14, 1183744\n" \
  "s_mov_b32 s15, 0x20000\n" \
  "s_mul_i32 s22, s7, 6\n" \
  "s_add_u32 s22, s22, s3\n" \
  "s_mov_b32 s23, s3\n" \
  ".LMnw_%=:\n" \
  "s_lshl_b32 s18, s4, 7\n" \
  "s_add_u32 s18, s18, 128\n" \
  "v_lshlrev_b32 v170, 3, v1\n" \
  "v_add_u32 v170, s18, v170\n" \
  "v_cmp_gt_u32 vcc, 4, v1\n" \
  "s_mov_b32 s29, 65536\n" \
  "s_nop 0\n" \
  "v_cndmask_b32 v171, 0, v170, vcc\n" \
  "s_cmp_ge_i32 s24, 0\n" \
  "s_cbranch_scc1 .LMother_%=\n" \
  "s_setprio 3\n" \
  "s_barrier\n" \
  "ds_read_b64_tr_b16 v[88:89], v156 offset:0\n" \
  "ds_read_b64_tr_b16 v[90:91], v156 offset:4608\n" \
  "ds_read_b64_tr_b16 v[92:93], v156 offset:9216\n" \
  "ds_read_b64_tr_b16 v[94:95], v156 offset:13824\n" \
  "ds_read_b64_tr_b16 v[96:97], v156 offset:32\n" \
  "ds_read_b64_tr_b16 v[98:99], v156 offset:4640\n" \
  "ds_read_b64_tr_b16 v[100:101], v156 offset:9248\n" \
  "ds_read_b64_tr_b16 v[102:103], v156 offset:13856\n" \
  "ds_read_b64_tr_b16 v[104:105], v156 offset:64\n" \
  "ds_read_b64_tr_b16 v[106:107], v156 offset:4672\n" \
  "ds_read_b64_tr_b16 v[108:109], v156 offset:9280\n" \
  "ds_read_b64_tr_b16 v[110:111], v156 offset:13888\n" \
  "s_cmp_eq_u32 s3, 3\n" \
  "s_cbranch_scc0 .Lw1_%=t\n" \
  "s_mov_b64 exec, 31\n" \
  "buffer_load_dwordx2 v[168:169], v171, s[40:43], s29 offen sc1\n" \
  "s_mov_b64 exec, -1\n" \
  ".Lw1_%=t:\n" \
  "s_bitcmp0_b32 s3, 0\n" \
  "s_waitcnt lgkmcnt(10)\n" \
  "v_mfma_f32_16x16x32_bf16 v[112:115], v[88:91], v[136:139], 0\n" \
  "v_mfma_f32_16x16x32_bf16 v[116:119], v[88:91], v[144:147], 0\n" \
  "s_waitcnt lgkmcnt(8)\n" \
  "v_mfma_f32_16x16x32_bf16 v[112:115], v[92:95], v[140:143], v[112:115]\n" \
  "v_mfma_f32_16x16x32_bf16 v[116:119], v[92:95], v[148:151], v[116:119]\n" \
  "s_waitcnt lgkmcnt(6)\n" \
  "v_mfma_f32_16x16x32_bf16 v[120:123], v[96:99], v[136:139], 0\n" \
  "v_mfma_f32_16x16x32_bf16 v[124:127], v[96:99], v[144:147], 0\n" \
  "s_waitcnt lgkmcnt(4)\n" \
  "v_mfma_f32_16x16x32_bf16 v[120:123], v[100:103], v[140:143], v[120:123]\n" \
  "v_mfma_f32_16x16x32_bf16 v[124:127], v[100:103], v[148:151], v[124:127]\n" \
  "s_waitcnt lgkmcnt(2)\n" \
  "v_mfma_f32_16x16x32_bf16 v[128:131], v[104:107], v[136:139], 0\n" \
  "v_mfma_f32_16x16x32_bf16 v[132:135], v[104:107], v[144:147], 0\n" \
  "s_waitcnt lgkmcnt(0)\n" \
  "v_mfma_f32_16x16x32_bf16 v[128:131], v[108:111], v[140:143], v[128:131]\n" \
  "v_mfma_f32_16x16x32_bf16 v[132:135], v[108:111], v[148:151], v[132:135]\n" \
  "v_cvt_pk_bf16_f32 v88, v112, v116\n" \
  "v_cvt_pk_bf16_f32 v89, v113, v117\n" \
  "v_cvt_pk_bf16_f32 v90, v114, v118\n" \
  "v_cvt_pk_bf16_f32 v91, v115, v119\n" \
  "ds_write_b128 v157, v[88:91] offset:0\n" \
  "v_cvt_pk_bf16_f32 v92, v120, v124\n" \
  "v_cvt_pk_bf16_f32 v93, v121, v125\n" \
  "v_cvt_pk_bf16_f32 v94, v122, v126\n" \
  "v_cvt_pk_bf16_f32 v95, v123, v127\n" \
  "ds_write_b128 v157, v[92:95] offset:64\n" \
  "v_cvt_pk_bf16_f32 v96, v128, v132\n" \
  "v_cvt_pk_bf16_f32 v97, v129, v133\n" \
  "v_cvt_pk_bf16_f32 v98, v130, v134\n" \
  "v_cvt_pk_bf16_f32 v99, v131, v135\n" \
  "ds_write_b128 v157, v[96:99] offset:128\n" \
  "ds_read_b128 v[104:107], v159\n" \
  "ds_read_b128 v[108:111], v160\n" \
  "ds_read_b128 v[100:103], v158\n" \
  "s_waitcnt lgkmcnt(2)\n" \
  "buffer_store_dwordx4 v[104:107], v162, s[40:43], s44 offen sc1\n" \
  "s_waitcnt lgkmcnt(1)\n" \
  "buffer_store_dwordx4 v[108:111], v163, s[40:43], s44 offen sc1\n" \
  "s_waitcnt lgkmcnt(0)\n" \
  "s_cbranch_scc0 .LMnodc_%=\n" \
  "s_mov_b64 exec, 0xfff\n" \
  "v_and_b32 v4, 0xffff0000, v100\n" \
  "v_pk_mul_f32 v[6:7], v[4:5], v[164:165] op_sel_hi:[0,1]\n" \
  "v_and_b32 v100, 0xffff, v100\n" \
  "v_cvt_pk_bf16_f32 v112, v6, v7\n" \
  "v_and_b32 v4, 0xffff0000, v101\n" \
  "v_pk_mul_f32 v[6:7], v[4:5], v[164:165] op_sel_hi:[0,1]\n" \
  "v_and_b32 v101, 0xffff, v101\n" \
  "v_cvt_pk_bf16_f32 v113, v6, v7\n" \
  "v_and_b32 v4, 0xffff0000, v102\n" \
  "v_pk_mul_f32 v[6:7], v[4:5], v[164:165] op_sel_hi:[0,1]\n" \
  "v_and_b32 v102, 0xffff, v102\n" \
  "v_cvt_pk_bf16_f32 v114, v6, v7\n" \
  "v_and_b32 v4, 0xffff0000, v103\n" \
  "v_pk_mul_f32 v[6:7], v[4:5], v[164:165] op_sel_hi:[0,1]\n" \
  "v_and_b32 v103, 0xffff, v103\n" \
  "v_cvt_pk_bf16_f32 v115, v6, v7\n" \
  "buffer_store_dwordx4 v[112:115], v161, s[40:43], s45 offen sc1\n" \
  "s_mov_b64 exec, -1\n" \
  ".LMnodc_%=:\n" \
  "buffer_store_dwordx4 v[100:103], v161, s[40:43], s44 offen sc1\n" \
  "s_setprio 0\n" \
  ".LMjoin_%=:\n" \
  "s_cmp_eq_u32 s3, 3\n" \
  "s_cbranch_scc1 .LM3a_%=\n" \
  "v_and_b32 v2, 31, v1\n" \
  "v_lshrrev_b32 v3, 5, v1\n" \
  "v_lshlrev_b32 v5, 2, v3\n" \
  "v_cvt_f32_u32 v7, v5\n" \
  "s_mul_i32 s26, s3, 6\n" \
  "s_add_u32 s26, s26, s7\n" \
  "s_mul_i32 s26, s3, 6\n" \
  "s_add_u32 s26, s26, s7\n" \
  "v_and_b32 v4, 19, v2\n" \
  "v_and_b32 v5, 4, v2\n" \
  "v_lshl_or_b32 v4, v5, 1, v4\n" \
  "v_bfe_u32 v5, v2, 3, 1\n" \
  "v_lshl_or_b32 v4, v5, 2, v4\n" \
  "v_mul_u32_u24 v5, s26, v4\n" \
  "v_cvt_f32_u32 v5, v5\n" \
  "v_mul_f32 v5, 0x3ae38e39, v5\n" \
  "v_cvt_f32_u32 v4, v4\n" \
  "v_mul_f32 v4, 0x3d000000, v4\n" \
  "v_fmac_f32 v5, v4, v7\n" \
  "v_mov_b32 v28, v5\n" \
  "v_add_f32 v29, v28, v4\n" \
  "v_cos_f32 v30, v28\n" \
  "v_cos_f32 v31, v29\n" \
  "v_sin_f32 v28, v28\n" \
  "v_sin_f32 v29, v29\n" \
  "v_cvt_pk_bf16_f32 v56, v30, v31\n" \
  "v_cvt_pk_bf16_f32 v64, v28, v29\n" \
  "v_fmamk_f32 v28, v4, 0x40000000, v5\n" \
  "v_add_f32 v29, v28, v4\n" \
  "v_cos_f32 v30, v28\n" \
  "v_cos_f32 v31, v29\n" \
  "v_sin_f32 v28, v28\n" \
  "v_sin_f32 v29, v29\n" \
  "v_cvt_pk_bf16_f32 v57, v30, v31\n" \
  "v_cvt_pk_bf16_f32 v65, v28, v29\n" \
  "v_fmamk_f32 v28, v4, 0x41000000, v5\n" \
  "v_add_f32 v29, v28, v4\n" \
  "v_cos_f32 v30, v28\n" \
  "v_cos_f32 v31, v29\n" \
  "v_sin_f32 v28, v28\n" \
  "v_sin_f32 v29, v29\n" \
  "v_cvt_pk_bf16_f32 v58, v30, v31\n" \
  "v_cvt_pk_bf16_f32 v66, v28, v29\n" \
  "v_fmamk_f32 v28, v4, 0x41200000, v5\n" \
  "v_add_f32 v29, v28, v4\n" \
  "v_cos_f32 v30, v28\n" \
  "v_cos_f32 v31, v29\n" \
  "v_sin_f32 v28, v28\n" \
  "v_sin_f32 v29, v29\n" \
  "v_cvt_pk_bf16_f32 v59, v30, v31\n" \
  "v_cvt_pk_bf16_f32 v67, v28, v29\n" \
  "v_fmamk_f32 v28, v4, 0x41800000, v5\n" \
  "v_add_f32 v29, v28, v4\n" \
  "v_cos_f32 v30, v28\n" \
  "v_cos_f32 v31, v29\n" \
  "v_sin_f32 v28, v28\n" \
  "v_sin_f32 v29, v29\n" \
  "v_cvt_pk_bf16_f32 v60, v30, v31\n" \
  "v_cvt_pk_bf16_f32 v68, v28, v29\n" \
  "v_fmamk_f32 v28, v4, 0x41900000, v5\n" \
  "v_add_f32 v29, v28, v4\n" \
  "v_cos_f32 v30, v28\n" \
  "v_cos_f32 v31, v29\n" \
  "v_sin_f32 v28, v28\n" \
  "v_sin_f32 v29, v29\n" \
  "v_cvt_pk_bf16_f32 v61, v30, v31\n" \
  "v_cvt_pk_bf16_f32 v69, v28, v29\n" \
  "v_fmamk_f32 v28, v4, 0x41c00000, v5\n" \
  "v_add_f32 v29, v28, v4\n" \
  "v_cos_f32 v30, v28\n" \
  "v_cos_f32 v31, v29\n" \
  "v_sin_f32 v28, v28\n" \
  "v_sin_f32 v29, v29\n" \
  "v_cvt_pk_bf16_f32 v62, v30, v31\n" \
  "v_cvt_pk_bf16_f32 v70, v28, v29\n" \
  "v_fmamk_f32 v28, v4, 0x41d00000, v5\n" \
  "v_add_f32 v29, v28, v4\n" \
  "v_cos_f32 v30, v28\n" \
  "v_cos_f32 v31, v29\n" \
  "v_sin_f32 v28, v28\n" \
  "v_sin_f32 v29, v29\n" \
  "v_cvt_pk_bf16_f32 v63, v30, v31\n" \
  "v_cvt_pk_bf16_f32 v71, v28, v29\n" \
  "v_xor_b32 v72, 0x80008000, v64\n" \
  "v_xor_b32 v73, 0x80008000, v65\n" \
  "v_xor_b32 v74, 0x80008000, v66\n" \
  "v_xor_b32 v75, 0x80008000, v67\n" \
  "v_xor_b32 v76, 0x80008000, v68\n" \
  "v_xor_b32 v77, 0x80008000, v69\n" \
  "v_xor_b32 v78, 0x80008000, v70\n" \
  "v_xor_b32 v79, 0x80008000, v71\n" \
  "s_waitcnt vmcnt(0)\n" \
  "s_barrier\n" \
  "s_cmp_gt_u32 s6, 32\n" \
  "s_cbranch_scc1 .LMidle_%=\n" \
  "s_mov_b32 s8, s82\n" \
  "s_and_b32 s9, s83, 0xffff\n" \
  "s_mov_b32 s10, 1183744\n" \
  "s_mov_b32 s11, 0x20000\n" \
  ".Lx2_%=:\n" \
  "s_lshl_b32 s18, s22, 6\n" \
  "v_add_u32 v2, s18, v1\n" \
  "v_mul_u32_u24 v3, 0xdd7, v2\n" \
  "v_lshrrev_b32 v3, 17, v3\n" \
  "v_lshl_add_u32 v4, v3, 6, s6\n" \
  "v_sub_u32 v5, 0, v4\n" \
  "v_cmp_gt_u32 vcc, 32, v3\n" \
  "v_and_b32 v5, 0x7ff, v5\n" \
  "v_mul_u32_u24 v3, 37, v3\n" \
  "v_cndmask_b32 v4, v5, v4, vcc\n" \
  "v_sub_u32 v2, v2, v3\n" \
  "v_mul_u32_u24 v4, 0x242, v4\n" \
  "v_and_b32 v4, -16, v4\n" \
  "v_lshl_add_u32 v2, v2, 4, v4\n" \
  "s_lshl_b32 s18, s22, 10\n" \
  "s_add_u32 m0, s18, 81920\n" \
  "s_add_u32 s22, s22, 4\n" \
  "buffer_load_dwordx4 v2, s[8:11], 0 offen lds\n" \
  "s_add_u32 m0, s18, 119808\n" \
  "s_sub_u32 s18, s22, 37\n" \
  "buffer_load_dwordx4 v2, s[12:15], 0 offen lds\n" \
  "s_cselect_b32 s22, s22, s18\n" \
  "s_add_u32 s23, s23, 4\n" \
  "s_cmp_lt_u32 s23, 37\n" \
  "s_cbranch_scc1 .Lx2_%=\n" \
  "s_lshr_b32 s16, s5, 3\n" \
  "s_lshl_b32 s16, s16, 3\n" \
  "s_add_u32 s16, s16, s4\n" \
  "s_mul_i32 s0, s16, 76800\n" \
  "s_add_u32 s0, s0, 7340032\n" \
  "s_lshl_b32 s16, s16, 3\n" \
  "s_and_b32 s17, s5, 7\n" \
  "s_add_u32 s16, s16, s17\n" \
  "s_lshr_b32 s17, s16, 1\n" \
  "s_mul_i32 s1, s17, 18432\n" \
  "s_and_b32 s17, s16, 1\n" \
  "s_lshl_b32 s17, s17, 7\n" \
  "s_add_u32 s1, s1, s17\n" \
  "v_and_b32 v24, 7, v1\n" \
  "v_lshrrev_b32 v26, 3, v1\n" \
  "s_lshl_b32 s17, s3, 3\n" \
  "v_add_u32 v26, s17, v26\n" \
  "v_lshlrev_b32 v27, 4, v26\n" \
  "v_lshlrev_b32 v26, 8, v26\n" \
  "v_lshl_add_u32 v26, v24, 4, v26\n" \
  "v_lshlrev_b32 v24, 5, v24\n" \
  "v_add_u32 v24, 76800, v24\n" \
  "s_lshl_b32 s17, s3, 10\n" \
  "s_add_u32 s17, s17, 155648\n" \
  "v_add_u32 v25, s17, v10\n" \
  "s_mov_b32 s44, s46\n" \
  "s_mov_b32 s45, s47\n" \
  "s_cmp_eq_u32 s6, 0\n" \
  "s_cbranch_scc1 .LMdc_%=\n" \
  "s_waitcnt vmcnt(0)\n" \
  "s_barrier\n" \
  ".LMdcx_%=:\n" \
  "ds_read_u16_d16_hi v176, v81 offset:0\n" \
  "ds_read_u16_d16_hi v192, v81 offset:37888\n" \
  "ds_read_u16_d16_hi v177, v81 offset:36\n" \
  "ds_read_u16_d16_hi v193, v81 offset:37924\n" \
  "ds_read_u16_d16_hi v178, v81 offset:72\n" \
  "ds_read_u16_d16_hi v194, v81 offset:37960\n" \
  "ds_read_u16_d16_hi v179, v81 offset:108\n" \
  "ds_read_u16_d16_hi v195, v81 offset:37996\n" \
  "ds_read_u16_d16_hi v180, v81 offset:288\n" \
  "ds_read_u16_d16_hi v196, v81 offset:38176\n" \
  "s_waitcnt lgkmcnt(2)\n" \
  "ds_read_u16_d16_hi v181, v81 offset:324\n" \
  "ds_read_u16_d16_hi v197, v81 offset:38212\n" \
  "ds_read_u16_d16_hi v182, v81 offset:360\n" \
  "ds_read_u16_d16_hi v198, v81 offset:38248\n" \
  "ds_read_u16_d16_hi v183, v81 offset:396\n" \
  "ds_read_u16_d16_hi v199, v81 offset:38284\n" \
  "ds_read_u16_d16_hi v184, v86 offset:396\n" \
  "ds_read_u16_d16_hi v200, v86 offset:38284\n" \
  "ds_read_u16_d16_hi v185, v86 offset:360\n" \
  "ds_read_u16_d16_hi v201, v86 offset:38248\n" \
  "ds_read_u16_d16_hi v186, v86 offset:324\n" \
  "ds_read_u16_d16_hi v202, v86 offset:38212\n" \
  "s_waitcnt lgkmcnt(2)\n" \
  "ds_read_u16_d16_hi v187, v86 offset:288\n" \
  "ds_read_u16_d16_hi v203, v86 offset:38176\n" \
  "ds_read_u16_d16_hi v188, v86 offset:108\n" \
  "ds_read_u16_d16_hi v204, v86 offset:37996\n" \
  "ds_read_u16_d16_hi v189, v86 offset:72\n" \
  "ds_read_u16_d16_hi v205, v86 offset:37960\n" \
  "ds_read_u16_d16_hi v190, v86 offset:36\n" \
  "ds_read_u16_d16_hi v206, v86 offset:37924\n" \
  "ds_read_u16_d16_hi v191, v86 offset:0\n" \
  "ds_read_u16_d16_hi v207, v86 offset:37888\n" \
  "s_cmp_lg_u32 s45, 0\n" \
  "s_cbranch_scc1 .LMns_%=\n" \
  "v_subrev_u32 v87, 180, v86\n" \
  "ds_read_u16_d16_hi v208, v87 offset:0\n" \
  "ds_read_u16_d16_hi v210, v81 offset:576\n" \
  "ds_read_u16_d16_hi v209, v87 offset:37888\n" \
  "ds_read_u16_d16_hi v211, v81 offset:38464\n" \
  "s_waitcnt lgkmcnt(0)\n" \
  "s_mov_b32 exec_hi, 0\n" \
  "v_add_f32 v176, v176, v208\n" \
  "v_sub_f32 v192, v192, v209\n" \
  "v_add_f32 v184, v184, v210\n" \
  "v_sub_f32 v200, v200, v211\n" \
  "v_mul_f32 v176, 0.5, v176\n" \
  "v_mul_f32 v192, 0.5, v192\n" \
  "v_mul_f32 v184, 0.5, v184\n" \
  "v_mul_f32 v200, 0.5, v200\n" \
  "s_mov_b32 exec_hi, -1\n" \
  ".LMns_%=:\n" \
  "s_waitcnt lgkmcnt(0)\n" \
  "s_barrier\n" \
  "s_setprio 3\n" \
  "s_barrier\n" \
  "ds_read_b128 v[88:91], v80\n" \
  "ds_read_b128 v[92:95], v80 offset:32\n" \
  "ds_read_b128 v[96:99], v80 offset:2560\n" \
  "ds_read_b128 v[100:103], v80 offset:2592\n" \
  "s_waitcnt lgkmcnt(3)\n" \
  "v_mfma_f32_32x32x16_bf16 v[104:119], v[88:91], v[32:35], 0\n" \
  "v_mfma_f32_32x32x16_bf16 v[120:135], v[88:91], v[48:51], 0\n" \
  "s_waitcnt lgkmcnt(2)\n" \
  "v_mfma_f32_32x32x16_bf16 v[104:119], v[92:95], v[36:39], v[104:119]\n" \
  "v_mfma_f32_32x32x16_bf16 v[120:135], v[92:95], v[52:55], v[120:135]\n" \
  "s_waitcnt lgkmcnt(1)\n" \
  "v_mfma_f32_32x32x16_bf16 v[104:119], v[96:99], v[40:43], v[104:119]\n" \
  "s_waitcnt lgkmcnt(0)\n" \
  "v_mfma_f32_32x32x16_bf16 v[104:119], v[100:103], v[44:47], v[104:119]\n" \
  "s_nop 4\n" \
  "v_mfma_f32_32x32x16_bf16 v[120:135], v[96:99], v[32:35], v[120:135]\n" \
  "s_nop 4\n" \
  "v_mfma_f32_32x32x16_bf16 v[120:135], v[100:103], v[36:39], v[120:135]\n" \
  "v_cvt_pk_bf16_f32 v88, v104, v105\n" \
  "v_cvt_pk_bf16_f32 v89, v106, v107\n" \
  "v_cvt_pk_bf16_f32 v90, v108, v109\n" \
  "v_cvt_pk_bf16_f32 v91, v110, v111\n" \
  "s_nop 1\n" \
  "v_mfma_f32_32x32x16_bf16 v[136:151], v[88:91], v[32:35], 0\n" \
  "v_cvt_pk_bf16_f32 v92, v112, v113\n" \
  "v_cvt_pk_bf16_f32 v93, v114, v115\n" \
  "v_cvt_pk_bf16_f32 v94, v116, v117\n" \
  "v_cvt_pk_bf16_f32 v95, v118, v119\n" \
  "v_mfma_f32_32x32x16_bf16 v[208:223], v[88:91], v[48:51], 0\n" \
  "v_cvt_pk_bf16_f32 v96, v120, v121\n" \
  "v_cvt_pk_bf16_f32 v97, v122, v123\n" \
  "v_mfma_f32_32x32x16_bf16 v[136:151], v[92:95], v[36:39], v[136:151]\n" \
  "v_cvt_pk_bf16_f32 v98, v124, v125\n" \
  "v_cvt_pk_bf16_f32 v99, v126, v127\n" \
  "v_cvt_pk_bf16_f32 v100, v128, v129\n" \
  "v_cvt_pk_bf16_f32 v101, v130, v131\n" \
  "v_mfma_f32_32x32x16_bf16 v[208:223], v[92:95], v[52:55], v[208:223]\n" \
  "v_cvt_pk_bf16_f32 v102, v132, v133\n" \
  "v_cvt_pk_bf16_f32 v103, v134, v135\n" \
  "v_mfma_f32_32x32x16_bf16 v[136:151], v[96:99], v[40:43], v[136:151]\n" \
  "s_nop 0\n" \
  "v_mfma_f32_32x32x16_bf16 v[136:151], v[100:103], v[44:47], v[136:151]\n" \
  "s_nop 4\n" \
  "v_mfma_f32_32x32x16_bf16 v[208:223], v[96:99], v[32:35], v[208:223]\n" \
  "s_nop 4\n" \
  "v_mfma_f32_32x32x16_bf16 v[208:223], v[100:103], v[36:39], v[208:223]\n" \
  "v_pk_mul_f32 v[224:225], v[136:137], v[176:177]\n" \
  "v_pk_mul_f32 v[226:227], v[138:139], v[178:179]\n" \
  "v_pk_mul_f32 v[228:229], v[140:141], v[180:181]\n" \
  "v_pk_mul_f32 v[230:231], v[142:143], v[182:183]\n" \
  "s_nop 7\n" \
  "v_pk_fma_f32 v[224:225], v[208:209], v[192:193], v[224:225] neg_lo:[1,0,0] neg_hi:[1,0,0]\n" \
  "v_pk_fma_f32 v[226:227], v[210:211], v[194:195], v[226:227] neg_lo:[1,0,0] neg_hi:[1,0,0]\n" \
  "v_pk_fma_f32 v[228:229], v[212:213], v[196:197], v[228:229] neg_lo:[1,0,0] neg_hi:[1,0,0]\n" \
  "v_pk_fma_f32 v[230:231], v[214:215], v[198:199], v[230:231] neg_lo:[1,0,0] neg_hi:[1,0,0]\n" \
  "v_cvt_pk_bf16_f32 v88, v224, v225\n" \
  "v_cvt_pk_bf16_f32 v89, v226, v227\n" \
  "v_cvt_pk_bf16_f32 v90, v228, v229\n" \
  "v_cvt_pk_bf16_f32 v91, v230, v231\n" \
  "s_nop 1\n" \
  "v_mfma_f32_32x32x16_bf16 v[104:119], v[88:91], v[56:59], 0\n" \
  "v_pk_mul_f32 v[224:225], v[144:145], v[184:185]\n" \
  "v_pk_mul_f32 v[226:227], v[146:147], v[186:187]\n" \
  "v_pk_mul_f32 v[228:229], v[148:149], v[188:189]\n" \
  "v_pk_mul_f32 v[230:231], v[150:151], v[190:191]\n" \
  "v_pk_fma_f32 v[224:225], v[216:217], v[200:201], v[224:225]\n" \
  "v_pk_fma_f32 v[226:227], v[218:219], v[202:203], v[226:227]\n" \
  "v_mfma_f32_32x32x16_bf16 v[120:135], v[88:91], v[64:67], 0\n" \
  "v_pk_fma_f32 v[228:229], v[220:221], v[204:205], v[228:229]\n" \
  "v_pk_fma_f32 v[230:231], v[222:223], v[206:207], v[230:231]\n" \
  "v_cvt_pk_bf16_f32 v92, v224, v225\n" \
  "v_cvt_pk_bf16_f32 v93, v226, v227\n" \
  "v_cvt_pk_bf16_f32 v94, v228, v229\n" \
  "v_cvt_pk_bf16_f32 v95, v230, v231\n" \
  "s_nop 1\n" \
  "v_mfma_f32_32x32x16_bf16 v[104:119], v[92:95], v[60:63], v[104:119]\n" \
  "v_pk_mul_f32 v[224:225], v[136:137], v[192:193]\n" \
  "v_pk_mul_f32 v[226:227], v[138:139], v[194:195]\n" \
  "v_pk_mul_f32 v[228:229], v[140:141], v[196:197]\n" \
  "v_pk_mul_f32 v[230:231], v[142:143], v[198:199]\n" \
  "v_pk_fma_f32 v[224:225], v[208:209], v[176:177], v[224:225]\n" \
  "v_pk_fma_f32 v[226:227], v[210:211], v[178:179], v[226:227]\n" \
  "v_mfma_f32_32x32x16_bf16 v[120:135], v[92:95], v[68:71], v[120:135]\n" \
  "v_pk_fma_f32 v[228:229], v[212:213], v[180:181], v[228:229]\n" \
  "v_pk_fma_f32 v[230:231], v[214:215], v[182:183], v[230:231]\n" \
  "v_cvt_pk_bf16_f32 v96, v224, v225\n" \
  "v_cvt_pk_bf16_f32 v97, v226, v227\n" \
  "v_cvt_pk_bf16_f32 v98, v228, v229\n" \
  "v_cvt_pk_bf16_f32 v99, v230, v231\n" \
  "s_nop 1\n" \
  "v_mfma_f32_32x32x16_bf16 v[104:119], v[96:99], v[72:75], v[104:119]\n" \
  "v_pk_mul_f32 v[224:225], v[144:145], v[200:201] neg_lo:[0,1] neg_hi:[0,1]\n" \
  "v_pk_mul_f32 v[226:227], v[146:147], v[202:203] neg_lo:[0,1] neg_hi:[0,1]\n" \
  "v_pk_mul_f32 v[228:229], v[148:149], v[204:205] neg_lo:[0,1] neg_hi:[0,1]\n" \
  "v_pk_mul_f32 v[230:231], v[150:151], v[206:207] neg_lo:[0,1] neg_hi:[0,1]\n" \
  "v_pk_fma_f32 v[224:225], v[216:217], v[184:185], v[224:225]\n" \
  "v_pk_fma_f32 v[226:227], v[218:219], v[186:187], v[226:227]\n" \
  "v_pk_fma_f32 v[228:229], v[220:221], v[188:189], v[228:229]\n" \
  "v_pk_fma_f32 v[230:231], v[222:223], v[190:191], v[230:231]\n" \
  "v_cvt_pk_bf16_f32 v100, v224, v225\n" \
  "v_cvt_pk_bf16_f32 v101, v226, v227\n" \
  "v_cvt_pk_bf16_f32 v102, v228, v229\n" \
  "v_cvt_pk_bf16_f32 v103, v230, v231\n" \
  "s_nop 1\n" \
  "v_mfma_f32_32x32x16_bf16 v[104:119], v[100:103], v[76:79], v[104:119]\n" \
  "s_nop 4\n" \
  "v_mfma_f32_32x32x16_bf16 v[120:135], v[96:99], v[56:59], v[120:135]\n" \
  "s_nop 4\n" \
  "v_mfma_f32_32x32x16_bf16 v[120:135], v[100:103], v[60:63], v[120:135]\n" \
  "v_cvt_pk_bf16_f32 v88, v104, v105\n" \
  "v_cvt_pk_bf16_f32 v89, v106, v107\n" \
  "v_cvt_pk_bf16_f32 v90, v108, v109\n" \
  "v_cvt_pk_bf16_f32 v91, v110, v111\n" \
  "s_nop 1\n" \
  "v_mfma_f32_32x32x16_bf16 v[136:151], v[88:91], v[32:35], 0\n" \
  "v_cvt_pk_bf16_f32 v92, v112, v113\n" \
  "v_cvt_pk_bf16_f32 v93, v114, v115\n" \
  "v_cvt_pk_bf16_f32 v94, v116, v117\n" \
  "v_cvt_pk_bf16_f32 v95, v118, v119\n" \
  "v_mfma_f32_32x32x16_bf16 v[208:223], v[88:91], v[40:43], 0\n" \
  "v_cvt_pk_bf16_f32 v96, v120, v121\n" \
  "v_cvt_pk_bf16_f32 v97, v122, v123\n" \
  "v_mfma_f32_32x32x16_bf16 v[136:151], v[92:95], v[36:39], v[136:151]\n" \
  "v_cvt_pk_bf16_f32 v98, v124, v125\n" \
  "v_cvt_pk_bf16_f32 v99, v126, v127\n" \
  "v_cvt_pk_bf16_f32 v100, v128, v129\n" \
  "v_cvt_pk_bf16_f32 v101, v130, v131\n" \
  "v_mfma_f32_32x32x16_bf16 v[208:223], v[92:95], v[44:47], v[208:223]\n" \
  "v_cvt_pk_bf16_f32 v102, v132, v133\n" \
  "v_cvt_pk_bf16_f32 v103, v134, v135\n" \
  "v_mfma_f32_32x32x16_bf16 v[136:151], v[96:99], v[48:51], v[136:151]\n" \
  "s_nop 0\n" \
  "v_mfma_f32_32x32x16_bf16 v[136:151], v[100:103], v[52:55], v[136:151]\n" \
  "s_nop 4\n" \
  "v_mfma_f32_32x32x16_bf16 v[208:223], v[96:99], v[32:35], v[208:223]\n" \
  "s_nop 4\n" \
  "v_mfma_f32_32x32x16_bf16 v[208:223], v[100:103], v[36:39], v[208:223]\n" \
  "v_cvt_pk_bf16_f32 v88, v136, v137\n" \
  "v_cvt_pk_bf16_f32 v89, v138, v139\n" \
  "v_cvt_pk_bf16_f32 v90, v140, v141\n" \
  "v_cvt_pk_bf16_f32 v91, v142, v143\n" \
  "v_cvt_pk_bf16_f32 v92, v144, v145\n" \
  "v_cvt_pk_bf16_f32 v93, v146, v147\n" \
  "v_cvt_pk_bf16_f32 v94, v148, v149\n" \
  "v_cvt_pk_bf16_f32 v95, v150, v151\n" \
  "ds_write_b128 v82, v[88:91]\n" \
  "ds_write_b128 v83, v[92:95]\n" \
  "s_nop 1\n" \
  "v_cvt_pk_bf16_f32 v96, v208, v209\n" \
  "v_cvt_pk_bf16_f32 v97, v210, v211\n" \
  "v_cvt_pk_bf16_f32 v98, v212, v213\n" \
  "v_cvt_pk_bf16_f32 v99, v214, v215\n" \
  "ds_write_b128 v82, v[96:99] offset:64\n" \
  "v_cvt_pk_bf16_f32 v100, v216, v217\n" \
  "v_cvt_pk_bf16_f32 v101, v218, v219\n" \
  "v_cvt_pk_bf16_f32 v102, v220, v221\n" \
  "v_cvt_pk_bf16_f32 v103, v222, v223\n" \
  "ds_write_b128 v83, v[100:103] offset:64\n" \
  "ds_read_b128 v[104:107], v84 offset:0\n" \
  "ds_read_b128 v[108:111], v84 offset:1152\n" \
  "ds_read_b128 v[112:115], v84 offset:2304\n" \
  "ds_read_b128 v[116:119], v84 offset:3456\n" \
  "s_waitcnt lgkmcnt(3)\n" \
  "buffer_store_dwordx4 v[104:107], v85, s[40:43], s44 offen sc1\n" \
  "s_add_u32 s44, s44, 614400\n" \
  "s_waitcnt lgkmcnt(2)\n" \
  "buffer_store_dwordx4 v[108:111], v85, s[40:43], s44 offen sc1\n" \
  "s_add_u32 s44, s44, 614400\n" \
  "s_waitcnt lgkmcnt(1)\n" \
  "buffer_store_dwordx4 v[112:115], v85, s[40:43], s44 offen sc1\n" \
  "s_add_u32 s44, s44, 614400\n" \
  "s_waitcnt lgkmcnt(0)\n" \
  "buffer_store_dwordx4 v[116:119], v85, s[40:43], s44 offen sc1\n" \
  "s_setprio 0\n" \
  "s_movk_i32 s16, 19840\n" \
  "buffer_load_dwordx4 v[160:163], v27, s[40:43], s16 offen offset:1152 sc1\n" \
  "buffer_load_dwordx4 v[164:167], v27, s[40:43], s16 offen sc1\n" \
  "s_cmp_eq_u32 s3, 3\n" \
  "s_cbranch_scc0 .Lo3_%=\n" \
  "buffer_load_dwordx4 v[168:171], v27, s[40:43], s16 offen offset:1792 sc1\n" \
  "buffer_load_dwordx4 v[172:175], v27, s[40:43], s16 offen offset:640 sc1\n" \
  ".Lo3_%=:\n" \
  ".LMq2x_%=:\n" \
  "s_and_b32 s16, s4, 3\n" \
  "s_and_b32 s17, s4, 4\n" \
  "s_lshr_b32 s18, s5, 3\n" \
  "s_add_u32 s17, s17, s18\n" \
  "s_and_b32 s18, s5, 7\n" \
  "s_lshr_b32 s19, s18, 2\n" \
  "s_and_b32 s20, s18, 3\n" \
  "s_lshr_b32 s21, s3, 2\n" \
  "s_and_b32 s22, s3, 3\n" \
  "s_cmp_lt_u32 s3, 4\n" \
  "s_cselect_b32 s48, s86, s88\n" \
  "s_cselect_b32 s49, s87, s89\n" \
  "s_mov_b32 s50, s96\n" \
  "s_mov_b32 s51, s97\n" \
  "s_mov_b32 s52, s98\n" \
  "s_mov_b32 s53, s99\n" \
  "s_lshl_b32 s23, s16, 1\n" \
  "s_add_u32 s23, s23, s19\n" \
  "s_mul_i32 s54, s23, 1152\n" \
  "s_lshl_b32 s24, s20, 8\n" \
  "s_add_u32 s54, s54, s24\n" \
  "s_lshl_b32 s57, s3, 10\n" \
  "s_lshl_b32 s24, s16, 3\n" \
  "s_add_u32 s24, s24, s18\n" \
  "s_mul_i32 s55, s24, 40960\n" \
  "s_add_u32 s55, s55, s57\n" \
  "s_add_u32 s55, s55, 48234496\n" \
  "s_lshl_b32 s24, s17, 3\n" \
  "s_add_u32 s25, s24, s3\n" \
  "s_lshl_b32 s56, s25, 2\n" \
  "s_add_u32 s56, s56, s16\n" \
  "s_lshl_b32 s56, s56, 6\n" \
  "s_add_u32 s56, s56, 262144\n" \
  "s_mul_i32 s60, s25, 36864\n" \
  "s_add_u32 s60, s60, 13631488\n" \
  "s_lshl_b32 s58, s4, 11\n" \
  "s_add_u32 s58, s58, 147456\n" \
  "s_add_u32 s25, s24, s23\n" \
  "s_lshl_b32 s25, s25, 3\n" \
  "s_add_u32 s25, s25, s3\n" \
  "s_mul_i32 s61, s25, 36864\n" \
  "s_add_u32 s61, s61, 16777216\n" \
  "s_lshl_b32 s66, s25, 6\n" \
  "s_add_u32 s66, s66, 229376\n" \
  "s_lshr_b32 s26, s20, 1\n" \
  "s_lshl_b32 s27, s26, 11\n" \
  "s_and_b32 s28, s20, 1\n" \
  "s_lshl_b32 s28, s28, 9\n" \
  "s_add_u32 s62, s61, s27\n" \
  "s_add_u32 s62, s62, s28\n" \
  "s_add_u32 s62, s62, 16384\n" \
  "s_lshl_b32 s27, s20, 11\n" \
  "s_add_u32 s61, s61, s27\n" \
  "s_lshl_b32 s25, s16, 1\n" \
  "s_add_u32 s25, s25, s24\n" \
  "s_add_u32 s25, s25, s21\n" \
  "s_lshl_b32 s25, s25, 1\n" \
  "s_add_u32 s25, s25, s19\n" \
  "s_lshl_b32 s25, s25, 2\n" \
  "s_add_u32 s25, s25, s22\n" \
  "s_mul_i32 s63, s25, 36864\n" \
  "s_add_u32 s63, s63, 16777216\n" \
  "s_lshl_b32 s67, s25, 6\n" \
  "s_add_u32 s67, s67, 229376\n" \
  "s_lshl_b32 s27, s21, 10\n" \
  "s_add_u32 s63, s63, s27\n" \
  "s_xor_b32 s64, s63, 1024\n" \
  "s_lshl_b32 s29, s16, 2\n" \
  "s_add_u32 s25, s29, s20\n" \
  "s_mul_i32 s65, s25, 335872\n" \
  "s_add_u32 s65, s65, s57\n" \
  "s_add_u32 s65, s65, 41943040\n" \
  "s_add_u32 s25, s24, s18\n" \
  "s_lshl_b32 s25, s25, 5\n" \
  "s_add_u32 s25, s25, s3\n" \
  "s_lshl_b32 s27, s25, 13\n" \
  "s_add_u32 s27, s27, 50331648\n" \
  "s_lshl_b32 s25, s25, 5\n" \
  "s_add_u32 s25, s25, 163840\n" \
  "s_add_u32 s29, s16, 0\n" \
  "s_and_b32 s29, s29, 3\n" \
  "s_lshl_b32 s73, s29, 16\n" \
  "s_add_u32 s73, s73, s27\n" \
  "s_lshl_b32 s29, s29, 8\n" \
  "s_add_u32 s76, s29, s25\n" \
  "s_add_u32 s29, s16, 1\n" \
  "s_and_b32 s29, s29, 3\n" \
  "s_lshl_b32 s68, s29, 16\n" \
  "s_add_u32 s68, s68, s27\n" \
  "s_lshl_b32 s29, s29, 8\n" \
  "s_add_u32 s78, s29, s25\n" \
  "s_add_u32 s29, s16, 2\n" \
  "s_and_b32 s29, s29, 3\n" \
  "s_lshl_b32 s69, s29, 16\n" \
  "s_add_u32 s69, s69, s27\n" \
  "s_lshl_b32 s29, s29, 8\n" \
  "s_add_u32 s79, s29, s25\n" \
  "s_add_u32 s29, s16, 3\n" \
  "s_and_b32 s29, s29, 3\n" \
  "s_lshl_b32 s70, s29, 16\n" \
  "s_add_u32 s70, s70, s27\n" \
  "s_lshl_b32 s29, s29, 8\n" \
  "s_add_u32 s59, s29, s25\n" \
  "s_sub_u32 s29, s22, s16\n" \
  "s_sub_u32 s29, s29, 1\n" \
  "s_and_b32 s29, s29, 3\n" \
  "s_add_u32 s74, s29, 1\n" \
  "s_lshr_b32 s74, s74, 2\n" \
  "s_lshl_b32 s71, s22, 7\n" \
  "s_add_u32 s71, s71, s29\n" \
  "s_lshl_b32 s71, s71, 9\n" \
  "s_add_u32 s71, s71, s27\n" \
  "s_add_u32 s71, s71, 3072\n" \
  "s_lshl_b32 s72, s22, 8\n" \
  "s_add_u32 s72, s72, s25\n" \
  "s_lshl_b32 s25, s17, 1\n" \
  "s_add_u32 s25, s25, s19\n" \
  "s_lshl_b32 s25, s25, 2\n" \
  "s_add_u32 s25, s25, s22\n" \
  "s_lshl_b32 s25, s25, 1\n" \
  "s_add_u32 s25, s25, s21\n" \
  "s_mul_i32 s27, s25, 18432\n" \
  "s_add_u32 s27, s27, 10485760\n" \
  "s_lshl_b32 s29, s20, 2\n" \
  "s_add_u32 s29, s29, s16\n" \
  "s_lshl_b32 s75, s29, 10\n" \
  "s_add_u32 s75, s75, s27\n" \
  "s_lshl_b32 s77, s20, 9\n" \
  "s_add_u32 s77, s77, s27\n" \
  "s_add_u32 s77, s77, 16384\n" \
  "s_lshr_b32 s31, s2, 1\n" \
  "s_lshl_b32 s31, s31, 13\n" \
  "s_and_b32 s27, s2, 1\n" \
  "s_cmp_eq_u32 s27, s21\n" \
  "s_cselect_b32 s31, s31, 0x40000000\n" \
  "s_cmp_lt_u32 s2, 216\n" \
  "s_cselect_b32 s31, s31, 0x40000000\n" \
  "s_or_b32 s31, s31, s22\n" \
  "v_lshrrev_b32 v2, 3, v1\n" \
  "s_lshl_b32 s27, s22, 3\n" \
  "v_add_u32 v2, s27, v2\n" \
  "v_mul_u32_u24 v8, 0x2400, v2\n" \
  "v_bfe_u32 v3, v1, 4, 1\n" \
  "s_and_b32 s27, s22, 1\n" \
  "s_lshl_b32 s27, s27, 1\n" \
  "v_or_b32 v3, s27, v3\n" \
  "v_lshlrev_b32 v3, 5, v3\n" \
  "v_and_b32 v4, 0x70, v10\n" \
  "v_xor_b32 v3, v3, v4\n" \
  "v_add_u32 v8, v8, v3\n" \
  "v_mov_b32 v9, v10\n" \
  "v_lshlrev_b32 v11, 3, v1\n" \
  "v_lshrrev_b32 v2, 4, v1\n" \
  "v_bfe_u32 v3, v1, 2, 2\n" \
  "v_lshl_add_u32 v4, v2, 3, v3\n" \
  "v_lshlrev_b32 v4, 7, v4\n" \
  "v_and_b32 v5, 24, v11\n" \
  "v_or_b32 v4, v4, v5\n" \
  "v_bfe_u32 v5, v1, 3, 2\n" \
  "v_lshl_or_b32 v12, v5, 5, v4\n" \
  "v_xor_b32 v13, 32, v12\n" \
  "v_xor_b32 v14, 64, v12\n" \
  "v_xor_b32 v15, 0x60, v12\n" \
  "v_and_b32 v3, 15, v1\n" \
  "v_lshrrev_b32 v4, 5, v1\n" \
  "v_lshl_add_u32 v4, v4, 4, v3\n" \
  "v_and_b32 v5, 1, v2\n" \
  "v_lshlrev_b32 v5, 3, v5\n" \
  "v_lshl_or_b32 v16, v4, 4, v5\n" \
  "s_lshl_b32 s27, s21, 12\n" \
  "v_add_u32 v17, s27, v10\n" \
  "s_mul_i32 s27, s3, 5120\n" \
  "s_add_u32 s27, s27, 98304\n" \
  "v_add_u32 v18, s27, v10\n" \
  "s_xor_b32 s27, s3, 4\n" \
  "s_mul_i32 s27, s27, 5120\n" \
  "s_add_u32 s27, s27, 98304\n" \
  "v_add_u32 v19, s27, v10\n" \
  "s_and_b32 s27, s25, 3\n" \
  "s_lshl_b32 s27, s27, 9\n" \
  "s_lshr_b32 s25, s25, 2\n" \
  "s_add_u32 s25, s25, s27\n" \
  "s_mul_i32 s25, s25, 1152\n" \
  "v_mul_u32_u24 v3, 0x9000, v3\n" \
  "v_add_u32 v3, s25, v3\n" \
  "s_lshl_b32 s29, s29, 5\n" \
  "v_lshl_add_u32 v4, v2, 3, s29\n" \
  "s_lshl_b32 s27, s20, 4\n" \
  "s_add_u32 s27, s27, 512\n" \
  "v_lshl_add_u32 v5, v2, 2, s27\n" \
  "v_lshl_add_u32 v20, v4, 1, v3\n" \
  "v_lshl_add_u32 v21, v5, 1, v3\n" \
  "s_movk_i32 s27, 11520\n" \
  "v_lshl_add_u32 v22, v4, 2, s27\n" \
  "v_lshl_add_u32 v23, v5, 2, s27\n" \
  "s_waitcnt vmcnt(0)\n" \
  "s_barrier\n" \
  "s_mul_i32 s16, s2, 3\n" \
  "s_add_u32 s16, s16, s3\n" \
  "s_cmp_lt_u32 s16, 648\n" \
  "s_cselect_b32 s10, 5308416, 0\n" \
  "s_bitcmp1_b32 s16, 0\n" \
  "s_cselect_b32 s8, s88, s86\n" \
  "s_cselect_b32 s9, s89, s87\n" \
  "s_and_b32 s9, s9, 0xffff\n" \
  "s_mov_b32 s11, 0x20000\n" \
  "s_lshr_b32 s16, s16, 1\n" \
  "s_min_u32 s16, s16, 323\n" \
  "s_lshl_b32 s16, s16, 13\n" \
  "s_add_u32 s16, s16, 2654208\n" \
  "v_lshlrev_b32 v252, 7, v1\n" \
  "buffer_load_dword v253, v252, s[8:11], s16 offen\n" \
  "v_lshlrev_b32 v144, 16, v160\n" \
  "v_and_b32 v145, 0xffff0000, v160\n" \
  "v_lshlrev_b32 v152, 16, v164\n" \
  "v_and_b32 v153, 0xffff0000, v164\n" \
  "v_lshlrev_b32 v146, 16, v161\n" \
  "v_and_b32 v147, 0xffff0000, v161\n" \
  "v_lshlrev_b32 v154, 16, v165\n" \
  "v_and_b32 v155, 0xffff0000, v165\n" \
  "v_lshlrev_b32 v148, 16, v162\n" \
  "v_and_b32 v149, 0xffff0000, v162\n" \
  "v_lshlrev_b32 v156, 16, v166\n" \
  "v_and_b32 v157, 0xffff0000, v166\n" \
  "v_lshlrev_b32 v150, 16, v163\n" \
  "v_and_b32 v151, 0xffff0000, v163\n" \
  "v_lshlrev_b32 v158, 16, v167\n" \
  "v_and_b32 v159, 0xffff0000, v167\n" \
  "v_pk_add_f32 v[144:145], v[144:145], 1.0 op_sel_hi:[1,0]\n" \
  "v_pk_add_f32 v[146:147], v[146:147], 1.0 op_sel_hi:[1,0]\n" \
  "v_pk_add_f32 v[148:149], v[148:149], 1.0 op_sel_hi:[1,0]\n" \
  "v_pk_add_f32 v[150:151], v[150:151], 1.0 op_sel_hi:[1,0]\n" \
  "s_cmp_gt_u32 s6, 32\n" \
  "s_cbranch_scc1 .LMb2_%=\n" \
  "s_mul_i32 s26, s6, 6\n" \
  "s_add_u32 s26, s26, s7\n" \
  "s_mul_i32 s26, s26, 5\n" \
  "s_add_u32 s26, s26, s3\n" \
  "s_lshl_b32 s27, s3, 12\n" \
  "s_add_u32 s27, s27, 21248\n" \
  "v_lshrrev_b32 v28, 4, v1\n" \
  "v_bfe_u32 v29, v1, 2, 2\n" \
  "v_and_b32 v30, 3, v1\n" \
  "s_cmp_lt_u32 s26, 1296\n" \
  "s_cbranch_scc1 .Lt4_%=d\n" \
  "s_sub_u32 s16, s26, 1296\n" \
  "s_bfe_u32 s17, s16, 0x30001\n" \
  "s_mul_i32 s17, s17, 163840\n" \
  "s_lshr_b32 s18, s16, 4\n" \
  "s_lshl_b32 s18, s18, 1\n" \
  "s_and_b32 s16, s16, 1\n" \
  "s_add_u32 s16, s16, s18\n" \
  "s_lshl_b32 s16, s16, 10\n" \
  "s_add_u32 s16, s16, s17\n" \
  "s_add_u32 s36, s16, 48234496\n" \
  "s_add_u32 s37, s36, 40960\n" \
  "s_add_u32 s38, s36, 81920\n" \
  "s_add_u32 s39, s36, 122880\n" \
  "s_mov_b32 s16, 3\n" \
  "s_mov_b32 s17, 3\n" \
  "s_mov_b32 s18, 32\n" \
  "s_movk_i32 s19, 512\n" \
  "s_branch .Lt4_%=j\n" \
  ".Lt4_%=d:\n" \
  "s_mul_i32 s16, s26, 7282\n" \
  "s_lshr_b32 s16, s16, 16\n" \
  "s_mul_i32 s17, s16, 9\n" \
  "s_sub_u32 s17, s26, s17\n" \
  "s_mul_i32 s19, s16, 3641\n" \
  "s_lshr_b32 s19, s19, 16\n" \
  "s_mul_i32 s20, s19, 18\n" \
  "s_sub_u32 s20, s16, s20\n" \
  "s_lshr_b32 s18, s19, 1\n" \
  "s_and_b32 s19, s19, 1\n" \
  "s_lshr_b32 s21, s20, 2\n" \
  "s_and_b32 s22, s20, 1\n" \
  "s_lshl_b32 s22, s22, 2\n" \
  "s_add_u32 s21, s21, s22\n" \
  "s_bitcmp1_b32 s20, 1\n" \
  "s_cselect_b32 s22, 10, 0\n" \
  "s_add_u32 s21, s21, s22\n" \
  "s_sub_u32 s22, s20, 8\n" \
  "s_cmp_lt_u32 s20, 16\n" \
  "s_cselect_b32 s21, s21, s22\n" \
  "s_mul_i32 s16, s18, 1343488\n" \
  "s_lshl_b32 s19, s19, 12\n" \
  "s_add_u32 s16, s16, s19\n" \
  "s_add_u32 s16, s16, 41943040\n" \
  "s_and_b32 s23, s21, 1\n" \
  "s_sub_u32 s24, s21, s23\n" \
  "s_lshl_b32 s22, s24, 1\n" \
  "s_add_u32 s22, s22, s23\n" \
  "s_mul_i32 s19, s24, 7\n" \
  "s_add_u32 s19, s19, s23\n" \
  "s_add_u32 s20, s19, 10\n" \
  "s_mul_i32 s24, s24, 5\n" \
  "s_add_u32 s24, s24, s23\n" \
  "s_add_u32 s24, s24, 20\n" \
  "s_add_u32 s23, s22, 90\n" \
  "s_cmp_lt_u32 s21, 10\n" \
  "s_cselect_b32 s19, s19, s24\n" \
  "s_cselect_b32 s20, s20, s23\n" \
  "s_add_u32 s22, s22, 126\n" \
  "s_cmp_eq_u32 s17, 8\n" \
  "s_cbranch_scc1 .Lt4_%=e\n" \
  "s_lshr_b32 s21, s17, 1\n" \
  "s_mul_i32 s21, s21, 335872\n" \
  "s_add_u32 s16, s16, s21\n" \
  "s_and_b32 s17, s17, 1\n" \
  "s_lshl_b32 s17, s17, 1\n" \
  "s_sub_u32 s17, s17, s18\n" \
  "s_add_u32 s21, s17, 0\n" \
  "s_and_b32 s21, s21, 3\n" \
  "s_add_u32 s23, s19, 4\n" \
  "s_cmp_eq_u32 s21, 1\n" \
  "s_cselect_b32 s23, s19, s23\n" \
  "s_cmp_eq_u32 s21, 3\n" \
  "s_cselect_b32 s23, s20, s23\n" \
  "s_cmp_eq_u32 s21, 0\n" \
  "s_cselect_b32 s23, s22, s23\n" \
  "s_andn2_b32 s24, s23, 3\n" \
  "s_add_u32 s24, s24, s23\n" \
  "s_lshl_b32 s24, s24, 10\n" \
  "s_add_u32 s36, s24, s16\n" \
  "s_add_u32 s23, s23, 2\n" \
  "s_andn2_b32 s24, s23, 3\n" \
  "s_add_u32 s24, s24, s23\n" \
  "s_lshl_b32 s24, s24, 10\n" \
  "s_add_u32 s37, s24, s16\n" \
  "s_add_u32 s21, s17, 1\n" \
  "s_and_b32 s21, s21, 3\n" \
  "s_add_u32 s23, s19, 4\n" \
  "s_cmp_eq_u32 s21, 1\n" \
  "s_cselect_b32 s23, s19, s23\n" \
  "s_cmp_eq_u32 s21, 3\n" \
  "s_cselect_b32 s23, s20, s23\n" \
  "s_cmp_eq_u32 s21, 0\n" \
  "s_cselect_b32 s23, s22, s23\n" \
  "s_andn2_b32 s24, s23, 3\n" \
  "s_add_u32 s24, s24, s23\n" \
  "s_lshl_b32 s24, s24, 10\n" \
  "s_add_u32 s38, s24, s16\n" \
  "s_add_u32 s23, s23, 2\n" \
  "s_andn2_b32 s24, s23, 3\n" \
  "s_add_u32 s24, s24, s23\n" \
  "s_lshl_b32 s24, s24, 10\n" \
  "s_add_u32 s39, s24, s16\n" \
  "s_mov_b32 s16, 2\n" \
  "s_mov_b32 s17, 4\n" \
  "s_mov_b32 s18, 8\n" \
  "s_movk_i32 s19, 2048\n" \
  "s_branch .Lt4_%=j\n" \
  ".Lt4_%=e:\n" \
  "s_add_u32 s23, s19, 8\n" \
  "s_andn2_b32 s24, s23, 3\n" \
  "s_add_u32 s24, s24, s23\n" \
  "s_lshl_b32 s24, s24, 10\n" \
  "s_add_u32 s36, s24, s16\n" \
  "s_add_u32 s37, s36, 335872\n" \
  "s_add_u32 s38, s36, 671744\n" \
  "s_add_u32 s39, s36, 1007616\n" \
  "s_mov_b32 s16, 2\n" \
  "s_mov_b32 s17, 3\n" \
  "s_mov_b32 s18, 32\n" \
  "s_movk_i32 s19, 2048\n" \
  ".Lt4_%=j:\n" \
  "v_lshlrev_b32 v28, s16, v28\n" \
  "v_add_u32 v28, v28, v29\n" \
  "v_lshlrev_b32 v30, s17, v30\n" \
  "v_lshl_add_u32 v28, v28, 7, v30\n" \
  "v_add_u32 v28, s27, v28\n" \
  "v_add_u32 v29, s19, v28\n" \
  "v_add_u32 v30, s18, v28\n" \
  "v_add_u32 v31, s18, v29\n" \
  "ds_read_b64_tr_b16 v[232:233], v28\n" \
  "ds_read_b64_tr_b16 v[234:235], v29\n" \
  "ds_read_b64_tr_b16 v[236:237], v30\n" \
  "ds_read_b64_tr_b16 v[238:239], v31\n" \
  "ds_read_b64_tr_b16 v[240:241], v28 offset:64\n" \
  "ds_read_b64_tr_b16 v[242:243], v29 offset:64\n" \
  "ds_read_b64_tr_b16 v[244:245], v30 offset:64\n" \
  "ds_read_b64_tr_b16 v[246:247], v31 offset:64\n" \
  "s_cmp_lt_u32 s3, 1\n" \
  "s_cbranch_scc0 .LMr1_%=\n" \
  "s_add_u32 s26, s26, 4\n" \
  "s_add_u32 s27, s27, 42496\n" \
  "v_lshrrev_b32 v32, 4, v1\n" \
  "v_bfe_u32 v33, v1, 2, 2\n" \
  "v_and_b32 v34, 3, v1\n" \
  "s_cmp_lt_u32 s26, 1296\n" \
  "s_cbranch_scc1 .Lt5_%=d\n" \
  "s_sub_u32 s16, s26, 1296\n" \
  "s_bfe_u32 s17, s16, 0x30001\n" \
  "s_mul_i32 s17, s17, 163840\n" \
  "s_lshr_b32 s18, s16, 4\n" \
  "s_lshl_b32 s18, s18, 1\n" \
  "s_and_b32 s16, s16, 1\n" \
  "s_add_u32 s16, s16, s18\n" \
  "s_lshl_b32 s16, s16, 10\n" \
  "s_add_u32 s16, s16, s17\n" \
  "s_add_u32 s12, s16, 48234496\n" \
  "s_add_u32 s13, s12, 40960\n" \
  "s_add_u32 s14, s12, 81920\n" \
  "s_add_u32 s15, s12, 122880\n" \
  "s_mov_b32 s16, 3\n" \
  "s_mov_b32 s17, 3\n" \
  "s_mov_b32 s18, 32\n" \
  "s_movk_i32 s19, 512\n" \
  "s_branch .Lt5_%=j\n" \
  ".Lt5_%=d:\n" \
  "s_mul_i32 s16, s26, 7282\n" \
  "s_lshr_b32 s16, s16, 16\n" \
  "s_mul_i32 s17, s16, 9\n" \
  "s_sub_u32 s17, s26, s17\n" \
  "s_mul_i32 s19, s16, 3641\n" \
  "s_lshr_b32 s19, s19, 16\n" \
  "s_mul_i32 s20, s19, 18\n" \
  "s_sub_u32 s20, s16, s20\n" \
  "s_lshr_b32 s18, s19, 1\n" \
  "s_and_b32 s19, s19, 1\n" \
  "s_lshr_b32 s21, s20, 2\n" \
  "s_and_b32 s22, s20, 1\n" \
  "s_lshl_b32 s22, s22, 2\n" \
  "s_add_u32 s21, s21, s22\n" \
  "s_bitcmp1_b32 s20, 1\n" \
  "s_cselect_b32 s22, 10, 0\n" \
  "s_add_u32 s21, s21, s22\n" \
  "s_sub_u32 s22, s20, 8\n" \
  "s_cmp_lt_u32 s20, 16\n" \
  "s_cselect_b32 s21, s21, s22\n" \
  "s_mul_i32 s16, s18, 1343488\n" \
  "s_lshl_b32 s19, s19, 12\n" \
  "s_add_u32 s16, s16, s19\n" \
  "s_add_u32 s16, s16, 41943040\n" \
  "s_and_b32 s23, s21, 1\n" \
  "s_sub_u32 s24, s21, s23\n" \
  "s_lshl_b32 s22, s24, 1\n" \
  "s_add_u32 s22, s22, s23\n" \
  "s_mul_i32 s19, s24, 7\n" \
  "s_add_u32 s19, s19, s23\n" \
  "s_add_u32 s20, s19, 10\n" \
  "s_mul_i32 s24, s24, 5\n" \
  "s_add_u32 s24, s24, s23\n" \
  "s_add_u32 s24, s24, 20\n" \
  "s_add_u32 s23, s22, 90\n" \
  "s_cmp_lt_u32 s21, 10\n" \
  "s_cselect_b32 s19, s19, s24\n" \
  "s_cselect_b32 s20, s20, s23\n" \
  "s_add_u32 s22, s22, 126\n" \
  "s_cmp_eq_u32 s17, 8\n" \
  "s_cbranch_scc1 .Lt5_%=e\n" \
  "s_lshr_b32 s21, s17, 1\n" \
  "s_mul_i32 s21, s21, 335872\n" \
  "s_add_u32 s16, s16, s21\n" \
  "s_and_b32 s17, s17, 1\n" \
  "s_lshl_b32 s17, s17, 1\n" \
  "s_sub_u32 s17, s17, s18\n" \
  "s_add_u32 s21, s17, 0\n" \
  "s_and_b32 s21, s21, 3\n" \
  "s_add_u32 s23, s19, 4\n" \
  "s_cmp_eq_u32 s21, 1\n" \
  "s_cselect_b32 s23, s19, s23\n" \
  "s_cmp_eq_u32 s21, 3\n" \
  "s_cselect_b32 s23, s20, s23\n" \
  "s_cmp_eq_u32 s21, 0\n" \
  "s_cselect_b32 s23, s22, s23\n" \
  "s_andn2_b32 s24, s23, 3\n" \
  "s_add_u32 s24, s24, s23\n" \
  "s_lshl_b32 s24, s24, 10\n" \
  "s_add_u32 s12, s24, s16\n" \
  "s_add_u32 s23, s23, 2\n" \
  "s_andn2_b32 s24, s23, 3\n" \
  "s_add_u32 s24, s24, s23\n" \
  "s_lshl_b32 s24, s24, 10\n" \
  "s_add_u32 s13, s24, s16\n" \
  "s_add_u32 s21, s17, 1\n" \
  "s_and_b32 s21, s21, 3\n" \
  "s_add_u32 s23, s19, 4\n" \
  "s_cmp_eq_u32 s21, 1\n" \
  "s_cselect_b32 s23, s19, s23\n" \
  "s_cmp_eq_u32 s21, 3\n" \
  "s_cselect_b32 s23, s20, s23\n" \
  "s_cmp_eq_u32 s21, 0\n" \
  "s_cselect_b32 s23, s22, s23\n" \
  "s_andn2_b32 s24, s23, 3\n" \
  "s_add_u32 s24, s24, s23\n" \
  "s_lshl_b32 s24, s24, 10\n" \
  "s_add_u32 s14, s24, s16\n" \
  "s_add_u32 s23, s23, 2\n" \
  "s_andn2_b32 s24, s23, 3\n" \
  "s_add_u32 s24, s24, s23\n" \
  "s_lshl_b32 s24, s24, 10\n" \
  "s_add_u32 s15, s24, s16\n" \
  "s_mov_b32 s16, 2\n" \
  "s_mov_b32 s17, 4\n" \
  "s_mov_b32 s18, 8\n" \
  "s_movk_i32 s19, 2048\n" \
  "s_branch .Lt5_%=j\n" \
  ".Lt5_%=e:\n" \
  "s_add_u32 s23, s19, 8\n" \
  "s_andn2_b32 s24, s23, 3\n" \
  "s_add_u32 s24, s24, s23\n" \
  "s_lshl_b32 s24, s24, 10\n" \
  "s_add_u32 s12, s24, s16\n" \
  "s_add_u32 s13, s12, 335872\n" \
  "s_add_u32 s14, s12, 671744\n" \
  "s_add_u32 s15, s12, 1007616\n" \
  "s_mov_b32 s16, 2\n" \
  "s_mov_b32 s17, 3\n" \
  "s_mov_b32 s18, 32\n" \
  "s_movk_i32 s19, 2048\n" \
  ".Lt5_%=j:\n" \
  "v_lshlrev_b32 v32, s16, v32\n" \
  "v_add_u32 v32, v32, v33\n" \
  "v_lshlrev_b32 v34, s17, v34\n" \
  "v_lshl_add_u32 v32, v32, 7, v34\n" \
  "v_add_u32 v32, s27, v32\n" \
  "v_add_u32 v33, s19, v32\n" \
  "v_add_u32 v34, s18, v32\n" \
  "v_add_u32 v35, s18, v33\n" \
  "ds_read_b64_tr_b16 v[216:217], v32\n" \
  "ds_read_b64_tr_b16 v[218:219], v33\n" \
  "ds_read_b64_tr_b16 v[220:221], v34\n" \
  "ds_read_b64_tr_b16 v[222:223], v35\n" \
  "ds_read_b64_tr_b16 v[224:225], v32 offset:64\n" \
  "ds_read_b64_tr_b16 v[226:227], v33 offset:64\n" \
  "ds_read_b64_tr_b16 v[228:229], v34 offset:64\n" \
  "ds_read_b64_tr_b16 v[230:231], v35 offset:64\n" \
  ".LMr1_%=:\n" \
  "s_waitcnt lgkmcnt(0)\n" \
  ".LMb2_%=:\n" \
  "s_lshl_b32 s21, s3, 10\n" \
  "s_add_u32 s16, s0, s21\n" \
  "s_mov_b32 m0, s21\n" \
  "s_barrier\n" \
  "buffer_load_dwordx4 v10, s[40:43], s16 offen sc1 lds\n" \
  "s_add_u32 m0, s21, 4096\n" \
  "s_add_u32 s16, s16, 4096\n" \
  "buffer_load_dwordx4 v10, s[40:43], s16 offen sc1 lds\n" \
  "s_add_u32 m0, s21, 8192\n" \
  "s_add_u32 s16, s16, 4096\n" \
  "buffer_load_dwordx4 v10, s[40:43], s16 offen sc1 lds\n" \
  "s_add_u32 m0, s21, 12288\n" \
  "s_add_u32 s16, s16, 4096\n" \
  "buffer_load_dwordx4 v10, s[40:43], s16 offen sc1 lds\n" \
  "s_add_u32 m0, s21, 16384\n" \
  "s_add_u32 s16, s16, 4096\n" \
  "buffer_load_dwordx4 v10, s[40:43], s16 offen sc1 lds\n" \
  "s_add_u32 m0, s21, 20480\n" \
  "s_add_u32 s16, s16, 4096\n" \
  "buffer_load_dwordx4 v10, s[40:43], s16 offen sc1 lds\n" \
  "s_add_u32 m0, s21, 24576\n" \
  "s_add_u32 s16, s16, 4096\n" \
  "buffer_load_dwordx4 v10, s[40:43], s16 offen sc1 lds\n" \
  "s_add_u32 m0, s21, 28672\n" \
  "s_add_u32 s16, s16, 4096\n" \
  "buffer_load_dwordx4 v10, s[40:43], s16 offen sc1 lds\n" \
  "s_add_u32 m0, s21, 32768\n" \
  "s_add_u32 s16, s16, 4096\n" \
  "buffer_load_dwordx4 v10, s[40:43], s16 offen sc1 lds\n" \
  "s_add_u32 m0, s21, 36864\n" \
  "s_add_u32 s16, s16, 4096\n" \
  "buffer_load_dwordx4 v10, s[40:43], s16 offen sc1 lds\n" \
  "s_add_u32 m0, s21, 40960\n" \
  "s_add_u32 s16, s16, 4096\n" \
  "buffer_load_dwordx4 v10, s[40:43], s16 offen sc1 lds\n" \
  "s_add_u32 m0, s21, 45056\n" \
  "s_add_u32 s16, s16, 4096\n" \
  "buffer_load_dwordx4 v10, s[40:43], s16 offen sc1 lds\n" \
  "s_add_u32 m0, s21, 49152\n" \
  "s_add_u32 s16, s16, 4096\n" \
  "buffer_load_dwordx4 v10, s[40:43], s16 offen sc1 lds\n" \
  "s_waitcnt vmcnt(6)\n" \
  "s_barrier\n" \
  "s_add_u32 m0, s21, 53248\n" \
  "s_add_u32 s16, s16, 4096\n" \
  "buffer_load_dwordx4 v10, s[40:43], s16 offen sc1 lds\n" \
  "s_add_u32 m0, s21, 57344\n" \
  "s_add_u32 s16, s16, 4096\n" \
  "buffer_load_dwordx4 v10, s[40:43], s16 offen sc1 lds\n" \
  "s_add_u32 m0, s21, 61440\n" \
  "s_add_u32 s16, s16, 4096\n" \
  "buffer_load_dwordx4 v10, s[40:43], s16 offen sc1 lds\n" \
  "s_add_u32 m0, s21, 65536\n" \
  "s_add_u32 s16, s16, 4096\n" \
  "buffer_load_dwordx4 v10, s[40:43], s16 offen sc1 lds\n" \
  "s_add_u32 m0, s21, 69632\n" \
  "s_add_u32 s16, s16, 4096\n" \
  "buffer_load_dwordx4 v10, s[40:43], s16 offen sc1 lds\n" \
  "s_cmp_eq_u32 s3, 3\n" \
  "s_cselect_b32 s17, 1024, 0\n" \
  "s_sub_u32 s16, s16, s17\n" \
  "s_sub_u32 s21, s21, s17\n" \
  "s_add_u32 m0, s21, 73728\n" \
  "s_add_u32 s16, s16, 4096\n" \
  "buffer_load_dwordx4 v10, s[40:43], s16 offen sc1 lds\n" \
  "s_cmp_gt_u32 s6, 32\n" \
  "s_waitcnt vmcnt(6)\n" \
  "s_barrier\n" \
  "s_waitcnt vmcnt(0)\n" \
  "s_barrier\n" \
  "s_cbranch_scc1 .LMnost_%=\n" \
  "buffer_store_dwordx4 v[232:235], v10, s[40:43], s36 offen sc1\n" \
  "buffer_store_dwordx4 v[236:239], v10, s[40:43], s37 offen sc1\n" \
  "buffer_store_dwordx4 v[240:243], v10, s[40:43], s38 offen sc1\n" \
  "buffer_store_dwordx4 v[244:247], v10, s[40:43], s39 offen sc1\n" \
  "s_cmp_lt_u32 s3, 1\n" \
  "s_cbranch_scc0 .LMnost_%=\n" \
  "buffer_store_dwordx4 v[216:219], v10, s[40:43], s12 offen sc1\n" \
  "buffer_store_dwordx4 v[220:223], v10, s[40:43], s13 offen sc1\n" \
  "buffer_store_dwordx4 v[224:227], v10, s[40:43], s14 offen sc1\n" \
  "buffer_store_dwordx4 v[228:231], v10, s[40:43], s15 offen sc1\n" \
  ".LMnost_%=:\n" \
  "s_cmp_eq_u32 s3, 3\n" \
  "s_cbranch_scc0 .Lfin_%=\n" \
  "s_mov_b32 s26, s46\n" \
  "s_mov_b32 s27, s47\n" \
  "s_mov_b32 s8, s86\n" \
  "s_and_b32 s9, s87, 0xffff\n" \
  "s_mov_b32 s10, 5308416\n" \
  "s_mov_b32 s11, 0x20000\n" \
  "s_mov_b32 s29, 4\n" \
  ".Lp6_%=:\n" \
  "s_waitcnt vmcnt(48)\n" \
  "s_mov_b32 m0, s27\n" \
  "s_add_u32 s28, s26, 147456\n" \
  "buffer_load_dwordx4 v82, s[8:11], s26 offen lds\n" \
  "s_add_u32 m0, s27, 1024\n" \
  "s_nop 0\n" \
  "buffer_load_dwordx4 v83, s[8:11], s26 offen lds\n" \
  "s_add_u32 m0, s27, 2048\n" \
  "s_nop 0\n" \
  "buffer_load_dwordx4 v82, s[8:11], s28 offen lds\n" \
  "s_add_u32 m0, s27, 3072\n" \
  "s_nop 0\n" \
  "buffer_load_dwordx4 v83, s[8:11], s28 offen lds\n" \
  "s_add_u32 m0, s27, 4096\n" \
  "s_nop 0\n" \
  "buffer_load_dwordx4 v82, s[12:15], s26 offen lds\n" \
  "s_add_u32 m0, s27, 5120\n" \
  "s_nop 0\n" \
  "buffer_load_dwordx4 v83, s[12:15], s26 offen lds\n" \
  "s_add_u32 m0, s27, 6144\n" \
  "s_nop 0\n" \
  "buffer_load_dwordx4 v82, s[12:15], s28 offen lds\n" \
  "s_add_u32 m0, s27, 7168\n" \
  "s_nop 0\n" \
  "buffer_load_dwordx4 v83, s[12:15], s28 offen lds\n" \
  "s_add_u32 s26, s26, 294912\n" \
  "s_add_u32 s27, s27, 8192\n" \
  "s_cmp_eq_u32 s27, 114688\n" \
  "s_cselect_b32 s27, 81920, s27\n" \
  "s_cselect_b32 s26, s54, s26\n" \
  "s_sub_u32 s29, s29, 1\n" \
  "s_cmp_lg_u32 s29, 0\n" \
  "s_cbranch_scc1 .Lp6_%=\n" \
  "s_branch .Lfin_%=\n" \
  ".LM3a_%=:\n" \
  "v_mul_u32_u24 v86, 1024, v1\n" \
  "v_mul_u32_u24 v87, 128, v1\n" \
  "v_mov_b32 v80, 1\n" \
  "v_mov_b32 v84, 32\n" \
  "v_mov_b32 v81, 24\n" \
  "v_cmp_gt_u32 vcc, 6, v1\n" \
  "s_bfe_u32 s44, s2, 196608\n" \
  "s_mul_i32 s44, s44, 128\n" \
  "s_add_u32 s44, s44, 67108864\n" \
  "v_addc_co_u32 v81, vcc, 0, v81, vcc\n" \
  "s_and_b32 s45, s2, 15\n" \
  "s_mul_i32 s45, s45, 1024\n" \
  "s_add_u32 s45, s45, 67108864\n" \
  "s_sub_u32 s16, s24, 18\n" \
  "s_cmp_lt_u32 s16, 36\n" \
  "s_cselect_b32 s44, 67108896, s44\n" \
  "v_lshlrev_b32 v2, 2, v1\n" \
  "s_mov_b32 s16, 0x66777300\n" \
  "v_bfe_u32 v2, s16, v2, 4\n" \
  "v_sub_u32 v84, 32, v2\n" \
  "s_mov_b32 s17, 65536\n" \
  "s_mov_b32 s35, 0x40000\n" \
  "s_waitcnt vmcnt(0) lgkmcnt(0)\n" \
  "v_readlane_b32 s18, v168, 4\n" \
  "v_readlane_b32 s19, v169, 4\n" \
  "s_mul_hi_u32 s47, s18, 2135587861\n" \
  "s_mul_i32 s20, s18, 2654435769\n" \
  "s_add_u32 s47, s47, s20\n" \
  "s_mul_i32 s20, s19, 2135587861\n" \
  "s_add_u32 s47, s47, s20\n" \
  "s_mul_i32 s46, s18, 2135587861\n" \
  "s_add_u32 s46, s46, 3652509721\n" \
  "s_addc_u32 s47, s47, 1663821211\n" \
  "s_mov_b64 exec, 15\n" \
  ".Ltag_%=:\n" \
  "v_cmp_ne_u64 vcc, s[46:47], v[168:169]\n" \
  "s_and_b64 vcc, exec, vcc\n" \
  "s_cbranch_vccz .Ltagx_%=\n" \
  "buffer_load_dwordx2 v[168:169], v170, s[40:43], s17 offen sc1\n" \
  "s_sub_u32 s35, s35, 1\n" \
  "s_waitcnt vmcnt(0)\n" \
  "s_cbranch_scc0 .Ltag_%=\n" \
  ".Ltagx_%=:\n" \
  "s_mov_b64 exec, -1\n" \
  "s_barrier\n" \
  "s_mov_b64 exec, 65535\n" \
  "buffer_atomic_add v80, v86, s[40:43], s44 offen\n" \
  "s_mov_b64 exec, -1\n" \
  "s_cmp_gt_u32 s6, 32\n" \
  "s_cbranch_scc1 .LMidle_%=\n" \
  ".Lx7_%=:\n" \
  "s_lshl_b32 s18, s22, 6\n" \
  "v_add_u32 v2, s18, v1\n" \
  "v_mul_u32_u24 v3, 0xdd7, v2\n" \
  "v_lshrrev_b32 v3, 17, v3\n" \
  "v_lshl_add_u32 v4, v3, 6, s6\n" \
  "v_sub_u32 v5, 0, v4\n" \
  "v_cmp_gt_u32 vcc, 32, v3\n" \
  "v_and_b32 v5, 0x7ff, v5\n" \
  "v_mul_u32_u24 v3, 37, v3\n" \
  "v_cndmask_b32 v4, v5, v4, vcc\n" \
  "v_sub_u32 v2, v2, v3\n" \
  "v_mul_u32_u24 v4, 0x242, v4\n" \
  "v_and_b32 v4, -16, v4\n" \
  "v_lshl_add_u32 v2, v2, 4, v4\n" \
  "s_lshl_b32 s18, s22, 10\n" \
  "s_add_u32 m0, s18, 81920\n" \
  "s_add_u32 s22, s22, 4\n" \
  "buffer_load_dwordx4 v2, s[8:11], 0 offen lds\n" \
  "s_add_u32 m0, s18, 119808\n" \
  "s_sub_u32 s18, s22, 37\n" \
  "buffer_load_dwordx4 v2, s[12:15], 0 offen lds\n" \
  "s_cselect_b32 s22, s22, s18\n" \
  "s_add_u32 s23, s23, 4\n" \
  "s_cmp_lt_u32 s23, 37\n" \
  "s_cbranch_scc1 .Lx7_%=\n" \
  "s_mov_b64 exec, 255\n" \
  "s_mov_b32 s35, 0x20000\n" \
  ".Lq8_%=:\n" \
  "v_mov_b32 v176, 0\n" \
  "buffer_atomic_add v176, v87, s[40:43], s45 offen sc0\n" \
  "s_waitcnt vmcnt(0)\n" \
  "v_cmp_gt_u32 vcc, v84, v176\n" \
  "s_and_b64 exec, exec, vcc\n" \
  "s_cbranch_scc0 .Lq8_%=x\n" \
  "s_sleep 2\n" \
  "s_sub_u32 s35, s35, 1\n" \
  "s_cbranch_scc0 .Lq8_%=\n" \
  ".Lq8_%=x:\n" \
  "s_mov_b64 exec, -1\n" \
  "s_barrier\n" \
  "s_lshr_b32 s16, s5, 3\n" \
  "s_lshl_b32 s16, s16, 3\n" \
  "s_add_u32 s16, s16, s4\n" \
  "s_mul_i32 s0, s16, 76800\n" \
  "s_add_u32 s0, s0, 7340032\n" \
  "s_lshl_b32 s16, s16, 3\n" \
  "s_and_b32 s17, s5, 7\n" \
  "s_add_u32 s16, s16, s17\n" \
  "s_lshr_b32 s17, s16, 1\n" \
  "s_mul_i32 s1, s17, 18432\n" \
  "s_and_b32 s17, s16, 1\n" \
  "s_lshl_b32 s17, s17, 7\n" \
  "s_add_u32 s1, s1, s17\n" \
  "v_and_b32 v24, 7, v1\n" \
  "v_lshrrev_b32 v26, 3, v1\n" \
  "s_lshl_b32 s17, s3, 3\n" \
  "v_add_u32 v26, s17, v26\n" \
  "v_lshlrev_b32 v27, 4, v26\n" \
  "v_lshlrev_b32 v26, 8, v26\n" \
  "v_lshl_add_u32 v26, v24, 4, v26\n" \
  "v_lshlrev_b32 v24, 5, v24\n" \
  "v_add_u32 v24, 76800, v24\n" \
  "s_lshl_b32 s17, s3, 10\n" \
  "s_add_u32 s17, s17, 155648\n" \
  "v_add_u32 v25, s17, v10\n" \
  "s_and_b32 s16, s4, 3\n" \
  "s_and_b32 s17, s4, 4\n" \
  "s_lshr_b32 s18, s5, 3\n" \
  "s_add_u32 s17, s17, s18\n" \
  "s_and_b32 s18, s5, 7\n" \
  "s_lshr_b32 s19, s18, 2\n" \
  "s_and_b32 s20, s18, 3\n" \
  "s_lshr_b32 s21, s3, 2\n" \
  "s_and_b32 s22, s3, 3\n" \
  "s_cmp_lt_u32 s3, 4\n" \
  "s_cselect_b32 s48, s86, s88\n" \
  "s_cselect_b32 s49, s87, s89\n" \
  "s_mov_b32 s50, s96\n" \
  "s_mov_b32 s51, s97\n" \
  "s_mov_b32 s52, s98\n" \
  "s_mov_b32 s53, s99\n" \
  "s_lshl_b32 s23, s16, 1\n" \
  "s_add_u32 s23, s23, s19\n" \
  "s_mul_i32 s54, s23, 1152\n" \
  "s_lshl_b32 s24, s20, 8\n" \
  "s_add_u32 s54, s54, s24\n" \
  "s_lshl_b32 s57, s3, 10\n" \
  "s_lshl_b32 s24, s16, 3\n" \
  "s_add_u32 s24, s24, s18\n" \
  "s_mul_i32 s55, s24, 40960\n" \
  "s_add_u32 s55, s55, s57\n" \
  "s_add_u32 s55, s55, 48234496\n" \
  "s_lshl_b32 s24, s17, 3\n" \
  "s_add_u32 s25, s24, s3\n" \
  "s_lshl_b32 s56, s25, 2\n" \
  "s_add_u32 s56, s56, s16\n" \
  "s_lshl_b32 s56, s56, 6\n" \
  "s_add_u32 s56, s56, 262144\n" \
  "s_mul_i32 s60, s25, 36864\n" \
  "s_add_u32 s60, s60, 13631488\n" \
  "s_lshl_b32 s58, s4, 11\n" \
  "s_add_u32 s58, s58, 147456\n" \
  "s_add_u32 s25, s24, s23\n" \
  "s_lshl_b32 s25, s25, 3\n" \
  "s_add_u32 s25, s25, s3\n" \
  "s_mul_i32 s61, s25, 36864\n" \
  "s_add_u32 s61, s61, 16777216\n" \
  "s_lshl_b32 s66, s25, 6\n" \
  "s_add_u32 s66, s66, 229376\n" \
  "s_lshr_b32 s26, s20, 1\n" \
  "s_lshl_b32 s27, s26, 11\n" \
  "s_and_b32 s28, s20, 1\n" \
  "s_lshl_b32 s28, s28, 9\n" \
  "s_add_u32 s62, s61, s27\n" \
  "s_add_u32 s62, s62, s28\n" \
  "s_add_u32 s62, s62, 16384\n" \
  "s_lshl_b32 s27, s20, 11\n" \
  "s_add_u32 s61, s61, s27\n" \
  "s_lshl_b32 s25, s16, 1\n" \
  "s_add_u32 s25, s25, s24\n" \
  "s_add_u32 s25, s25, s21\n" \
  "s_lshl_b32 s25, s25, 1\n" \
  "s_add_u32 s25, s25, s19\n" \
  "s_lshl_b32 s25, s25, 2\n" \
  "s_add_u32 s25, s25, s22\n" \
  "s_mul_i32 s63, s25, 36864\n" \
  "s_add_u32 s63, s63, 16777216\n" \
  "s_lshl_b32 s67, s25, 6\n" \
  "s_add_u32 s67, s67, 229376\n" \
  "s_lshl_b32 s27, s21, 10\n" \
  "s_add_u32 s63, s63, s27\n" \
  "s_xor_b32 s64, s63, 1024\n" \
  "s_lshl_b32 s29, s16, 2\n" \
  "s_add_u32 s25, s29, s20\n" \
  "s_mul_i32 s65, s25, 335872\n" \
  "s_add_u32 s65, s65, s57\n" \
  "s_add_u32 s65, s65, 41943040\n" \
  "s_add_u32 s25, s24, s18\n" \
  "s_lshl_b32 s25, s25, 5\n" \
  "s_add_u32 s25, s25, s3\n" \
  "s_lshl_b32 s27, s25, 13\n" \
  "s_add_u32 s27, s27, 50331648\n" \
  "s_lshl_b32 s25, s25, 5\n" \
  "s_add_u32 s25, s25, 163840\n" \
  "s_add_u32 s29, s16, 0\n" \
  "s_and_b32 s29, s29, 3\n" \
  "s_lshl_b32 s73, s29, 16\n" \
  "s_add_u32 s73, s73, s27\n" \
  "s_lshl_b32 s29, s29, 8\n" \
  "s_add_u32 s76, s29, s25\n" \
  "s_add_u32 s29, s16, 1\n" \
  "s_and_b32 s29, s29, 3\n" \
  "s_lshl_b32 s68, s29, 16\n" \
  "s_add_u32 s68, s68, s27\n" \
  "s_lshl_b32 s29, s29, 8\n" \
  "s_add_u32 s78, s29, s25\n" \
  "s_add_u32 s29, s16, 2\n" \
  "s_and_b32 s29, s29, 3\n" \
  "s_lshl_b32 s69, s29, 16\n" \
  "s_add_u32 s69, s69, s27\n" \
  "s_lshl_b32 s29, s29, 8\n" \
  "s_add_u32 s79, s29, s25\n" \
  "s_add_u32 s29, s16, 3\n" \
  "s_and_b32 s29, s29, 3\n" \
  "s_lshl_b32 s70, s29, 16\n" \
  "s_add_u32 s70, s70, s27\n" \
  "s_lshl_b32 s29, s29, 8\n" \
  "s_add_u32 s59, s29, s25\n" \
  "s_sub_u32 s29, s22, s16\n" \
  "s_sub_u32 s29, s29, 1\n" \
  "s_and_b32 s29, s29, 3\n" \
  "s_add_u32 s74, s29, 1\n" \
  "s_lshr_b32 s74, s74, 2\n" \
  "s_lshl_b32 s71, s22, 7\n" \
  "s_add_u32 s71, s71, s29\n" \
  "s_lshl_b32 s71, s71, 9\n" \
  "s_add_u32 s71, s71, s27\n" \
  "s_add_u32 s71, s71, 3072\n" \
  "s_lshl_b32 s72, s22, 8\n" \
  "s_add_u32 s72, s72, s25\n" \
  "s_lshl_b32 s25, s17, 1\n" \
  "s_add_u32 s25, s25, s19\n" \
  "s_lshl_b32 s25, s25, 2\n" \
  "s_add_u32 s25, s25, s22\n" \
  "s_lshl_b32 s25, s25, 1\n" \
  "s_add_u32 s25, s25, s21\n" \
  "s_mul_i32 s27, s25, 18432\n" \
  "s_add_u32 s27, s27, 10485760\n" \
  "s_lshl_b32 s29, s20, 2\n" \
  "s_add_u32 s29, s29, s16\n" \
  "s_lshl_b32 s75, s29, 10\n" \
  "s_add_u32 s75, s75, s27\n" \
  "s_lshl_b32 s77, s20, 9\n" \
  "s_add_u32 s77, s77, s27\n" \
  "s_add_u32 s77, s77, 16384\n" \
  "s_lshr_b32 s31, s2, 1\n" \
  "s_lshl_b32 s31, s31, 13\n" \
  "s_and_b32 s27, s2, 1\n" \
  "s_cmp_eq_u32 s27, s21\n" \
  "s_cselect_b32 s31, s31, 0x40000000\n" \
  "s_cmp_lt_u32 s2, 216\n" \
  "s_cselect_b32 s31, s31, 0x40000000\n" \
  "s_or_b32 s31, s31, s22\n" \
  "v_lshrrev_b32 v2, 3, v1\n" \
  "s_lshl_b32 s27, s22, 3\n" \
  "v_add_u32 v2, s27, v2\n" \
  "v_mul_u32_u24 v8, 0x2400, v2\n" \
  "v_bfe_u32 v3, v1, 4, 1\n" \
  "s_and_b32 s27, s22, 1\n" \
  "s_lshl_b32 s27, s27, 1\n" \
  "v_or_b32 v3, s27, v3\n" \
  "v_lshlrev_b32 v3, 5, v3\n" \
  "v_and_b32 v4, 0x70, v10\n" \
  "v_xor_b32 v3, v3, v4\n" \
  "v_add_u32 v8, v8, v3\n" \
  "v_mov_b32 v9, v10\n" \
  "v_lshlrev_b32 v11, 3, v1\n" \
  "v_lshrrev_b32 v2, 4, v1\n" \
  "v_bfe_u32 v3, v1, 2, 2\n" \
  "v_lshl_add_u32 v4, v2, 3, v3\n" \
  "v_lshlrev_b32 v4, 7, v4\n" \
  "v_and_b32 v5, 24, v11\n" \
  "v_or_b32 v4, v4, v5\n" \
  "v_bfe_u32 v5, v1, 3, 2\n" \
  "v_lshl_or_b32 v12, v5, 5, v4\n" \
  "v_xor_b32 v13, 32, v12\n" \
  "v_xor_b32 v14, 64, v12\n" \
  "v_xor_b32 v15, 0x60, v12\n" \
  "v_and_b32 v3, 15, v1\n" \
  "v_lshrrev_b32 v4, 5, v1\n" \
  "v_lshl_add_u32 v4, v4, 4, v3\n" \
  "v_and_b32 v5, 1, v2\n" \
  "v_lshlrev_b32 v5, 3, v5\n" \
  "v_lshl_or_b32 v16, v4, 4, v5\n" \
  "s_lshl_b32 s27, s21, 12\n" \
  "v_add_u32 v17, s27, v10\n" \
  "s_mul_i32 s27, s3, 5120\n" \
  "s_add_u32 s27, s27, 98304\n" \
  "v_add_u32 v18, s27, v10\n" \
  "s_xor_b32 s27, s3, 4\n" \
  "s_mul_i32 s27, s27, 5120\n" \
  "s_add_u32 s27, s27, 98304\n" \
  "v_add_u32 v19, s27, v10\n" \
  "s_and_b32 s27, s25, 3\n" \
  "s_lshl_b32 s27, s27, 9\n" \
  "s_lshr_b32 s25, s25, 2\n" \
  "s_add_u32 s25, s25, s27\n" \
  "s_mul_i32 s25, s25, 1152\n" \
  "v_mul_u32_u24 v3, 0x9000, v3\n" \
  "v_add_u32 v3, s25, v3\n" \
  "s_lshl_b32 s29, s29, 5\n" \
  "v_lshl_add_u32 v4, v2, 3, s29\n" \
  "s_lshl_b32 s27, s20, 4\n" \
  "s_add_u32 s27, s27, 512\n" \
  "v_lshl_add_u32 v5, v2, 2, s27\n" \
  "v_lshl_add_u32 v20, v4, 1, v3\n" \
  "v_lshl_add_u32 v21, v5, 1, v3\n" \
  "s_movk_i32 s27, 11520\n" \
  "v_lshl_add_u32 v22, v4, 2, s27\n" \
  "v_lshl_add_u32 v23, v5, 2, s27\n" \
  "s_barrier\n" \
  "s_mov_b64 exec, 1\n" \
  "v_mov_b32 v92, 0\n" \
  "buffer_atomic_add v92, off, s[40:43], s45 offset:32 sc0\n" \
  "s_mov_b64 exec, -1\n" \
  "s_mul_i32 s26, s6, 6\n" \
  "s_add_u32 s26, s26, s7\n" \
  "s_mul_i32 s26, s26, 5\n" \
  "s_add_u32 s26, s26, s3\n" \
  "s_lshl_b32 s27, s3, 12\n" \
  "s_add_u32 s27, s27, 21248\n" \
  "s_sub_u32 s26, s26, 3\n" \
  "s_mov_b32 s27, 21248\n" \
  ".LM3r_%=:\n" \
  "s_cmp_lt_u32 s26, 1296\n" \
  "s_cbranch_scc1 .Lr9_%=d\n" \
  "s_sub_u32 s16, s26, 1296\n" \
  "s_and_b32 s17, s16, 1\n" \
  "s_cselect_b32 s8, s88, s86\n" \
  "s_cselect_b32 s9, s89, s87\n" \
  "s_lshr_b32 s17, s16, 4\n" \
  "s_mul_i32 s17, s17, 294912\n" \
  "s_bfe_u32 s16, s16, 0x30001\n" \
  "s_mul_i32 s16, s16, 1152\n" \
  "s_add_u32 s17, s17, s16\n" \
  "s_add_u32 s17, s17, 1024\n" \
  "s_movk_i32 s18, 9216\n" \
  "s_branch .Lr9_%=j\n" \
  ".Lr9_%=d:\n" \
  "s_mul_i32 s16, s26, 7282\n" \
  "s_lshr_b32 s16, s16, 16\n" \
  "s_mul_i32 s16, s16, 35712\n" \
  "s_lshl_b32 s17, s26, 7\n" \
  "s_add_u32 s17, s17, s16\n" \
  "s_mov_b32 s8, s90\n" \
  "s_mov_b32 s9, s91\n" \
  "s_movk_i32 s18, 1152\n" \
  ".Lr9_%=j:\n" \
  "s_and_b32 s9, s9, 0xffff\n" \
  "s_mov_b32 s10, 5308416\n" \
  "s_mov_b32 s11, 0x20000\n" \
  "v_lshrrev_b32 v28, 3, v1\n" \
  "v_mul_u32_u24 v28, s18, v28\n" \
  "v_and_b32 v29, 0x70, v10\n" \
  "v_add_u32 v28, v28, v29\n" \
  "s_lshl_b32 s18, s18, 3\n" \
  "s_add_u32 m0, s27, 0\n" \
  "s_nop 0\n" \
  "buffer_load_dwordx4 v28, s[8:11], s17 offen lds\n" \
  "s_add_u32 m0, s27, 1024\n" \
  "s_add_u32 s17, s17, s18\n" \
  "buffer_load_dwordx4 v28, s[8:11], s17 offen lds\n" \
  "s_add_u32 m0, s27, 2048\n" \
  "s_add_u32 s17, s17, s18\n" \
  "buffer_load_dwordx4 v28, s[8:11], s17 offen lds\n" \
  "s_add_u32 m0, s27, 3072\n" \
  "s_add_u32 s17, s17, s18\n" \
  "buffer_load_dwordx4 v28, s[8:11], s17 offen lds\n" \
  "s_add_u32 s26, s26, 1\n" \
  "s_add_u32 s27, s27, 4096\n" \
  "s_cmp_eq_u32 s27, 37632\n" \
  "s_cselect_b32 s27, 63744, s27\n" \
  "s_cmp_lg_u32 s27, 67840\n" \
  "s_cbranch_scc1 .LM3r_%=\n" \
  "s_mov_b32 s8, s86\n" \
  "s_and_b32 s9, s87, 0xffff\n" \
  "s_mov_b32 s10, 5308416\n" \
  "s_mov_b32 s11, 0x20000\n" \
  "s_mov_b32 s12, s88\n" \
  "s_and_b32 s13, s89, 0xffff\n" \
  "s_mov_b32 s14, 5308416\n" \
  "s_mov_b32 s15, 0x20000\n" \
  "s_and_b32 s17, s4, 4\n" \
  "s_lshr_b32 s18, s5, 3\n" \
  "s_add_u32 s17, s17, s18\n" \
  "s_mul_i32 s17, s17, 4\n" \
  "s_lshr_b32 s17, s17, 3\n" \
  "s_mul_i32 s26, s17, 294912\n" \
  "s_add_u32 s26, s26, s54\n" \
  "s_lshl_b32 s27, s17, 13\n" \
  "s_add_u32 s27, s27, 81920\n" \
  "v_subrev_u32 v83, 147456, v8\n" \
  "v_subrev_u32 v82, 73728, v83\n" \
  "v_xor_b32 v82, 64, v82\n" \
  "s_waitcnt vmcnt(20)\n" \
  "v_readfirstlane_b32 s16, v92\n" \
  "s_cmp_eq_u32 s16, 36\n" \
  "s_cbranch_scc1 .LM3g_%=\n" \
  "s_mov_b64 exec, 1\n" \
  "s_mov_b32 s35, 0x20000\n" \
  ".Lq10_%=:\n" \
  "v_mov_b32 v176, 0\n" \
  "buffer_atomic_add v176, v87, s[40:43], s45 offen offset:32 sc0\n" \
  "s_waitcnt vmcnt(0)\n" \
  "v_cmp_gt_u32 vcc, 36, v176\n" \
  "s_and_b64 exec, exec, vcc\n" \
  "s_cbranch_scc0 .Lq10_%=x\n" \
  "s_sleep 2\n" \
  "s_sub_u32 s35, s35, 1\n" \
  "s_cbranch_scc0 .Lq10_%=\n" \
  ".Lq10_%=x:\n" \
  "s_mov_b64 exec, -1\n" \
  ".LM3g_%=:\n" \
  "s_movk_i32 s16, 19840\n" \
  "buffer_load_dwordx4 v[160:163], v27, s[40:43], s16 offen offset:1152 sc1\n" \
  "buffer_load_dwordx4 v[164:167], v27, s[40:43], s16 offen sc1\n" \
  "s_cmp_eq_u32 s3, 3\n" \
  "s_cbranch_scc0 .Lo11_%=\n" \
  "buffer_load_dwordx4 v[168:171], v27, s[40:43], s16 offen offset:1792 sc1\n" \
  "buffer_load_dwordx4 v[172:175], v27, s[40:43], s16 offen offset:640 sc1\n" \
  ".Lo11_%=:\n" \
  "s_barrier\n" \
  "s_mov_b32 s46, s26\n" \
  "s_mov_b32 s47, s27\n" \
  "s_mul_i32 s26, s6, 6\n" \
  "s_add_u32 s26, s26, s7\n" \
  "s_mul_i32 s26, s26, 5\n" \
  "s_add_u32 s26, s26, s3\n" \
  "s_lshl_b32 s27, s3, 12\n" \
  "s_add_u32 s27, s27, 21248\n" \
  "v_lshrrev_b32 v28, 4, v1\n" \
  "v_bfe_u32 v29, v1, 2, 2\n" \
  "v_and_b32 v30, 3, v1\n" \
  "s_cmp_lt_u32 s26, 1296\n" \
  "s_cbranch_scc1 .Lt12_%=d\n" \
  "s_sub_u32 s16, s26, 1296\n" \
  "s_bfe_u32 s17, s16, 0x30001\n" \
  "s_mul_i32 s17, s17, 163840\n" \
  "s_lshr_b32 s18, s16, 4\n" \
  "s_lshl_b32 s18, s18, 1\n" \
  "s_and_b32 s16, s16, 1\n" \
  "s_add_u32 s16, s16, s18\n" \
  "s_lshl_b32 s16, s16, 10\n" \
  "s_add_u32 s16, s16, s17\n" \
  "s_add_u32 s36, s16, 48234496\n" \
  "s_add_u32 s37, s36, 40960\n" \
  "s_add_u32 s38, s36, 81920\n" \
  "s_add_u32 s39, s36, 122880\n" \
  "s_mov_b32 s16, 3\n" \
  "s_mov_b32 s17, 3\n" \
  "s_mov_b32 s18, 32\n" \
  "s_movk_i32 s19, 512\n" \
  "s_branch .Lt12_%=j\n" \
  ".Lt12_%=d:\n" \
  "s_mul_i32 s16, s26, 7282\n" \
  "s_lshr_b32 s16, s16, 16\n" \
  "s_mul_i32 s17, s16, 9\n" \
  "s_sub_u32 s17, s26, s17\n" \
  "s_mul_i32 s19, s16, 3641\n" \
  "s_lshr_b32 s19, s19, 16\n" \
  "s_mul_i32 s20, s19, 18\n" \
  "s_sub_u32 s20, s16, s20\n" \
  "s_lshr_b32 s18, s19, 1\n" \
  "s_and_b32 s19, s19, 1\n" \
  "s_lshr_b32 s21, s20, 2\n" \
  "s_and_b32 s22, s20, 1\n" \
  "s_lshl_b32 s22, s22, 2\n" \
  "s_add_u32 s21, s21, s22\n" \
  "s_bitcmp1_b32 s20, 1\n" \
  "s_cselect_b32 s22, 10, 0\n" \
  "s_add_u32 s21, s21, s22\n" \
  "s_sub_u32 s22, s20, 8\n" \
  "s_cmp_lt_u32 s20, 16\n" \
  "s_cselect_b32 s21, s21, s22\n" \
  "s_mul_i32 s16, s18, 1343488\n" \
  "s_lshl_b32 s19, s19, 12\n" \
  "s_add_u32 s16, s16, s19\n" \
  "s_add_u32 s16, s16, 41943040\n" \
  "s_and_b32 s23, s21, 1\n" \
  "s_sub_u32 s24, s21, s23\n" \
  "s_lshl_b32 s22, s24, 1\n" \
  "s_add_u32 s22, s22, s23\n" \
  "s_mul_i32 s19, s24, 7\n" \
  "s_add_u32 s19, s19, s23\n" \
  "s_add_u32 s20, s19, 10\n" \
  "s_mul_i32 s24, s24, 5\n" \
  "s_add_u32 s24, s24, s23\n" \
  "s_add_u32 s24, s24, 20\n" \
  "s_add_u32 s23, s22, 90\n" \
  "s_cmp_lt_u32 s21, 10\n" \
  "s_cselect_b32 s19, s19, s24\n" \
  "s_cselect_b32 s20, s20, s23\n" \
  "s_add_u32 s22, s22, 126\n" \
  "s_cmp_eq_u32 s17, 8\n" \
  "s_cbranch_scc1 .Lt12_%=e\n" \
  "s_lshr_b32 s21, s17, 1\n" \
  "s_mul_i32 s21, s21, 335872\n" \
  "s_add_u32 s16, s16, s21\n" \
  "s_and_b32 s17, s17, 1\n" \
  "s_lshl_b32 s17, s17, 1\n" \
  "s_sub_u32 s17, s17, s18\n" \
  "s_add_u32 s21, s17, 0\n" \
  "s_and_b32 s21, s21, 3\n" \
  "s_add_u32 s23, s19, 4\n" \
  "s_cmp_eq_u32 s21, 1\n" \
  "s_cselect_b32 s23, s19, s23\n" \
  "s_cmp_eq_u32 s21, 3\n" \
  "s_cselect_b32 s23, s20, s23\n" \
  "s_cmp_eq_u32 s21, 0\n" \
  "s_cselect_b32 s23, s22, s23\n" \
  "s_andn2_b32 s24, s23, 3\n" \
  "s_add_u32 s24, s24, s23\n" \
  "s_lshl_b32 s24, s24, 10\n" \
  "s_add_u32 s36, s24, s16\n" \
  "s_add_u32 s23, s23, 2\n" \
  "s_andn2_b32 s24, s23, 3\n" \
  "s_add_u32 s24, s24, s23\n" \
  "s_lshl_b32 s24, s24, 10\n" \
  "s_add_u32 s37, s24, s16\n" \
  "s_add_u32 s21, s17, 1\n" \
  "s_and_b32 s21, s21, 3\n" \
  "s_add_u32 s23, s19, 4\n" \
  "s_cmp_eq_u32 s21, 1\n" \
  "s_cselect_b32 s23, s19, s23\n" \
  "s_cmp_eq_u32 s21, 3\n" \
  "s_cselect_b32 s23, s20, s23\n" \
  "s_cmp_eq_u32 s21, 0\n" \
  "s_cselect_b32 s23, s22, s23\n" \
  "s_andn2_b32 s24, s23, 3\n" \
  "s_add_u32 s24, s24, s23\n" \
  "s_lshl_b32 s24, s24, 10\n" \
  "s_add_u32 s38, s24, s16\n" \
  "s_add_u32 s23, s23, 2\n" \
  "s_andn2_b32 s24, s23, 3\n" \
  "s_add_u32 s24, s24, s23\n" \
  "s_lshl_b32 s24, s24, 10\n" \
  "s_add_u32 s39, s24, s16\n" \
  "s_mov_b32 s16, 2\n" \
  "s_mov_b32 s17, 4\n" \
  "s_mov_b32 s18, 8\n" \
  "s_movk_i32 s19, 2048\n" \
  "s_branch .Lt12_%=j\n" \
  ".Lt12_%=e:\n" \
  "s_add_u32 s23, s19, 8\n" \
  "s_andn2_b32 s24, s23, 3\n" \
  "s_add_u32 s24, s24, s23\n" \
  "s_lshl_b32 s24, s24, 10\n" \
  "s_add_u32 s36, s24, s16\n" \
  "s_add_u32 s37, s36, 335872\n" \
  "s_add_u32 s38, s36, 671744\n" \
  "s_add_u32 s39, s36, 1007616\n" \
  "s_mov_b32 s16, 2\n" \
  "s_mov_b32 s17, 3\n" \
  "s_mov_b32 s18, 32\n" \
  "s_movk_i32 s19, 2048\n" \
  ".Lt12_%=j:\n" \
  "v_lshlrev_b32 v28, s16, v28\n" \
  "v_add_u32 v28, v28, v29\n" \
  "v_lshlrev_b32 v30, s17, v30\n" \
  "v_lshl_add_u32 v28, v28, 7, v30\n" \
  "v_add_u32 v28, s27, v28\n" \
  "v_add_u32 v29, s19, v28\n" \
  "v_add_u32 v30, s18, v28\n" \
  "v_add_u32 v31, s18, v29\n" \
  "s_waitcnt vmcnt(0)\n" \
  "ds_read_b64_tr_b16 v[232:233], v28\n" \
  "ds_read_b64_tr_b16 v[234:235], v29\n" \
  "ds_read_b64_tr_b16 v[236:237], v30\n" \
  "ds_read_b64_tr_b16 v[238:239], v31\n" \
  "ds_read_b64_tr_b16 v[240:241], v28 offset:64\n" \
  "ds_read_b64_tr_b16 v[242:243], v29 offset:64\n" \
  "ds_read_b64_tr_b16 v[244:245], v30 offset:64\n" \
  "ds_read_b64_tr_b16 v[246:247], v31 offset:64\n" \
  "s_waitcnt lgkmcnt(0)\n" \
  ".LM3c_%=:\n" \
  "s_waitcnt vmcnt(0)\n" \
  "v_lshlrev_b32 v144, 16, v160\n" \
  "v_and_b32 v145, 0xffff0000, v160\n" \
  "v_lshlrev_b32 v152, 16, v164\n" \
  "v_and_b32 v153, 0xffff0000, v164\n" \
  "v_lshlrev_b32 v146, 16, v161\n" \
  "v_and_b32 v147, 0xffff0000, v161\n" \
  "v_lshlrev_b32 v154, 16, v165\n" \
  "v_and_b32 v155, 0xffff0000, v165\n" \
  "v_lshlrev_b32 v148, 16, v162\n" \
  "v_and_b32 v149, 0xffff0000, v162\n" \
  "v_lshlrev_b32 v156, 16, v166\n" \
  "v_and_b32 v157, 0xffff0000, v166\n" \
  "v_lshlrev_b32 v150, 16, v163\n" \
  "v_and_b32 v151, 0xffff0000, v163\n" \
  "v_lshlrev_b32 v158, 16, v167\n" \
  "v_and_b32 v159, 0xffff0000, v167\n" \
  "v_pk_add_f32 v[144:145], v[144:145], 1.0 op_sel_hi:[1,0]\n" \
  "v_pk_add_f32 v[146:147], v[146:147], 1.0 op_sel_hi:[1,0]\n" \
  "v_pk_add_f32 v[148:149], v[148:149], 1.0 op_sel_hi:[1,0]\n" \
  "v_pk_add_f32 v[150:151], v[150:151], 1.0 op_sel_hi:[1,0]\n" \
  "v_lshlrev_b32 v124, 16, v168\n" \
  "v_and_b32 v125, 0xffff0000, v168\n" \
  "v_lshlrev_b32 v132, 16, v172\n" \
  "v_and_b32 v133, 0xffff0000, v172\n" \
  "v_lshlrev_b32 v126, 16, v169\n" \
  "v_and_b32 v127, 0xffff0000, v169\n" \
  "v_lshlrev_b32 v134, 16, v173\n" \
  "v_and_b32 v135, 0xffff0000, v173\n" \
  "v_lshlrev_b32 v128, 16, v170\n" \
  "v_and_b32 v129, 0xffff0000, v170\n" \
  "v_lshlrev_b32 v136, 16, v174\n" \
  "v_and_b32 v137, 0xffff0000, v174\n" \
  "v_lshlrev_b32 v130, 16, v171\n" \
  "v_and_b32 v131, 0xffff0000, v171\n" \
  "v_lshlrev_b32 v138, 16, v175\n" \
  "v_and_b32 v139, 0xffff0000, v175\n" \
  "v_pk_add_f32 v[124:125], v[124:125], 1.0 op_sel_hi:[1,0]\n" \
  "v_pk_add_f32 v[126:127], v[126:127], 1.0 op_sel_hi:[1,0]\n" \
  "v_pk_add_f32 v[128:129], v[128:129], 1.0 op_sel_hi:[1,0]\n" \
  "v_pk_add_f32 v[130:131], v[130:131], 1.0 op_sel_hi:[1,0]\n" \
  "s_mul_i32 s16, s6, 6\n" \
  "s_add_u32 s16, s16, s7\n" \
  "s_and_b32 s16, s16, 7\n" \
  "s_mul_i32 s16, s16, 128\n" \
  "s_add_u32 s16, s16, 67174400\n" \
  "s_add_u32 s45, s45, 65536\n" \
  "s_cmp_gt_u32 s6, 32\n" \
  "s_barrier\n" \
  "s_cbranch_scc1 .LM3d_%=\n" \
  "s_mov_b64 exec, 65535\n" \
  "buffer_atomic_add v80, v86, s[40:43], s16 offen\n" \
  "s_mov_b64 exec, -1\n" \
  ".LM3d_%=:\n" \
  "s_mov_b64 exec, 255\n" \
  "s_mov_b32 s35, 0x20000\n" \
  ".Lq13_%=:\n" \
  "v_mov_b32 v176, 0\n" \
  "buffer_atomic_add v176, v87, s[40:43], s45 offen sc0\n" \
  "s_waitcnt vmcnt(0)\n" \
  "v_cmp_gt_u32 vcc, v81, v176\n" \
  "s_and_b64 exec, exec, vcc\n" \
  "s_cbranch_scc0 .Lq13_%=x\n" \
  "s_sleep 2\n" \
  "s_sub_u32 s35, s35, 1\n" \
  "s_cbranch_scc0 .Lq13_%=\n" \
  ".Lq13_%=x:\n" \
  "s_mov_b64 exec, -1\n" \
  "s_branch .LMb2_%=\n" \
  ".LMidle_%=:\n" \
  "s_lshl_b32 s25, s4, 3\n" \
  "s_add_u32 s25, s25, s5\n" \
  "s_sub_u32 s25, s25, 30\n" \
  "s_mul_i32 s26, s25, 8\n" \
  "s_add_u32 s26, s26, s3\n" \
  "s_add_u32 s26, s26, 990\n" \
  "s_mul_i32 s27, s3, 58\n" \
  "s_add_u32 s27, s27, s25\n" \
  "s_add_u32 s27, s27, 1454\n" \
  "s_lshl_b32 s28, s3, 13\n" \
  "s_add_u32 s29, s28, 4096\n" \
  "s_cmp_lt_u32 s26, 1296\n" \
  "s_cbranch_scc1 .Lr15_%=d\n" \
  "s_sub_u32 s16, s26, 1296\n" \
  "s_and_b32 s17, s16, 1\n" \
  "s_cselect_b32 s8, s88, s86\n" \
  "s_cselect_b32 s9, s89, s87\n" \
  "s_lshr_b32 s17, s16, 4\n" \
  "s_mul_i32 s17, s17, 294912\n" \
  "s_bfe_u32 s16, s16, 0x30001\n" \
  "s_mul_i32 s16, s16, 1152\n" \
  "s_add_u32 s17, s17, s16\n" \
  "s_add_u32 s17, s17, 1024\n" \
  "s_movk_i32 s18, 9216\n" \
  "s_branch .Lr15_%=j\n" \
  ".Lr15_%=d:\n" \
  "s_mul_i32 s16, s26, 7282\n" \
  "s_lshr_b32 s16, s16, 16\n" \
  "s_mul_i32 s16, s16, 35712\n" \
  "s_lshl_b32 s17, s26, 7\n" \
  "s_add_u32 s17, s17, s16\n" \
  "s_mov_b32 s8, s90\n" \
  "s_mov_b32 s9, s91\n" \
  "s_movk_i32 s18, 1152\n" \
  ".Lr15_%=j:\n" \
  "s_and_b32 s9, s9, 0xffff\n" \
  "s_mov_b32 s10, 5308416\n" \
  "s_mov_b32 s11, 0x20000\n" \
  "v_lshrrev_b32 v200, 3, v1\n" \
  "v_mul_u32_u24 v200, s18, v200\n" \
  "v_and_b32 v201, 0x70, v10\n" \
  "v_add_u32 v200, v200, v201\n" \
  "s_lshl_b32 s18, s18, 3\n" \
  "s_add_u32 m0, s28, 0\n" \
  "s_nop 0\n" \
  "buffer_load_dwordx4 v200, s[8:11], s17 offen lds\n" \
  "s_add_u32 m0, s28, 1024\n" \
  "s_add_u32 s17, s17, s18\n" \
  "buffer_load_dwordx4 v200, s[8:11], s17 offen lds\n" \
  "s_add_u32 m0, s28, 2048\n" \
  "s_add_u32 s17, s17, s18\n" \
  "buffer_load_dwordx4 v200, s[8:11], s17 offen lds\n" \
  "s_add_u32 m0, s28, 3072\n" \
  "s_add_u32 s17, s17, s18\n" \
  "buffer_load_dwordx4 v200, s[8:11], s17 offen lds\n" \
  "s_cmp_lt_u32 s27, 1584\n" \
  "s_cbranch_scc0 .Li14_%=a\n" \
  "s_cmp_lt_u32 s27, 1296\n" \
  "s_cbranch_scc1 .Lr16_%=d\n" \
  "s_sub_u32 s16, s27, 1296\n" \
  "s_and_b32 s17, s16, 1\n" \
  "s_cselect_b32 s8, s88, s86\n" \
  "s_cselect_b32 s9, s89, s87\n" \
  "s_lshr_b32 s17, s16, 4\n" \
  "s_mul_i32 s17, s17, 294912\n" \
  "s_bfe_u32 s16, s16, 0x30001\n" \
  "s_mul_i32 s16, s16, 1152\n" \
  "s_add_u32 s17, s17, s16\n" \
  "s_add_u32 s17, s17, 1024\n" \
  "s_movk_i32 s18, 9216\n" \
  "s_branch .Lr16_%=j\n" \
  ".Lr16_%=d:\n" \
  "s_mul_i32 s16, s27, 7282\n" \
  "s_lshr_b32 s16, s16, 16\n" \
  "s_mul_i32 s16, s16, 35712\n" \
  "s_lshl_b32 s17, s27, 7\n" \
  "s_add_u32 s17, s17, s16\n" \
  "s_mov_b32 s8, s90\n" \
  "s_mov_b32 s9, s91\n" \
  "s_movk_i32 s18, 1152\n" \
  ".Lr16_%=j:\n" \
  "s_and_b32 s9, s9, 0xffff\n" \
  "s_mov_b32 s10, 5308416\n" \
  "s_mov_b32 s11, 0x20000\n" \
  "v_lshrrev_b32 v200, 3, v1\n" \
  "v_mul_u32_u24 v200, s18, v200\n" \
  "v_and_b32 v201, 0x70, v10\n" \
  "v_add_u32 v200, v200, v201\n" \
  "s_lshl_b32 s18, s18, 3\n" \
  "s_add_u32 m0, s29, 0\n" \
  "s_nop 0\n" \
  "buffer_load_dwordx4 v200, s[8:11], s17 offen lds\n" \
  "s_add_u32 m0, s29, 1024\n" \
  "s_add_u32 s17, s17, s18\n" \
  "buffer_load_dwordx4 v200, s[8:11], s17 offen lds\n" \
  "s_add_u32 m0, s29, 2048\n" \
  "s_add_u32 s17, s17, s18\n" \
  "buffer_load_dwordx4 v200, s[8:11], s17 offen lds\n" \
  "s_add_u32 m0, s29, 3072\n" \
  "s_add_u32 s17, s17, s18\n" \
  "buffer_load_dwordx4 v200, s[8:11], s17 offen lds\n" \
  "v_lshrrev_b32 v200, 4, v1\n" \
  "v_bfe_u32 v201, v1, 2, 2\n" \
  "v_and_b32 v202, 3, v1\n" \
  "s_cmp_lt_u32 s26, 1296\n" \
  "s_cbranch_scc1 .Lt17_%=d\n" \
  "s_sub_u32 s16, s26, 1296\n" \
  "s_bfe_u32 s17, s16, 0x30001\n" \
  "s_mul_i32 s17, s17, 163840\n" \
  "s_lshr_b32 s18, s16, 4\n" \
  "s_lshl_b32 s18, s18, 1\n" \
  "s_and_b32 s16, s16, 1\n" \
  "s_add_u32 s16, s16, s18\n" \
  "s_lshl_b32 s16, s16, 10\n" \
  "s_add_u32 s16, s16, s17\n" \
  "s_add_u32 s12, s16, 48234496\n" \
  "s_add_u32 s13, s12, 40960\n" \
  "s_add_u32 s14, s12, 81920\n" \
  "s_add_u32 s15, s12, 122880\n" \
  "s_mov_b32 s16, 3\n" \
  "s_mov_b32 s17, 3\n" \
  "s_mov_b32 s18, 32\n" \
  "s_movk_i32 s19, 512\n" \
  "s_branch .Lt17_%=j\n" \
  ".Lt17_%=d:\n" \
  "s_mul_i32 s16, s26, 7282\n" \
  "s_lshr_b32 s16, s16, 16\n" \
  "s_mul_i32 s17, s16, 9\n" \
  "s_sub_u32 s17, s26, s17\n" \
  "s_mul_i32 s19, s16, 3641\n" \
  "s_lshr_b32 s19, s19, 16\n" \
  "s_mul_i32 s20, s19, 18\n" \
  "s_sub_u32 s20, s16, s20\n" \
  "s_lshr_b32 s18, s19, 1\n" \
  "s_and_b32 s19, s19, 1\n" \
  "s_lshr_b32 s21, s20, 2\n" \
  "s_and_b32 s22, s20, 1\n" \
  "s_lshl_b32 s22, s22, 2\n" \
  "s_add_u32 s21, s21, s22\n" \
  "s_bitcmp1_b32 s20, 1\n" \
  "s_cselect_b32 s22, 10, 0\n" \
  "s_add_u32 s21, s21, s22\n" \
  "s_sub_u32 s22, s20, 8\n" \
  "s_cmp_lt_u32 s20, 16\n" \
  "s_cselect_b32 s21, s21, s22\n" \
  "s_mul_i32 s16, s18, 1343488\n" \
  "s_lshl_b32 s19, s19, 12\n" \
  "s_add_u32 s16, s16, s19\n" \
  "s_add_u32 s16, s16, 41943040\n" \
  "s_and_b32 s23, s21, 1\n" \
  "s_sub_u32 s24, s21, s23\n" \
  "s_lshl_b32 s22, s24, 1\n" \
  "s_add_u32 s22, s22, s23\n" \
  "s_mul_i32 s19, s24, 7\n" \
  "s_add_u32 s19, s19, s23\n" \
  "s_add_u32 s20, s19, 10\n" \
  "s_mul_i32 s24, s24, 5\n" \
  "s_add_u32 s24, s24, s23\n" \
  "s_add_u32 s24, s24, 20\n" \
  "s_add_u32 s23, s22, 90\n" \
  "s_cmp_lt_u32 s21, 10\n" \
  "s_cselect_b32 s19, s19, s24\n" \
  "s_cselect_b32 s20, s20, s23\n" \
  "s_add_u32 s22, s22, 126\n" \
  "s_cmp_eq_u32 s17, 8\n" \
  "s_cbranch_scc1 .Lt17_%=e\n" \
  "s_lshr_b32 s21, s17, 1\n" \
  "s_mul_i32 s21, s21, 335872\n" \
  "s_add_u32 s16, s16, s21\n" \
  "s_and_b32 s17, s17, 1\n" \
  "s_lshl_b32 s17, s17, 1\n" \
  "s_sub_u32 s17, s17, s18\n" \
  "s_add_u32 s21, s17, 0\n" \
  "s_and_b32 s21, s21, 3\n" \
  "s_add_u32 s23, s19, 4\n" \
  "s_cmp_eq_u32 s21, 1\n" \
  "s_cselect_b32 s23, s19, s23\n" \
  "s_cmp_eq_u32 s21, 3\n" \
  "s_cselect_b32 s23, s20, s23\n" \
  "s_cmp_eq_u32 s21, 0\n" \
  "s_cselect_b32 s23, s22, s23\n" \
  "s_andn2_b32 s24, s23, 3\n" \
  "s_add_u32 s24, s24, s23\n" \
  "s_lshl_b32 s24, s24, 10\n" \
  "s_add_u32 s12, s24, s16\n" \
  "s_add_u32 s23, s23, 2\n" \
  "s_andn2_b32 s24, s23, 3\n" \
  "s_add_u32 s24, s24, s23\n" \
  "s_lshl_b32 s24, s24, 10\n" \
  "s_add_u32 s13, s24, s16\n" \
  "s_add_u32 s21, s17, 1\n" \
  "s_and_b32 s21, s21, 3\n" \
  "s_add_u32 s23, s19, 4\n" \
  "s_cmp_eq_u32 s21, 1\n" \
  "s_cselect_b32 s23, s19, s23\n" \
  "s_cmp_eq_u32 s21, 3\n" \
  "s_cselect_b32 s23, s20, s23\n" \
  "s_cmp_eq_u32 s21, 0\n" \
  "s_cselect_b32 s23, s22, s23\n" \
  "s_andn2_b32 s24, s23, 3\n" \
  "s_add_u32 s24, s24, s23\n" \
  "s_lshl_b32 s24, s24, 10\n" \
  "s_add_u32 s14, s24, s16\n" \
  "s_add_u32 s23, s23, 2\n" \
  "s_andn2_b32 s24, s23, 3\n" \
  "s_add_u32 s24, s24, s23\n" \
  "s_lshl_b32 s24, s24, 10\n" \
  "s_add_u32 s15, s24, s16\n" \
  "s_mov_b32 s16, 2\n" \
  "s_mov_b32 s17, 4\n" \
  "s_mov_b32 s18, 8\n" \
  "s_movk_i32 s19, 2048\n" \
  "s_branch .Lt17_%=j\n" \
  ".Lt17_%=e:\n" \
  "s_add_u32 s23, s19, 8\n" \
  "s_andn2_b32 s24, s23, 3\n" \
  "s_add_u32 s24, s24, s23\n" \
  "s_lshl_b32 s24, s24, 10\n" \
  "s_add_u32 s12, s24, s16\n" \
  "s_add_u32 s13, s12, 335872\n" \
  "s_add_u32 s14, s12, 671744\n" \
  "s_add_u32 s15, s12, 1007616\n" \
  "s_mov_b32 s16, 2\n" \
  "s_mov_b32 s17, 3\n" \
  "s_mov_b32 s18, 32\n" \
  "s_movk_i32 s19, 2048\n" \
  ".Lt17_%=j:\n" \
  "v_lshlrev_b32 v200, s16, v200\n" \
  "v_add_u32 v200, v200, v201\n" \
  "v_lshlrev_b32 v202, s17, v202\n" \
  "v_lshl_add_u32 v200, v200, 7, v202\n" \
  "v_add_u32 v200, s28, v200\n" \
  "v_add_u32 v201, s19, v200\n" \
  "v_add_u32 v202, s18, v200\n" \
  "v_add_u32 v203, s18, v201\n" \
  "s_waitcnt vmcnt(4)\n" \
  "ds_read_b64_tr_b16 v[208:209], v200\n" \
  "ds_read_b64_tr_b16 v[210:211], v201\n" \
  "ds_read_b64_tr_b16 v[212:213], v202\n" \
  "ds_read_b64_tr_b16 v[214:215], v203\n" \
  "ds_read_b64_tr_b16 v[216:217], v200 offset:64\n" \
  "ds_read_b64_tr_b16 v[218:219], v201 offset:64\n" \
  "ds_read_b64_tr_b16 v[220:221], v202 offset:64\n" \
  "ds_read_b64_tr_b16 v[222:223], v203 offset:64\n" \
  "s_waitcnt lgkmcnt(0)\n" \
  "buffer_store_dwordx4 v[208:211], v10, s[40:43], s12 offen sc1\n" \
  "buffer_store_dwordx4 v[212:215], v10, s[40:43], s13 offen sc1\n" \
  "buffer_store_dwordx4 v[216:219], v10, s[40:43], s14 offen sc1\n" \
  "buffer_store_dwordx4 v[220:223], v10, s[40:43], s15 offen sc1\n" \
  "s_mov_b32 s26, s27\n" \
  "s_mov_b32 s28, s29\n" \
  "s_waitcnt vmcnt(4)\n" \
  "s_branch .Li14_%=b\n" \
  ".Li14_%=a:\n" \
  "s_waitcnt vmcnt(0)\n" \
  ".Li14_%=b:\n" \
  "v_lshrrev_b32 v200, 4, v1\n" \
  "v_bfe_u32 v201, v1, 2, 2\n" \
  "v_and_b32 v202, 3, v1\n" \
  "s_cmp_lt_u32 s26, 1296\n" \
  "s_cbranch_scc1 .Lt18_%=d\n" \
  "s_sub_u32 s16, s26, 1296\n" \
  "s_bfe_u32 s17, s16, 0x30001\n" \
  "s_mul_i32 s17, s17, 163840\n" \
  "s_lshr_b32 s18, s16, 4\n" \
  "s_lshl_b32 s18, s18, 1\n" \
  "s_and_b32 s16, s16, 1\n" \
  "s_add_u32 s16, s16, s18\n" \
  "s_lshl_b32 s16, s16, 10\n" \
  "s_add_u32 s16, s16, s17\n" \
  "s_add_u32 s12, s16, 48234496\n" \
  "s_add_u32 s13, s12, 40960\n" \
  "s_add_u32 s14, s12, 81920\n" \
  "s_add_u32 s15, s12, 122880\n" \
  "s_mov_b32 s16, 3\n" \
  "s_mov_b32 s17, 3\n" \
  "s_mov_b32 s18, 32\n" \
  "s_movk_i32 s19, 512\n" \
  "s_branch .Lt18_%=j\n" \
  ".Lt18_%=d:\n" \
  "s_mul_i32 s16, s26, 7282\n" \
  "s_lshr_b32 s16, s16, 16\n" \
  "s_mul_i32 s17, s16, 9\n" \
  "s_sub_u32 s17, s26, s17\n" \
  "s_mul_i32 s19, s16, 3641\n" \
  "s_lshr_b32 s19, s19, 16\n" \
  "s_mul_i32 s20, s19, 18\n" \
  "s_sub_u32 s20, s16, s20\n" \
  "s_lshr_b32 s18, s19, 1\n" \
  "s_and_b32 s19, s19, 1\n" \
  "s_lshr_b32 s21, s20, 2\n" \
  "s_and_b32 s22, s20, 1\n" \
  "s_lshl_b32 s22, s22, 2\n" \
  "s_add_u32 s21, s21, s22\n" \
  "s_bitcmp1_b32 s20, 1\n" \
  "s_cselect_b32 s22, 10, 0\n" \
  "s_add_u32 s21, s21, s22\n" \
  "s_sub_u32 s22, s20, 8\n" \
  "s_cmp_lt_u32 s20, 16\n" \
  "s_cselect_b32 s21, s21, s22\n" \
  "s_mul_i32 s16, s18, 1343488\n" \
  "s_lshl_b32 s19, s19, 12\n" \
  "s_add_u32 s16, s16, s19\n" \
  "s_add_u32 s16, s16, 41943040\n" \
  "s_and_b32 s23, s21, 1\n" \
  "s_sub_u32 s24, s21, s23\n" \
  "s_lshl_b32 s22, s24, 1\n" \
  "s_add_u32 s22, s22, s23\n" \
  "s_mul_i32 s19, s24, 7\n" \
  "s_add_u32 s19, s19, s23\n" \
  "s_add_u32 s20, s19, 10\n" \
  "s_mul_i32 s24, s24, 5\n" \
  "s_add_u32 s24, s24, s23\n" \
  "s_add_u32 s24, s24, 20\n" \
  "s_add_u32 s23, s22, 90\n" \
  "s_cmp_lt_u32 s21, 10\n" \
  "s_cselect_b32 s19, s19, s24\n" \
  "s_cselect_b32 s20, s20, s23\n" \
  "s_add_u32 s22, s22, 126\n" \
  "s_cmp_eq_u32 s17, 8\n" \
  "s_cbranch_scc1 .Lt18_%=e\n" \
  "s_lshr_b32 s21, s17, 1\n" \
  "s_mul_i32 s21, s21, 335872\n" \
  "s_add_u32 s16, s16, s21\n" \
  "s_and_b32 s17, s17, 1\n" \
  "s_lshl_b32 s17, s17, 1\n" \
  "s_sub_u32 s17, s17, s18\n" \
  "s_add_u32 s21, s17, 0\n" \
  "s_and_b32 s21, s21, 3\n" \
  "s_add_u32 s23, s19, 4\n" \
  "s_cmp_eq_u32 s21, 1\n" \
  "s_cselect_b32 s23, s19, s23\n" \
  "s_cmp_eq_u32 s21, 3\n" \
  "s_cselect_b32 s23, s20, s23\n" \
  "s_cmp_eq_u32 s21, 0\n" \
  "s_cselect_b32 s23, s22, s23\n" \
  "s_andn2_b32 s24, s23, 3\n" \
  "s_add_u32 s24, s24, s23\n" \
  "s_lshl_b32 s24, s24, 10\n" \
  "s_add_u32 s12, s24, s16\n" \
  "s_add_u32 s23, s23, 2\n" \
  "s_andn2_b32 s24, s23, 3\n" \
  "s_add_u32 s24, s24, s23\n" \
  "s_lshl_b32 s24, s24, 10\n" \
  "s_add_u32 s13, s24, s16\n" \
  "s_add_u32 s21, s17, 1\n" \
  "s_and_b32 s21, s21, 3\n" \
  "s_add_u32 s23, s19, 4\n" \
  "s_cmp_eq_u32 s21, 1\n" \
  "s_cselect_b32 s23, s19, s23\n" \
  "s_cmp_eq_u32 s21, 3\n" \
  "s_cselect_b32 s23, s20, s23\n" \
  "s_cmp_eq_u32 s21, 0\n" \
  "s_cselect_b32 s23, s22, s23\n" \
  "s_andn2_b32 s24, s23, 3\n" \
  "s_add_u32 s24, s24, s23\n" \
  "s_lshl_b32 s24, s24, 10\n" \
  "s_add_u32 s14, s24, s16\n" \
  "s_add_u32 s23, s23, 2\n" \
  "s_andn2_b32 s24, s23, 3\n" \
  "s_add_u32 s24, s24, s23\n" \
  "s_lshl_b32 s24, s24, 10\n" \
  "s_add_u32 s15, s24, s16\n" \
  "s_mov_b32 s16, 2\n" \
  "s_mov_b32 s17, 4\n" \
  "s_mov_b32 s18, 8\n" \
  "s_movk_i32 s19, 2048\n" \
  "s_branch .Lt18_%=j\n" \
  ".Lt18_%=e:\n" \
  "s_add_u32 s23, s19, 8\n" \
  "s_andn2_b32 s24, s23, 3\n" \
  "s_add_u32 s24, s24, s23\n" \
  "s_lshl_b32 s24, s24, 10\n" \
  "s_add_u32 s12, s24, s16\n" \
  "s_add_u32 s13, s12, 335872\n" \
  "s_add_u32 s14, s12, 671744\n" \
  "s_add_u32 s15, s12, 1007616\n" \
  "s_mov_b32 s16, 2\n" \
  "s_mov_b32 s17, 3\n" \
  "s_mov_b32 s18, 32\n" \
  "s_movk_i32 s19, 2048\n" \
  ".Lt18_%=j:\n" \
  "v_lshlrev_b32 v200, s16, v200\n" \
  "v_add_u32 v200, v200, v201\n" \
  "v_lshlrev_b32 v202, s17, v202\n" \
  "v_lshl_add_u32 v200, v200, 7, v202\n" \
  "v_add_u32 v200, s28, v200\n" \
  "v_add_u32 v201, s19, v200\n" \
  "v_add_u32 v202, s18, v200\n" \
  "v_add_u32 v203, s18, v201\n" \
  "ds_read_b64_tr_b16 v[224:225], v200\n" \
  "ds_read_b64_tr_b16 v[226:227], v201\n" \
  "ds_read_b64_tr_b16 v[228:229], v202\n" \
  "ds_read_b64_tr_b16 v[230:231], v203\n" \
  "ds_read_b64_tr_b16 v[232:233], v200 offset:64\n" \
  "ds_read_b64_tr_b16 v[234:235], v201 offset:64\n" \
  "ds_read_b64_tr_b16 v[236:237], v202 offset:64\n" \
  "ds_read_b64_tr_b16 v[238:239], v203 offset:64\n" \
  "s_waitcnt lgkmcnt(0)\n" \
  "buffer_store_dwordx4 v[224:227], v10, s[40:43], s12 offen sc1\n" \
  "buffer_store_dwordx4 v[228:231], v10, s[40:43], s13 offen sc1\n" \
  "buffer_store_dwordx4 v[232:235], v10, s[40:43], s14 offen sc1\n" \
  "buffer_store_dwordx4 v[236:239], v10, s[40:43], s15 offen sc1\n" \
  ".Li14_%=c:\n" \
  "s_lshr_b32 s16, s5, 3\n" \
  "s_lshl_b32 s16, s16, 3\n" \
  "s_add_u32 s16, s16, s4\n" \
  "s_mul_i32 s0, s16, 76800\n" \
  "s_add_u32 s0, s0, 7340032\n" \
  "s_lshl_b32 s16, s16, 3\n" \
  "s_and_b32 s17, s5, 7\n" \
  "s_add_u32 s16, s16, s17\n" \
  "s_lshr_b32 s17, s16, 1\n" \
  "s_mul_i32 s1, s17, 18432\n" \
  "s_and_b32 s17, s16, 1\n" \
  "s_lshl_b32 s17, s17, 7\n" \
  "s_add_u32 s1, s1, s17\n" \
  "v_and_b32 v24, 7, v1\n" \
  "v_lshrrev_b32 v26, 3, v1\n" \
  "s_lshl_b32 s17, s3, 3\n" \
  "v_add_u32 v26, s17, v26\n" \
  "v_lshlrev_b32 v27, 4, v26\n" \
  "v_lshlrev_b32 v26, 8, v26\n" \
  "v_lshl_add_u32 v26, v24, 4, v26\n" \
  "v_lshlrev_b32 v24, 5, v24\n" \
  "v_add_u32 v24, 76800, v24\n" \
  "s_lshl_b32 s17, s3, 10\n" \
  "s_add_u32 s17, s17, 155648\n" \
  "v_add_u32 v25, s17, v10\n" \
  "s_cmp_eq_u32 s3, 3\n" \
  "s_cbranch_scc0 .LMi2_%=\n" \
  "s_mov_b64 exec, 255\n" \
  "s_mov_b32 s35, 0x20000\n" \
  ".Lq19_%=:\n" \
  "v_mov_b32 v176, 0\n" \
  "buffer_atomic_add v176, v87, s[40:43], s45 offen sc0\n" \
  "s_waitcnt vmcnt(0)\n" \
  "v_cmp_gt_u32 vcc, v84, v176\n" \
  "s_and_b64 exec, exec, vcc\n" \
  "s_cbranch_scc0 .Lq19_%=x\n" \
  "s_sleep 2\n" \
  "s_sub_u32 s35, s35, 1\n" \
  "s_cbranch_scc0 .Lq19_%=\n" \
  ".Lq19_%=x:\n" \
  "s_mov_b64 exec, -1\n" \
  "s_mov_b64 exec, 1\n" \
  "s_mov_b32 s35, 0x20000\n" \
  ".Lq20_%=:\n" \
  "v_mov_b32 v176, 0\n" \
  "buffer_atomic_add v176, v87, s[40:43], s45 offen offset:32 sc0\n" \
  "s_waitcnt vmcnt(0)\n" \
  "v_cmp_gt_u32 vcc, 36, v176\n" \
  "s_and_b64 exec, exec, vcc\n" \
  "s_cbranch_scc0 .Lq20_%=x\n" \
  "s_sleep 2\n" \
  "s_sub_u32 s35, s35, 1\n" \
  "s_cbranch_scc0 .Lq20_%=\n" \
  ".Lq20_%=x:\n" \
  "s_mov_b64 exec, -1\n" \
  "s_cmp_lg_u32 s2, 0\n" \
  "s_cbranch_scc1 .LMi2_%=\n" \
  "v_lshrrev_b32 v2, 2, v1\n" \
  "v_and_b32 v3, 3, v1\n" \
  "v_lshlrev_b32 v2, 7, v2\n" \
  "v_lshl_add_u32 v2, v3, 3, v2\n" \
  "s_not_b64 s[18:19], s[46:47]\n" \
  "v_mov_b32 v4, s18\n" \
  "v_mov_b32 v5, s19\n" \
  "v_mov_b32 v6, s46\n" \
  "v_mov_b32 v7, s47\n" \
  "s_mov_b32 s17, 65536\n" \
  "s_mov_b32 exec_hi, 0\n" \
  "buffer_store_dwordx2 v[4:5], v2, s[40:43], s17 offen offset:128 sc1\n" \
  "s_mov_b64 exec, 1\n" \
  "buffer_store_dwordx2 v[6:7], off, s[40:43], s17 sc1\n" \
  "s_mov_b64 exec, -1\n" \
  ".LMi2_%=:\n" \
  "s_barrier\n" \
  "s_movk_i32 s16, 19840\n" \
  "buffer_load_dwordx4 v[160:163], v27, s[40:43], s16 offen offset:1152 sc1\n" \
  "buffer_load_dwordx4 v[164:167], v27, s[40:43], s16 offen sc1\n" \
  "s_cmp_eq_u32 s3, 3\n" \
  "s_cbranch_scc0 .Lo21_%=\n" \
  "buffer_load_dwordx4 v[168:171], v27, s[40:43], s16 offen offset:1792 sc1\n" \
  "buffer_load_dwordx4 v[172:175], v27, s[40:43], s16 offen offset:640 sc1\n" \
  ".Lo21_%=:\n" \
  "s_waitcnt vmcnt(0)\n" \
  "s_cbranch_scc0 .LMq2x_%=\n" \
  "s_and_b32 s16, s4, 3\n" \
  "s_and_b32 s17, s4, 4\n" \
  "s_lshr_b32 s18, s5, 3\n" \
  "s_add_u32 s17, s17, s18\n" \
  "s_and_b32 s18, s5, 7\n" \
  "s_lshr_b32 s19, s18, 2\n" \
  "s_and_b32 s20, s18, 3\n" \
  "s_lshr_b32 s21, s3, 2\n" \
  "s_and_b32 s22, s3, 3\n" \
  "s_cmp_lt_u32 s3, 4\n" \
  "s_cselect_b32 s48, s86, s88\n" \
  "s_cselect_b32 s49, s87, s89\n" \
  "s_mov_b32 s50, s96\n" \
  "s_mov_b32 s51, s97\n" \
  "s_mov_b32 s52, s98\n" \
  "s_mov_b32 s53, s99\n" \
  "s_lshl_b32 s23, s16, 1\n" \
  "s_add_u32 s23, s23, s19\n" \
  "s_mul_i32 s54, s23, 1152\n" \
  "s_lshl_b32 s24, s20, 8\n" \
  "s_add_u32 s54, s54, s24\n" \
  "s_lshl_b32 s57, s3, 10\n" \
  "s_lshl_b32 s24, s16, 3\n" \
  "s_add_u32 s24, s24, s18\n" \
  "s_mul_i32 s55, s24, 40960\n" \
  "s_add_u32 s55, s55, s57\n" \
  "s_add_u32 s55, s55, 48234496\n" \
  "s_lshl_b32 s24, s17, 3\n" \
  "s_add_u32 s25, s24, s3\n" \
  "s_lshl_b32 s56, s25, 2\n" \
  "s_add_u32 s56, s56, s16\n" \
  "s_lshl_b32 s56, s56, 6\n" \
  "s_add_u32 s56, s56, 262144\n" \
  "s_mul_i32 s60, s25, 36864\n" \
  "s_add_u32 s60, s60, 13631488\n" \
  "s_lshl_b32 s58, s4, 11\n" \
  "s_add_u32 s58, s58, 147456\n" \
  "s_add_u32 s25, s24, s23\n" \
  "s_lshl_b32 s25, s25, 3\n" \
  "s_add_u32 s25, s25, s3\n" \
  "s_mul_i32 s61, s25, 36864\n" \
  "s_add_u32 s61, s61, 16777216\n" \
  "s_lshl_b32 s66, s25, 6\n" \
  "s_add_u32 s66, s66, 229376\n" \
  "s_lshr_b32 s26, s20, 1\n" \
  "s_lshl_b32 s27, s26, 11\n" \
  "s_and_b32 s28, s20, 1\n" \
  "s_lshl_b32 s28, s28, 9\n" \
  "s_add_u32 s62, s61, s27\n" \
  "s_add_u32 s62, s62, s28\n" \
  "s_add_u32 s62, s62, 16384\n" \
  "s_lshl_b32 s27, s20, 11\n" \
  "s_add_u32 s61, s61, s27\n" \
  "s_lshl_b32 s25, s16, 1\n" \
  "s_add_u32 s25, s25, s24\n" \
  "s_add_u32 s25, s25, s21\n" \
  "s_lshl_b32 s25, s25, 1\n" \
  "s_add_u32 s25, s25, s19\n" \
  "s_lshl_b32 s25, s25, 2\n" \
  "s_add_u32 s25, s25, s22\n" \
  "s_mul_i32 s63, s25, 36864\n" \
  "s_add_u32 s63, s63, 16777216\n" \
  "s_lshl_b32 s67, s25, 6\n" \
  "s_add_u32 s67, s67, 229376\n" \
  "s_lshl_b32 s27, s21, 10\n" \
  "s_add_u32 s63, s63, s27\n" \
  "s_xor_b32 s64, s63, 1024\n" \
  "s_lshl_b32 s29, s16, 2\n" \
  "s_add_u32 s25, s29, s20\n" \
  "s_mul_i32 s65, s25, 335872\n" \
  "s_add_u32 s65, s65, s57\n" \
  "s_add_u32 s65, s65, 41943040\n" \
  "s_add_u32 s25, s24, s18\n" \
  "s_lshl_b32 s25, s25, 5\n" \
  "s_add_u32 s25, s25, s3\n" \
  "s_lshl_b32 s27, s25, 13\n" \
  "s_add_u32 s27, s27, 50331648\n" \
  "s_lshl_b32 s25, s25, 5\n" \
  "s_add_u32 s25, s25, 163840\n" \
  "s_add_u32 s29, s16, 0\n" \
  "s_and_b32 s29, s29, 3\n" \
  "s_lshl_b32 s73, s29, 16\n" \
  "s_add_u32 s73, s73, s27\n" \
  "s_lshl_b32 s29, s29, 8\n" \
  "s_add_u32 s76, s29, s25\n" \
  "s_add_u32 s29, s16, 1\n" \
  "s_and_b32 s29, s29, 3\n" \
  "s_lshl_b32 s68, s29, 16\n" \
  "s_add_u32 s68, s68, s27\n" \
  "s_lshl_b32 s29, s29, 8\n" \
  "s_add_u32 s78, s29, s25\n" \
  "s_add_u32 s29, s16, 2\n" \
  "s_and_b32 s29, s29, 3\n" \
  "s_lshl_b32 s69, s29, 16\n" \
  "s_add_u32 s69, s69, s27\n" \
  "s_lshl_b32 s29, s29, 8\n" \
  "s_add_u32 s79, s29, s25\n" \
  "s_add_u32 s29, s16, 3\n" \
  "s_and_b32 s29, s29, 3\n" \
  "s_lshl_b32 s70, s29, 16\n" \
  "s_add_u32 s70, s70, s27\n" \
  "s_lshl_b32 s29, s29, 8\n" \
  "s_add_u32 s59, s29, s25\n" \
  "s_sub_u32 s29, s22, s16\n" \
  "s_sub_u32 s29, s29, 1\n" \
  "s_and_b32 s29, s29, 3\n" \
  "s_add_u32 s74, s29, 1\n" \
  "s_lshr_b32 s74, s74, 2\n" \
  "s_lshl_b32 s71, s22, 7\n" \
  "s_add_u32 s71, s71, s29\n" \
  "s_lshl_b32 s71, s71, 9\n" \
  "s_add_u32 s71, s71, s27\n" \
  "s_add_u32 s71, s71, 3072\n" \
  "s_lshl_b32 s72, s22, 8\n" \
  "s_add_u32 s72, s72, s25\n" \
  "s_lshl_b32 s25, s17, 1\n" \
  "s_add_u32 s25, s25, s19\n" \
  "s_lshl_b32 s25, s25, 2\n" \
  "s_add_u32 s25, s25, s22\n" \
  "s_lshl_b32 s25, s25, 1\n" \
  "s_add_u32 s25, s25, s21\n" \
  "s_mul_i32 s27, s25, 18432\n" \
  "s_add_u32 s27, s27, 10485760\n" \
  "s_lshl_b32 s29, s20, 2\n" \
  "s_add_u32 s29, s29, s16\n" \
  "s_lshl_b32 s75, s29, 10\n" \
  "s_add_u32 s75, s75, s27\n" \
  "s_lshl_b32 s77, s20, 9\n" \
  "s_add_u32 s77, s77, s27\n" \
  "s_add_u32 s77, s77, 16384\n" \
  "s_lshr_b32 s31, s2, 1\n" \
  "s_lshl_b32 s31, s31, 13\n" \
  "s_and_b32 s27, s2, 1\n" \
  "s_cmp_eq_u32 s27, s21\n" \
  "s_cselect_b32 s31, s31, 0x40000000\n" \
  "s_cmp_lt_u32 s2, 216\n" \
  "s_cselect_b32 s31, s31, 0x40000000\n" \
  "s_or_b32 s31, s31, s22\n" \
  "v_lshrrev_b32 v2, 3, v1\n" \
  "s_lshl_b32 s27, s22, 3\n" \
  "v_add_u32 v2, s27, v2\n" \
  "v_mul_u32_u24 v8, 0x2400, v2\n" \
  "v_bfe_u32 v3, v1, 4, 1\n" \
  "s_and_b32 s27, s22, 1\n" \
  "s_lshl_b32 s27, s27, 1\n" \
  "v_or_b32 v3, s27, v3\n" \
  "v_lshlrev_b32 v3, 5, v3\n" \
  "v_and_b32 v4, 0x70, v10\n" \
  "v_xor_b32 v3, v3, v4\n" \
  "v_add_u32 v8, v8, v3\n" \
  "v_mov_b32 v9, v10\n" \
  "v_lshlrev_b32 v11, 3, v1\n" \
  "v_lshrrev_b32 v2, 4, v1\n" \
  "v_bfe_u32 v3, v1, 2, 2\n" \
  "v_lshl_add_u32 v4, v2, 3, v3\n" \
  "v_lshlrev_b32 v4, 7, v4\n" \
  "v_and_b32 v5, 24, v11\n" \
  "v_or_b32 v4, v4, v5\n" \
  "v_bfe_u32 v5, v1, 3, 2\n" \
  "v_lshl_or_b32 v12, v5, 5, v4\n" \
  "v_xor_b32 v13, 32, v12\n" \
  "v_xor_b32 v14, 64, v12\n" \
  "v_xor_b32 v15, 0x60, v12\n" \
  "v_and_b32 v3, 15, v1\n" \
  "v_lshrrev_b32 v4, 5, v1\n" \
  "v_lshl_add_u32 v4, v4, 4, v3\n" \
  "v_and_b32 v5, 1, v2\n" \
  "v_lshlrev_b32 v5, 3, v5\n" \
  "v_lshl_or_b32 v16, v4, 4, v5\n" \
  "s_lshl_b32 s27, s21, 12\n" \
  "v_add_u32 v17, s27, v10\n" \
  "s_mul_i32 s27, s3, 5120\n" \
  "s_add_u32 s27, s27, 98304\n" \
  "v_add_u32 v18, s27, v10\n" \
  "s_xor_b32 s27, s3, 4\n" \
  "s_mul_i32 s27, s27, 5120\n" \
  "s_add_u32 s27, s27, 98304\n" \
  "v_add_u32 v19, s27, v10\n" \
  "s_and_b32 s27, s25, 3\n" \
  "s_lshl_b32 s27, s27, 9\n" \
  "s_lshr_b32 s25, s25, 2\n" \
  "s_add_u32 s25, s25, s27\n" \
  "s_mul_i32 s25, s25, 1152\n" \
  "v_mul_u32_u24 v3, 0x9000, v3\n" \
  "v_add_u32 v3, s25, v3\n" \
  "s_lshl_b32 s29, s29, 5\n" \
  "v_lshl_add_u32 v4, v2, 3, s29\n" \
  "s_lshl_b32 s27, s20, 4\n" \
  "s_add_u32 s27, s27, 512\n" \
  "v_lshl_add_u32 v5, v2, 2, s27\n" \
  "v_lshl_add_u32 v20, v4, 1, v3\n" \
  "v_lshl_add_u32 v21, v5, 1, v3\n" \
  "s_movk_i32 s27, 11520\n" \
  "v_lshl_add_u32 v22, v4, 2, s27\n" \
  "v_lshl_add_u32 v23, v5, 2, s27\n" \
  "s_mov_b32 s8, s86\n" \
  "s_and_b32 s9, s87, 0xffff\n" \
  "s_mov_b32 s10, 5308416\n" \
  "s_mov_b32 s11, 0x20000\n" \
  "s_mov_b32 s12, s88\n" \
  "s_and_b32 s13, s89, 0xffff\n" \
  "s_mov_b32 s14, 5308416\n" \
  "s_mov_b32 s15, 0x20000\n" \
  "s_and_b32 s17, s4, 4\n" \
  "s_lshr_b32 s18, s5, 3\n" \
  "s_add_u32 s17, s17, s18\n" \
  "s_mul_i32 s17, s17, 4\n" \
  "s_lshr_b32 s17, s17, 3\n" \
  "s_mul_i32 s26, s17, 294912\n" \
  "s_add_u32 s26, s26, s54\n" \
  "s_lshl_b32 s27, s17, 13\n" \
  "s_add_u32 s27, s27, 81920\n" \
  "v_subrev_u32 v83, 147456, v8\n" \
  "v_subrev_u32 v82, 73728, v83\n" \
  "v_xor_b32 v82, 64, v82\n" \
  "s_mov_b32 s46, s26\n" \
  "s_mov_b32 s47, s27\n" \
  "s_branch .LM3c_%=\n" \
  ".LMdc_%=:\n" \
  "v_lshrrev_b32 v2, 3, v1\n" \
  "v_mul_u32_u24 v2, 0x60, v2\n" \
  "v_and_b32 v3, 7, v1\n" \
  "v_lshl_add_u32 v2, v3, 2, v2\n" \
  "s_lshl_b32 s16, s3, 5\n" \
  "v_add_u32 v2, s16, v2\n" \
  "v_lshlrev_b32 v5, 1, v2\n" \
  "v_mov_b32 v6, 0\n" \
  "v_mov_b32 v7, 0\n" \
  "s_movk_i32 s17, 16384\n" \
  "v_cvt_f32_u32 v4, s7\n" \
  "v_mul_f32 v4, 0xbae38e39, v4\n" \
  "v_cvt_f32_u32 v2, v2\n" \
  "v_mul_f32 v2, v2, v4\n" \
  "v_cos_f32 v88, v2\n" \
  "v_sin_f32 v92, v2\n" \
  "v_add_f32 v2, v2, v4\n" \
  "v_cos_f32 v89, v2\n" \
  "v_sin_f32 v93, v2\n" \
  "v_add_f32 v2, v2, v4\n" \
  "v_cos_f32 v90, v2\n" \
  "v_sin_f32 v94, v2\n" \
  "v_add_f32 v2, v2, v4\n" \
  "v_cos_f32 v91, v2\n" \
  "v_sin_f32 v95, v2\n" \
  "v_lshrrev_b32 v2, 5, v1\n" \
  "v_lshlrev_b32 v2, 4, v2\n" \
  "v_lshl_add_u32 v2, v3, 5, v2\n" \
  "s_lshl_b32 s16, s16, 3\n" \
  "v_add_u32 v2, s16, v2\n" \
  "s_setprio 3\n" \
  "s_mov_b32 exec_hi, 0xffff\n" \
  "s_waitcnt vmcnt(0)\n" \
  "s_barrier\n" \
  "buffer_load_dwordx2 v[6:7], v5, s[40:43], s17 offen sc1\n" \
  "s_mov_b64 exec, -1\n" \
  "s_waitcnt vmcnt(0)\n" \
  "v_lshlrev_b32 v96, 16, v6\n" \
  "v_and_b32 v97, 0xffff0000, v6\n" \
  "v_lshlrev_b32 v98, 16, v7\n" \
  "v_and_b32 v99, 0xffff0000, v7\n" \
  "v_mul_f32 v88, v96, v88\n" \
  "v_mul_f32 v92, v96, v92\n" \
  "v_mul_f32 v89, v97, v89\n" \
  "v_mul_f32 v93, v97, v93\n" \
  "v_mul_f32 v90, v98, v90\n" \
  "v_mul_f32 v94, v98, v94\n" \
  "v_mul_f32 v91, v99, v91\n" \
  "v_mul_f32 v95, v99, v95\n" \
  "v_permlane32_swap_b32 v88, v92\n" \
  "v_permlane32_swap_b32 v89, v93\n" \
  "v_permlane32_swap_b32 v90, v94\n" \
  "v_permlane32_swap_b32 v91, v95\n" \
  "v_add_f32 v88, v88, v92\n" \
  "v_mov_b32 v92, v88\n" \
  "v_add_f32 v89, v89, v93\n" \
  "v_mov_b32 v93, v89\n" \
  "v_add_f32 v90, v90, v94\n" \
  "v_mov_b32 v94, v90\n" \
  "v_add_f32 v91, v91, v95\n" \
  "v_mov_b32 v95, v91\n" \
  "v_permlane16_swap_b32 v88, v92\n" \
  "v_permlane16_swap_b32 v89, v93\n" \
  "v_permlane16_swap_b32 v90, v94\n" \
  "v_permlane16_swap_b32 v91, v95\n" \
  "v_add_f32 v88, v88, v92\n" \
  "v_add_f32 v89, v89, v93\n" \
  "v_add_f32 v90, v90, v94\n" \
  "v_add_f32 v91, v91, v95\n" \
  "v_add_f32_dpp v88, v88, v88 row_ror:8 row_mask:0xf bank_mask:0xf\n" \
  "v_add_f32_dpp v89, v89, v89 row_ror:8 row_mask:0xf bank_mask:0xf\n" \
  "v_add_f32_dpp v90, v90, v90 row_ror:8 row_mask:0xf bank_mask:0xf\n" \
  "v_add_f32_dpp v91, v91, v91 row_ror:8 row_mask:0xf bank_mask:0xf\n" \
  "v_mul_f32 v88, 0x42800000, v88\n" \
  "v_mul_f32 v89, 0x42800000, v89\n" \
  "v_mul_f32 v90, 0x42800000, v90\n" \
  "v_mul_f32 v91, 0x42800000, v91\n" \
  "ds_write_b128 v2, v[88:91] offset:5120\n" \
  "s_setprio 0\n" \
  "s_waitcnt lgkmcnt(0)\n" \
  "s_branch .LMdcx_%=\n" \
  ".LMother_%=:\n" \
  "s_cmp_ge_i32 s24, 54\n" \
  "s_cbranch_scc0 .LMg_%=\n" \
  "s_barrier\n" \
  ".LMgx_%=:\n" \
  "s_mov_b32 s16, 4194304\n" \
  "s_cmp_eq_u32 s3, 3\n" \
  "s_cbranch_scc0 .Lw22_%=t\n" \
  "s_mov_b64 exec, 31\n" \
  "buffer_load_dwordx2 v[168:169], v171, s[40:43], s29 offen sc1\n" \
  "s_mov_b64 exec, -1\n" \
  ".Lw22_%=t:\n" \
  "s_branch .LMjoin_%=\n" \
  ".LMg_%=:\n" \
  "s_lshl_b32 s16, s24, 6\n" \
  "s_sub_u32 s16, s16, 576\n" \
  "v_add_u32 v2, s16, v1\n" \
  "v_cvt_f32_i32 v2, v2\n" \
  "v_mul_f32 v2, 0xbae38e39, v2\n" \
  "s_lshl_b32 s16, s3, 1\n" \
  "v_cvt_f32_u32 v3, s16\n" \
  "v_mul_f32 v3, v3, v2\n" \
  "v_add_f32 v2, v3, v2\n" \
  "v_cos_f32 v88, v3\n" \
  "v_sin_f32 v89, v3\n" \
  "v_cos_f32 v90, v2\n" \
  "v_sin_f32 v91, v2\n" \
  "v_lshlrev_b32 v2, 2, v1\n" \
  "v_lshlrev_b32 v3, 1, v1\n" \
  "s_mul_i32 s17, s3, 10240\n" \
  "s_lshl_b32 s18, s24, 8\n" \
  "s_add_u32 s17, s17, s18\n" \
  "s_add_u32 s17, s17, 30464\n" \
  "s_add_u32 s19, s17, 5120\n" \
  "s_lshl_b32 s20, s24, 7\n" \
  "s_add_u32 s20, s20, 16384\n" \
  "s_sub_u32 s21, s24, 9\n" \
  "s_cmp_eq_u32 s3, 3\n" \
  "s_barrier\n" \
  "v_add_u32 v29, 65536, v2\n" \
  "ds_read2st64_b32 v[4:5], v29 offset1:1\n" \
  "ds_read2st64_b32 v[6:7], v29 offset0:2 offset1:3\n" \
  "s_waitcnt lgkmcnt(0)\n" \
  "v_pk_add_f32 v[4:5], v[4:5], v[6:7]\n" \
  "v_add_f32 v4, v4, v5\n" \
  "v_cvt_pk_bf16_f32 v4, 0, v4\n" \
  "s_cbranch_scc0 .LMg2_%=\n" \
  "buffer_store_dword v4, v2, s[40:43], s18 offen sc1\n" \
  "v_mov_b32_dpp v5, v4 quad_perm:[1,0,3,2] row_mask:0xf bank_mask:0xf\n" \
  "v_lshrrev_b32 v6, 16, v4\n" \
  "v_or_b32 v5, v5, v6\n" \
  "s_mov_b32 exec_lo, 0x55555555\n" \
  "s_mov_b32 exec_hi, 0x55555555\n" \
  "buffer_store_dword v5, v3, s[40:43], s20 offen sc1\n" \
  "s_mov_b64 exec, -1\n" \
  "s_branch .LMgx_%=\n" \
  ".LMg2_%=:\n" \
  "s_cmp_lt_u32 s21, 9\n" \
  "s_cbranch_scc0 .LMgx_%=\n" \
  "v_add_f32 v4, 1.0, v4\n" \
  "v_pk_mul_f32 v[88:89], v[88:89], v[4:5] op_sel_hi:[1,0]\n" \
  "v_pk_mul_f32 v[90:91], v[90:91], v[4:5] op_sel_hi:[1,0]\n" \
  "v_cvt_pk_bf16_f32 v4, v88, -v89\n" \
  "v_cvt_pk_bf16_f32 v5, v89, v88\n" \
  "v_cvt_pk_bf16_f32 v6, v90, -v91\n" \
  "v_cvt_pk_bf16_f32 v7, v91, v90\n" \
  "buffer_store_dword v4, v2, s[40:43], s17 offen sc1\n" \
  "buffer_store_dword v5, v2, s[40:43], s17 offen offset:2304 sc1\n" \
  "buffer_store_dword v6, v2, s[40:43], s19 offen sc1\n" \
  "buffer_store_dword v7, v2, s[40:43], s19 offen offset:2304 sc1\n" \
  "s_branch .LMgx_%=\n" \
  ".LSgemv_%=:\n" \
  "s_lshl_b32 s16, s25, 9\n" \
  "s_add_u32 s16, s16, 66560\n" \
  "v_add_u32 v30, s16, v30\n" \
  "v_mul_u32_u24 v7, 36, v7\n" \
  "v_add_u32 v7, s16, v7\n" \
  "s_mov_b32 s18, 0x05040100\n" \
  "s_mov_b32 s19, 0x07060302\n" \
  "v_mov_b32 v120, 0\n" \
  "v_mov_b32 v121, 0\n" \
  "v_mov_b32 v122, 0\n" \
  "v_mov_b32 v123, 0\n" \
  "v_mov_b32 v124, 0\n" \
  "v_mov_b32 v125, 0\n" \
  "v_mov_b32 v126, 0\n" \
  "v_mov_b32 v127, 0\n" \
  "s_waitcnt vmcnt(18)\n" \
  "v_lshlrev_b32 v104, 16, v4\n" \
  "v_and_b32 v105, 0xffff0000, v4\n" \
  "v_lshlrev_b32 v106, 16, v5\n" \
  "v_and_b32 v107, 0xffff0000, v5\n" \
  "v_mul_f32 v108, 0xbfb8aa3b, v104\n" \
  "v_mul_f32 v109, 0xbfb8aa3b, v105\n" \
  "v_mul_f32 v110, 0xbfb8aa3b, v106\n" \
  "v_mul_f32 v111, 0xbfb8aa3b, v107\n" \
  "v_exp_f32 v108, v108\n" \
  "v_exp_f32 v109, v109\n" \
  "v_exp_f32 v110, v110\n" \
  "v_exp_f32 v111, v111\n" \
  "v_add_f32 v108, 1.0, v108\n" \
  "v_add_f32 v109, 1.0, v109\n" \
  "v_add_f32 v110, 1.0, v110\n" \
  "v_add_f32 v111, 1.0, v111\n" \
  "v_rcp_f32 v108, v108\n" \
  "v_rcp_f32 v109, v109\n" \
  "v_rcp_f32 v110, v110\n" \
  "v_rcp_f32 v111, v111\n" \
  "v_mul_f32 v104, v104, v108\n" \
  "v_mul_f32 v105, v105, v109\n" \
  "v_mul_f32 v106, v106, v110\n" \
  "v_mul_f32 v107, v107, v111\n" \
  "v_cvt_pk_bf16_f32 v4, v104, v105\n" \
  "v_cvt_pk_bf16_f32 v5, v106, v107\n" \
  "ds_write_b64 v30, v[4:5]\n" \
  "ds_read2_b32 v[104:105], v7 offset0:0 offset1:1\n" \
  "ds_read2_b32 v[106:107], v7 offset0:2 offset1:3\n" \
  "ds_read2_b32 v[108:109], v7 offset0:4 offset1:5\n" \
  "ds_read2_b32 v[110:111], v7 offset0:6 offset1:7\n" \
  "ds_read_b32 v112, v7 offset:32\n" \
  "v_lshrrev_b32 v2, 4, v1\n" \
  "v_lshlrev_b32 v2, 3, v2\n" \
  "v_lshl_add_u32 v2, v6, 1, v2\n" \
  "s_lshl_b32 s16, s25, 8\n" \
  "s_add_u32 s16, s16, 65536\n" \
  "v_add_u32 v2, s16, v2\n" \
  "s_waitcnt lgkmcnt(0)\n" \
  "s_waitcnt vmcnt(16)\n" \
  "v_perm_b32 v128, v36, v32, s18\n" \
  "v_perm_b32 v129, v36, v32, s19\n" \
  "v_perm_b32 v130, v37, v33, s18\n" \
  "v_perm_b32 v131, v37, v33, s19\n" \
  "v_perm_b32 v132, v38, v34, s18\n" \
  "v_perm_b32 v133, v38, v34, s19\n" \
  "v_perm_b32 v134, v39, v35, s18\n" \
  "v_perm_b32 v135, v39, v35, s19\n" \
  "v_dot2c_f32_bf16 v120, v128, v104\n" \
  "v_dot2c_f32_bf16 v121, v129, v104\n" \
  "v_dot2c_f32_bf16 v122, v130, v104\n" \
  "v_dot2c_f32_bf16 v123, v131, v104\n" \
  "v_dot2c_f32_bf16 v124, v132, v104\n" \
  "v_dot2c_f32_bf16 v125, v133, v104\n" \
  "v_dot2c_f32_bf16 v126, v134, v104\n" \
  "v_dot2c_f32_bf16 v127, v135, v104\n" \
  "s_waitcnt vmcnt(14)\n" \
  "v_perm_b32 v128, v44, v40, s18\n" \
  "v_perm_b32 v129, v44, v40, s19\n" \
  "v_perm_b32 v130, v45, v41, s18\n" \
  "v_perm_b32 v131, v45, v41, s19\n" \
  "v_perm_b32 v132, v46, v42, s18\n" \
  "v_perm_b32 v133, v46, v42, s19\n" \
  "v_perm_b32 v134, v47, v43, s18\n" \
  "v_perm_b32 v135, v47, v43, s19\n" \
  "v_dot2c_f32_bf16 v120, v128, v105\n" \
  "v_dot2c_f32_bf16 v121, v129, v105\n" \
  "v_dot2c_f32_bf16 v122, v130, v105\n" \
  "v_dot2c_f32_bf16 v123, v131, v105\n" \
  "v_dot2c_f32_bf16 v124, v132, v105\n" \
  "v_dot2c_f32_bf16 v125, v133, v105\n" \
  "v_dot2c_f32_bf16 v126, v134, v105\n" \
  "v_dot2c_f32_bf16 v127, v135, v105\n" \
  "s_waitcnt vmcnt(12)\n" \
  "v_perm_b32 v128, v52, v48, s18\n" \
  "v_perm_b32 v129, v52, v48, s19\n" \
  "v_perm_b32 v130, v53, v49, s18\n" \
  "v_perm_b32 v131, v53, v49, s19\n" \
  "v_perm_b32 v132, v54, v50, s18\n" \
  "v_perm_b32 v133, v54, v50, s19\n" \
  "v_perm_b32 v134, v55, v51, s18\n" \
  "v_perm_b32 v135, v55, v51, s19\n" \
  "v_dot2c_f32_bf16 v120, v128, v106\n" \
  "v_dot2c_f32_bf16 v121, v129, v106\n" \
  "v_dot2c_f32_bf16 v122, v130, v106\n" \
  "v_dot2c_f32_bf16 v123, v131, v106\n" \
  "v_dot2c_f32_bf16 v124, v132, v106\n" \
  "v_dot2c_f32_bf16 v125, v133, v106\n" \
  "v_dot2c_f32_bf16 v126, v134, v106\n" \
  "v_dot2c_f32_bf16 v127, v135, v106\n" \
  "s_waitcnt vmcnt(10)\n" \
  "v_perm_b32 v128, v60, v56, s18\n" \
  "v_perm_b32 v129, v60, v56, s19\n" \
  "v_perm_b32 v130, v61, v57, s18\n" \
  "v_perm_b32 v131, v61, v57, s19\n" \
  "v_perm_b32 v132, v62, v58, s18\n" \
  "v_perm_b32 v133, v62, v58, s19\n" \
  "v_perm_b32 v134, v63, v59, s18\n" \
  "v_perm_b32 v135, v63, v59, s19\n" \
  "v_dot2c_f32_bf16 v120, v128, v107\n" \
  "v_dot2c_f32_bf16 v121, v129, v107\n" \
  "v_dot2c_f32_bf16 v122, v130, v107\n" \
  "v_dot2c_f32_bf16 v123, v131, v107\n" \
  "v_dot2c_f32_bf16 v124, v132, v107\n" \
  "v_dot2c_f32_bf16 v125, v133, v107\n" \
  "v_dot2c_f32_bf16 v126, v134, v107\n" \
  "v_dot2c_f32_bf16 v127, v135, v107\n" \
  "s_waitcnt vmcnt(8)\n" \
  "v_perm_b32 v128, v68, v64, s18\n" \
  "v_perm_b32 v129, v68, v64, s19\n" \
  "v_perm_b32 v130, v69, v65, s18\n" \
  "v_perm_b32 v131, v69, v65, s19\n" \
  "v_perm_b32 v132, v70, v66, s18\n" \
  "v_perm_b32 v133, v70, v66, s19\n" \
  "v_perm_b32 v134, v71, v67, s18\n" \
  "v_perm_b32 v135, v71, v67, s19\n" \
  "v_dot2c_f32_bf16 v120, v128, v108\n" \
  "v_dot2c_f32_bf16 v121, v129, v108\n" \
  "v_dot2c_f32_bf16 v122, v130, v108\n" \
  "v_dot2c_f32_bf16 v123, v131, v108\n" \
  "v_dot2c_f32_bf16 v124, v132, v108\n" \
  "v_dot2c_f32_bf16 v125, v133, v108\n" \
  "v_dot2c_f32_bf16 v126, v134, v108\n" \
  "v_dot2c_f32_bf16 v127, v135, v108\n" \
  "s_waitcnt vmcnt(6)\n" \
  "v_perm_b32 v128, v76, v72, s18\n" \
  "v_perm_b32 v129, v76, v72, s19\n" \
  "v_perm_b32 v130, v77, v73, s18\n" \
  "v_perm_b32 v131, v77, v73, s19\n" \
  "v_perm_b32 v132, v78, v74, s18\n" \
  "v_perm_b32 v133, v78, v74, s19\n" \
  "v_perm_b32 v134, v79, v75, s18\n" \
  "v_perm_b32 v135, v79, v75, s19\n" \
  "v_dot2c_f32_bf16 v120, v128, v109\n" \
  "v_dot2c_f32_bf16 v121, v129, v109\n" \
  "v_dot2c_f32_bf16 v122, v130, v109\n" \
  "v_dot2c_f32_bf16 v123, v131, v109\n" \
  "v_dot2c_f32_bf16 v124, v132, v109\n" \
  "v_dot2c_f32_bf16 v125, v133, v109\n" \
  "v_dot2c_f32_bf16 v126, v134, v109\n" \
  "v_dot2c_f32_bf16 v127, v135, v109\n" \
  "s_waitcnt vmcnt(4)\n" \
  "v_perm_b32 v128, v84, v80, s18\n" \
  "v_perm_b32 v129, v84, v80, s19\n" \
  "v_perm_b32 v130, v85, v81, s18\n" \
  "v_perm_b32 v131, v85, v81, s19\n" \
  "v_perm_b32 v132, v86, v82, s18\n" \
  "v_perm_b32 v133, v86, v82, s19\n" \
  "v_perm_b32 v134, v87, v83, s18\n" \
  "v_perm_b32 v135, v87, v83, s19\n" \
  "v_dot2c_f32_bf16 v120, v128, v110\n" \
  "v_dot2c_f32_bf16 v121, v129, v110\n" \
  "v_dot2c_f32_bf16 v122, v130, v110\n" \
  "v_dot2c_f32_bf16 v123, v131, v110\n" \
  "v_dot2c_f32_bf16 v124, v132, v110\n" \
  "v_dot2c_f32_bf16 v125, v133, v110\n" \
  "v_dot2c_f32_bf16 v126, v134, v110\n" \
  "v_dot2c_f32_bf16 v127, v135, v110\n" \
  "s_waitcnt vmcnt(2)\n" \
  "v_perm_b32 v128, v92, v88, s18\n" \
  "v_perm_b32 v129, v92, v88, s19\n" \
  "v_perm_b32 v130, v93, v89, s18\n" \
  "v_perm_b32 v131, v93, v89, s19\n" \
  "v_perm_b32 v132, v94, v90, s18\n" \
  "v_perm_b32 v133, v94, v90, s19\n" \
  "v_perm_b32 v134, v95, v91, s18\n" \
  "v_perm_b32 v135, v95, v91, s19\n" \
  "v_dot2c_f32_bf16 v120, v128, v111\n" \
  "v_dot2c_f32_bf16 v121, v129, v111\n" \
  "v_dot2c_f32_bf16 v122, v130, v111\n" \
  "v_dot2c_f32_bf16 v123, v131, v111\n" \
  "v_dot2c_f32_bf16 v124, v132, v111\n" \
  "v_dot2c_f32_bf16 v125, v133, v111\n" \
  "v_dot2c_f32_bf16 v126, v134, v111\n" \
  "v_dot2c_f32_bf16 v127, v135, v111\n" \
  "s_waitcnt vmcnt(0)\n" \
  "v_perm_b32 v128, v100, v96, s18\n" \
  "v_perm_b32 v129, v100, v96, s19\n" \
  "v_perm_b32 v130, v101, v97, s18\n" \
  "v_perm_b32 v131, v101, v97, s19\n" \
  "v_perm_b32 v132, v102, v98, s18\n" \
  "v_perm_b32 v133, v102, v98, s19\n" \
  "v_perm_b32 v134, v103, v99, s18\n" \
  "v_perm_b32 v135, v103, v99, s19\n" \
  "v_dot2c_f32_bf16 v120, v128, v112\n" \
  "v_dot2c_f32_bf16 v121, v129, v112\n" \
  "v_dot2c_f32_bf16 v122, v130, v112\n" \
  "v_dot2c_f32_bf16 v123, v131, v112\n" \
  "v_dot2c_f32_bf16 v124, v132, v112\n" \
  "v_dot2c_f32_bf16 v125, v133, v112\n" \
  "v_dot2c_f32_bf16 v126, v134, v112\n" \
  "v_dot2c_f32_bf16 v127, v135, v112\n" \
  "v_permlane32_swap_b32 v120, v124\n" \
  "v_permlane32_swap_b32 v121, v125\n" \
  "v_permlane32_swap_b32 v122, v126\n" \
  "v_permlane32_swap_b32 v123, v127\n" \
  "v_pk_add_f32 v[120:121], v[120:121], v[124:125]\n" \
  "v_pk_add_f32 v[122:123], v[122:123], v[126:127]\n" \
  "s_nop 1\n" \
  "v_permlane16_swap_b32 v120, v122\n" \
  "v_permlane16_swap_b32 v121, v123\n" \
  "v_pk_add_f32 v[120:121], v[120:121], v[122:123]\n" \
  "s_nop 1\n" \
  "v_add_f32_dpp v120, v120, v120 row_ror:8 row_mask:0xf bank_mask:0xf\n" \
  "v_add_f32_dpp v121, v121, v121 row_ror:8 row_mask:0xf bank_mask:0xf\n" \
  "ds_write_b64 v2, v[120:121]\n" \
  "s_branch .LSjoin_%=\n" \
  ".LSzero_%=:\n" \
  "s_waitcnt lgkmcnt(0)\n" \
  "s_mov_b32 s40, s96\n" \
  "s_and_b32 s41, s97, 0xffff\n" \
  "s_mov_b32 s42, 134217728\n" \
  "s_mov_b32 s43, 0x20000\n" \
  "s_cmp_lg_u32 s5, 31\n" \
  "s_cbranch_scc1 .LSjoin_%=\n" \
  "v_mov_b32 v4, 0\n" \
  "v_mov_b32 v5, 0\n" \
  "v_mov_b32 v6, 0\n" \
  "v_mov_b32 v7, 0\n" \
  "s_cmp_eq_u32 s4, 0\n" \
  "s_cbranch_scc1 .LSz0_%=\n" \
  "s_mul_i32 s16, s4, 20480\n" \
  "s_lshl_b32 s17, s25, 10\n" \
  "s_add_u32 s16, s16, s17\n" \
  "s_add_u32 s16, s16, 126976\n" \
  "buffer_store_dwordx4 v[4:7], v10, s[40:43], s16 offen sc1\n" \
  "s_add_u32 s16, s16, 4096\n" \
  "buffer_store_dwordx4 v[4:7], v10, s[40:43], s16 offen sc1\n" \
  "s_add_u32 s16, s16, 4096\n" \
  "buffer_store_dwordx4 v[4:7], v10, s[40:43], s16 offen sc1\n" \
  "s_add_u32 s16, s16, 4096\n" \
  "buffer_store_dwordx4 v[4:7], v10, s[40:43], s16 offen sc1\n" \
  "s_add_u32 s16, s16, 4096\n" \
  "buffer_store_dwordx4 v[4:7], v10, s[40:43], s16 offen sc1\n" \
  "s_add_u32 s16, s16, 4096\n" \
  "s_cmp_eq_u32 s4, 1\n" \
  "s_cbranch_scc1 .LSz1_%=\n" \
  "s_waitcnt vmcnt(0)\n" \
  "s_branch .LSz2_%=\n" \
  ".LSz0_%=:\n" \
  "s_mov_b32 s17, 65536\n" \
  "buffer_load_dwordx2 v[28:29], off, s[40:43], s17 sc1\n" \
  "v_lshrrev_b32 v2, 2, v1\n" \
  "v_mul_u32_u24 v2, 128, v2\n" \
  "v_and_b32 v3, 48, v10\n" \
  "v_add_u32 v2, v2, v3\n" \
  "s_mul_i32 s16, s25, 4096\n" \
  "s_add_u32 s16, s16, 67108864\n" \
  "buffer_store_dwordx4 v[4:7], v2, s[40:43], s16 offen sc1\n" \
  "s_add_u32 s16, s16, 2048\n" \
  "buffer_store_dwordx4 v[4:7], v2, s[40:43], s16 offen sc1\n" \
  "s_add_u32 s16, s16, 2048\n" \
  "v_lshlrev_b32 v3, 7, v1\n" \
  "s_lshl_b32 s16, s25, 3\n" \
  "s_add_u32 s16, s16, 65664\n" \
  "s_waitcnt vmcnt(2)\n" \
  "v_readfirstlane_b32 s18, v28\n" \
  "v_readfirstlane_b32 s19, v29\n" \
  "s_mul_hi_u32 s21, s18, 2135587861\n" \
  "s_mul_i32 s20, s18, 2654435769\n" \
  "s_add_u32 s21, s21, s20\n" \
  "s_mul_i32 s20, s19, 2135587861\n" \
  "s_add_u32 s21, s21, s20\n" \
  "s_mul_i32 s20, s18, 2135587861\n" \
  "s_add_u32 s20, s20, 3652509721\n" \
  "s_addc_u32 s21, s21, 1663821211\n" \
  "v_mov_b32 v28, s20\n" \
  "v_mov_b32 v29, s21\n" \
  "s_mov_b64 exec, 0xff\n" \
  "s_waitcnt vmcnt(0)\n" \
  "buffer_store_dwordx2 v[28:29], v3, s[40:43], s16 offen sc1\n" \
  "s_mov_b64 exec, -1\n" \
  "s_branch .LSz2_%=\n" \
  ".LSz1_%=:\n" \
  "v_lshrrev_b32 v2, 2, v1\n" \
  "v_mul_u32_u24 v2, 128, v2\n" \
  "v_and_b32 v3, 48, v10\n" \
  "v_add_u32 v2, v2, v3\n" \
  "s_mul_i32 s16, s25, 4096\n" \
  "s_add_u32 s16, s16, 67174400\n" \
  "buffer_store_dwordx4 v[4:7], v2, s[40:43], s16 offen sc1\n" \
  "s_add_u32 s16, s16, 2048\n" \
  "buffer_store_dwordx4 v[4:7], v2, s[40:43], s16 offen sc1\n" \
  "s_add_u32 s16, s16, 2048\n" \
  "s_waitcnt vmcnt(0)\n" \
  ".LSz2_%=:\n" \
  "v_mul_u32_u24 v2, 1052928, v1\n" \
  "s_cmp_eq_u32 s25, 0\n" \
  "s_cbranch_scc0 .LSz3_%=\n" \
  "buffer_load_dword v253, v2, s[40:43], 0 offen\n" \
  "s_branch .LSjoin_%=\n" \
  ".LSz3_%=:\n" \
  "s_cmp_eq_u32 s25, 2\n" \
  "s_cselect_b32 s8, s86, s88\n" \
  "s_cselect_b32 s9, s87, s89\n" \
  "s_cmp_eq_u32 s25, 1\n" \
  "s_cselect_b32 s8, s90, s8\n" \
  "s_cselect_b32 s9, s91, s9\n" \
  "s_and_b32 s9, s9, 0xffff\n" \
  "s_mov_b32 s10, 5308416\n" \
  "s_mov_b32 s11, 0x20000\n" \
  "v_min_u32 v3, 5308412, v2\n" \
  "s_mov_b64 exec, 0x7f\n" \
  "buffer_load_dword v253, v3, s[8:11], 0 offen\n" \
  "s_mov_b32 s8, s98\n" \
  "s_and_b32 s9, s99, 0xffff\n" \
  "s_mov_b32 s10, 2359296\n" \
  "s_mov_b32 s11, 0x20000\n" \
  "v_min_u32 v3, 2359292, v2\n" \
  "s_mov_b64 exec, 15\n" \
  "buffer_load_dword v252, v3, s[8:11], 0 offen\n" \
  "s_mov_b64 exec, -1\n" \
  "s_branch .LSjoin_%=\n" \
  ".LSidle_%=:\n" \
  "s_lshl_b32 s25, s4, 3\n" \
  "s_add_u32 s25, s25, s5\n" \
  "s_sub_u32 s25, s25, 30\n" \
  "s_mul_i32 s26, s25, 8\n" \
  "s_add_u32 s26, s26, s3\n" \
  "s_add_u32 s26, s26, 990\n" \
  "s_mul_i32 s27, s3, 58\n" \
  "s_add_u32 s27, s27, s25\n" \
  "s_add_u32 s27, s27, 1454\n" \
  "s_lshl_b32 s28, s3, 13\n" \
  "s_add_u32 s29, s28, 4096\n" \
  "s_cmp_lt_u32 s26, 1296\n" \
  "s_cbranch_scc1 .Lr24_%=d\n" \
  "s_sub_u32 s16, s26, 1296\n" \
  "s_and_b32 s17, s16, 1\n" \
  "s_cselect_b32 s8, s88, s86\n" \
  "s_cselect_b32 s9, s89, s87\n" \
  "s_lshr_b32 s17, s16, 4\n" \
  "s_mul_i32 s17, s17, 294912\n" \
  "s_bfe_u32 s16, s16, 0x30001\n" \
  "s_mul_i32 s16, s16, 1152\n" \
  "s_add_u32 s17, s17, s16\n" \
  "s_add_u32 s17, s17, 1024\n" \
  "s_movk_i32 s18, 9216\n" \
  "s_branch .Lr24_%=j\n" \
  ".Lr24_%=d:\n" \
  "s_mul_i32 s16, s26, 7282\n" \
  "s_lshr_b32 s16, s16, 16\n" \
  "s_mul_i32 s16, s16, 35712\n" \
  "s_lshl_b32 s17, s26, 7\n" \
  "s_add_u32 s17, s17, s16\n" \
  "s_mov_b32 s8, s90\n" \
  "s_mov_b32 s9, s91\n" \
  "s_movk_i32 s18, 1152\n" \
  ".Lr24_%=j:\n" \
  "s_and_b32 s9, s9, 0xffff\n" \
  "s_mov_b32 s10, 5308416\n" \
  "s_mov_b32 s11, 0x20000\n" \
  "v_lshrrev_b32 v200, 3, v1\n" \
  "v_mul_u32_u24 v200, s18, v200\n" \
  "v_and_b32 v201, 0x70, v10\n" \
  "v_add_u32 v200, v200, v201\n" \
  "s_lshl_b32 s18, s18, 3\n" \
  "s_add_u32 m0, s28, 0\n" \
  "s_nop 0\n" \
  "buffer_load_dwordx4 v200, s[8:11], s17 offen lds\n" \
  "s_add_u32 m0, s28, 1024\n" \
  "s_add_u32 s17, s17, s18\n" \
  "buffer_load_dwordx4 v200, s[8:11], s17 offen lds\n" \
  "s_add_u32 m0, s28, 2048\n" \
  "s_add_u32 s17, s17, s18\n" \
  "buffer_load_dwordx4 v200, s[8:11], s17 offen lds\n" \
  "s_add_u32 m0, s28, 3072\n" \
  "s_add_u32 s17, s17, s18\n" \
  "buffer_load_dwordx4 v200, s[8:11], s17 offen lds\n" \
  "s_cmp_lt_u32 s27, 1584\n" \
  "s_cbranch_scc0 .Li23_%=a\n" \
  "s_cmp_lt_u32 s27, 1296\n" \
  "s_cbranch_scc1 .Lr25_%=d\n" \
  "s_sub_u32 s16, s27, 1296\n" \
  "s_and_b32 s17, s16, 1\n" \
  "s_cselect_b32 s8, s88, s86\n" \
  "s_cselect_b32 s9, s89, s87\n" \
  "s_lshr_b32 s17, s16, 4\n" \
  "s_mul_i32 s17, s17, 294912\n" \
  "s_bfe_u32 s16, s16, 0x30001\n" \
  "s_mul_i32 s16, s16, 1152\n" \
  "s_add_u32 s17, s17, s16\n" \
  "s_add_u32 s17, s17, 1024\n" \
  "s_movk_i32 s18, 9216\n" \
  "s_branch .Lr25_%=j\n" \
  ".Lr25_%=d:\n" \
  "s_mul_i32 s16, s27, 7282\n" \
  "s_lshr_b32 s16, s16, 16\n" \
  "s_mul_i32 s16, s16, 35712\n" \
  "s_lshl_b32 s17, s27, 7\n" \
  "s_add_u32 s17, s17, s16\n" \
  "s_mov_b32 s8, s90\n" \
  "s_mov_b32 s9, s91\n" \
  "s_movk_i32 s18, 1152\n" \
  ".Lr25_%=j:\n" \
  "s_and_b32 s9, s9, 0xffff\n" \
  "s_mov_b32 s10, 5308416\n" \
  "s_mov_b32 s11, 0x20000\n" \
  "v_lshrrev_b32 v200, 3, v1\n" \
  "v_mul_u32_u24 v200, s18, v200\n" \
  "v_and_b32 v201, 0x70, v10\n" \
  "v_add_u32 v200, v200, v201\n" \
  "s_lshl_b32 s18, s18, 3\n" \
  "s_add_u32 m0, s29, 0\n" \
  "s_nop 0\n" \
  "buffer_load_dwordx4 v200, s[8:11], s17 offen lds\n" \
  "s_add_u32 m0, s29, 1024\n" \
  "s_add_u32 s17, s17, s18\n" \
  "buffer_load_dwordx4 v200, s[8:11], s17 offen lds\n" \
  "s_add_u32 m0, s29, 2048\n" \
  "s_add_u32 s17, s17, s18\n" \
  "buffer_load_dwordx4 v200, s[8:11], s17 offen lds\n" \
  "s_add_u32 m0, s29, 3072\n" \
  "s_add_u32 s17, s17, s18\n" \
  "buffer_load_dwordx4 v200, s[8:11], s17 offen lds\n" \
  "v_lshrrev_b32 v200, 4, v1\n" \
  "v_bfe_u32 v201, v1, 2, 2\n" \
  "v_and_b32 v202, 3, v1\n" \
  "s_cmp_lt_u32 s26, 1296\n" \
  "s_cbranch_scc1 .Lt26_%=d\n" \
  "s_sub_u32 s16, s26, 1296\n" \
  "s_bfe_u32 s17, s16, 0x30001\n" \
  "s_mul_i32 s17, s17, 163840\n" \
  "s_lshr_b32 s18, s16, 4\n" \
  "s_lshl_b32 s18, s18, 1\n" \
  "s_and_b32 s16, s16, 1\n" \
  "s_add_u32 s16, s16, s18\n" \
  "s_lshl_b32 s16, s16, 10\n" \
  "s_add_u32 s16, s16, s17\n" \
  "s_add_u32 s12, s16, 48234496\n" \
  "s_add_u32 s13, s12, 40960\n" \
  "s_add_u32 s14, s12, 81920\n" \
  "s_add_u32 s15, s12, 122880\n" \
  "s_mov_b32 s16, 3\n" \
  "s_mov_b32 s17, 3\n" \
  "s_mov_b32 s18, 32\n" \
  "s_movk_i32 s19, 512\n" \
  "s_branch .Lt26_%=j\n" \
  ".Lt26_%=d:\n" \
  "s_mul_i32 s16, s26, 7282\n" \
  "s_lshr_b32 s16, s16, 16\n" \
  "s_mul_i32 s17, s16, 9\n" \
  "s_sub_u32 s17, s26, s17\n" \
  "s_mul_i32 s19, s16, 3641\n" \
  "s_lshr_b32 s19, s19, 16\n" \
  "s_mul_i32 s20, s19, 18\n" \
  "s_sub_u32 s20, s16, s20\n" \
  "s_lshr_b32 s18, s19, 1\n" \
  "s_and_b32 s19, s19, 1\n" \
  "s_lshr_b32 s21, s20, 2\n" \
  "s_and_b32 s22, s20, 1\n" \
  "s_lshl_b32 s22, s22, 2\n" \
  "s_add_u32 s21, s21, s22\n" \
  "s_bitcmp1_b32 s20, 1\n" \
  "s_cselect_b32 s22, 10, 0\n" \
  "s_add_u32 s21, s21, s22\n" \
  "s_sub_u32 s22, s20, 8\n" \
  "s_cmp_lt_u32 s20, 16\n" \
  "s_cselect_b32 s21, s21, s22\n" \
  "s_mul_i32 s16, s18, 1343488\n" \
  "s_lshl_b32 s19, s19, 12\n" \
  "s_add_u32 s16, s16, s19\n" \
  "s_add_u32 s16, s16, 41943040\n" \
  "s_and_b32 s23, s21, 1\n" \
  "s_sub_u32 s24, s21, s23\n" \
  "s_lshl_b32 s22, s24, 1\n" \
  "s_add_u32 s22, s22, s23\n" \
  "s_mul_i32 s19, s24, 7\n" \
  "s_add_u32 s19, s19, s23\n" \
  "s_add_u32 s20, s19, 10\n" \
  "s_mul_i32 s24, s24, 5\n" \
  "s_add_u32 s24, s24, s23\n" \
  "s_add_u32 s24, s24, 20\n" \
  "s_add_u32 s23, s22, 90\n" \
  "s_cmp_lt_u32 s21, 10\n" \
  "s_cselect_b32 s19, s19, s24\n" \
  "s_cselect_b32 s20, s20, s23\n" \
  "s_add_u32 s22, s22, 126\n" \
  "s_cmp_eq_u32 s17, 8\n" \
  "s_cbranch_scc1 .Lt26_%=e\n" \
  "s_lshr_b32 s21, s17, 1\n" \
  "s_mul_i32 s21, s21, 335872\n" \
  "s_add_u32 s16, s16, s21\n" \
  "s_and_b32 s17, s17, 1\n" \
  "s_lshl_b32 s17, s17, 1\n" \
  "s_sub_u32 s17, s17, s18\n" \
  "s_add_u32 s21, s17, 0\n" \
  "s_and_b32 s21, s21, 3\n" \
  "s_add_u32 s23, s19, 4\n" \
  "s_cmp_eq_u32 s21, 1\n" \
  "s_cselect_b32 s23, s19, s23\n" \
  "s_cmp_eq_u32 s21, 3\n" \
  "s_cselect_b32 s23, s20, s23\n" \
  "s_cmp_eq_u32 s21, 0\n" \
  "s_cselect_b32 s23, s22, s23\n" \
  "s_andn2_b32 s24, s23, 3\n" \
  "s_add_u32 s24, s24, s23\n" \
  "s_lshl_b32 s24, s24, 10\n" \
  "s_add_u32 s12, s24, s16\n" \
  "s_add_u32 s23, s23, 2\n" \
  "s_andn2_b32 s24, s23, 3\n" \
  "s_add_u32 s24, s24, s23\n" \
  "s_lshl_b32 s24, s24, 10\n" \
  "s_add_u32 s13, s24, s16\n" \
  "s_add_u32 s21, s17, 1\n" \
  "s_and_b32 s21, s21, 3\n" \
  "s_add_u32 s23, s19, 4\n" \
  "s_cmp_eq_u32 s21, 1\n" \
  "s_cselect_b32 s23, s19, s23\n" \
  "s_cmp_eq_u32 s21, 3\n" \
  "s_cselect_b32 s23, s20, s23\n" \
  "s_cmp_eq_u32 s21, 0\n" \
  "s_cselect_b32 s23, s22, s23\n" \
  "s_andn2_b32 s24, s23, 3\n" \
  "s_add_u32 s24, s24, s23\n" \
  "s_lshl_b32 s24, s24, 10\n" \
  "s_add_u32 s14, s24, s16\n" \
  "s_add_u32 s23, s23, 2\n" \
  "s_andn2_b32 s24, s23, 3\n" \
  "s_add_u32 s24, s24, s23\n" \
  "s_lshl_b32 s24, s24, 10\n" \
  "s_add_u32 s15, s24, s16\n" \
  "s_mov_b32 s16, 2\n" \
  "s_mov_b32 s17, 4\n" \
  "s_mov_b32 s18, 8\n" \
  "s_movk_i32 s19, 2048\n" \
  "s_branch .Lt26_%=j\n" \
  ".Lt26_%=e:\n" \
  "s_add_u32 s23, s19, 8\n" \
  "s_andn2_b32 s24, s23, 3\n" \
  "s_add_u32 s24, s24, s23\n" \
  "s_lshl_b32 s24, s24, 10\n" \
  "s_add_u32 s12, s24, s16\n" \
  "s_add_u32 s13, s12, 335872\n" \
  "s_add_u32 s14, s12, 671744\n" \
  "s_add_u32 s15, s12, 1007616\n" \
  "s_mov_b32 s16, 2\n" \
  "s_mov_b32 s17, 3\n" \
  "s_mov_b32 s18, 32\n" \
  "s_movk_i32 s19, 2048\n" \
  ".Lt26_%=j:\n" \
  "v_lshlrev_b32 v200, s16, v200\n" \
  "v_add_u32 v200, v200, v201\n" \
  "v_lshlrev_b32 v202, s17, v202\n" \
  "v_lshl_add_u32 v200, v200, 7, v202\n" \
  "v_add_u32 v200, s28, v200\n" \
  "v_add_u32 v201, s19, v200\n" \
  "v_add_u32 v202, s18, v200\n" \
  "v_add_u32 v203, s18, v201\n" \
  "s_waitcnt vmcnt(4)\n" \
  "ds_read_b64_tr_b16 v[208:209], v200\n" \
  "ds_read_b64_tr_b16 v[210:211], v201\n" \
  "ds_read_b64_tr_b16 v[212:213], v202\n" \
  "ds_read_b64_tr_b16 v[214:215], v203\n" \
  "ds_read_b64_tr_b16 v[216:217], v200 offset:64\n" \
  "ds_read_b64_tr_b16 v[218:219], v201 offset:64\n" \
  "ds_read_b64_tr_b16 v[220:221], v202 offset:64\n" \
  "ds_read_b64_tr_b16 v[222:223], v203 offset:64\n" \
  "s_waitcnt lgkmcnt(0)\n" \
  "buffer_store_dwordx4 v[208:211], v10, s[40:43], s12 offen sc1\n" \
  "buffer_store_dwordx4 v[212:215], v10, s[40:43], s13 offen sc1\n" \
  "buffer_store_dwordx4 v[216:219], v10, s[40:43], s14 offen sc1\n" \
  "buffer_store_dwordx4 v[220:223], v10, s[40:43], s15 offen sc1\n" \
  "s_mov_b32 s26, s27\n" \
  "s_mov_b32 s28, s29\n" \
  "s_waitcnt vmcnt(4)\n" \
  "s_branch .Li23_%=b\n" \
  ".Li23_%=a:\n" \
  "s_waitcnt vmcnt(0)\n" \
  ".Li23_%=b:\n" \
  "v_lshrrev_b32 v200, 4, v1\n" \
  "v_bfe_u32 v201, v1, 2, 2\n" \
  "v_and_b32 v202, 3, v1\n" \
  "s_cmp_lt_u32 s26, 1296\n" \
  "s_cbranch_scc1 .Lt27_%=d\n" \
  "s_sub_u32 s16, s26, 1296\n" \
  "s_bfe_u32 s17, s16, 0x30001\n" \
  "s_mul_i32 s17, s17, 163840\n" \
  "s_lshr_b32 s18, s16, 4\n" \
  "s_lshl_b32 s18, s18, 1\n" \
  "s_and_b32 s16, s16, 1\n" \
  "s_add_u32 s16, s16, s18\n" \
  "s_lshl_b32 s16, s16, 10\n" \
  "s_add_u32 s16, s16, s17\n" \
  "s_add_u32 s12, s16, 48234496\n" \
  "s_add_u32 s13, s12, 40960\n" \
  "s_add_u32 s14, s12, 81920\n" \
  "s_add_u32 s15, s12, 122880\n" \
  "s_mov_b32 s16, 3\n" \
  "s_mov_b32 s17, 3\n" \
  "s_mov_b32 s18, 32\n" \
  "s_movk_i32 s19, 512\n" \
  "s_branch .Lt27_%=j\n" \
  ".Lt27_%=d:\n" \
  "s_mul_i32 s16, s26, 7282\n" \
  "s_lshr_b32 s16, s16, 16\n" \
  "s_mul_i32 s17, s16, 9\n" \
  "s_sub_u32 s17, s26, s17\n" \
  "s_mul_i32 s19, s16, 3641\n" \
  "s_lshr_b32 s19, s19, 16\n" \
  "s_mul_i32 s20, s19, 18\n" \
  "s_sub_u32 s20, s16, s20\n" \
  "s_lshr_b32 s18, s19, 1\n" \
  "s_and_b32 s19, s19, 1\n" \
  "s_lshr_b32 s21, s20, 2\n" \
  "s_and_b32 s22, s20, 1\n" \
  "s_lshl_b32 s22, s22, 2\n" \
  "s_add_u32 s21, s21, s22\n" \
  "s_bitcmp1_b32 s20, 1\n" \
  "s_cselect_b32 s22, 10, 0\n" \
  "s_add_u32 s21, s21, s22\n" \
  "s_sub_u32 s22, s20, 8\n" \
  "s_cmp_lt_u32 s20, 16\n" \
  "s_cselect_b32 s21, s21, s22\n" \
  "s_mul_i32 s16, s18, 1343488\n" \
  "s_lshl_b32 s19, s19, 12\n" \
  "s_add_u32 s16, s16, s19\n" \
  "s_add_u32 s16, s16, 41943040\n" \
  "s_and_b32 s23, s21, 1\n" \
  "s_sub_u32 s24, s21, s23\n" \
  "s_lshl_b32 s22, s24, 1\n" \
  "s_add_u32 s22, s22, s23\n" \
  "s_mul_i32 s19, s24, 7\n" \
  "s_add_u32 s19, s19, s23\n" \
  "s_add_u32 s20, s19, 10\n" \
  "s_mul_i32 s24, s24, 5\n" \
  "s_add_u32 s24, s24, s23\n" \
  "s_add_u32 s24, s24, 20\n" \
  "s_add_u32 s23, s22, 90\n" \
  "s_cmp_lt_u32 s21, 10\n" \
  "s_cselect_b32 s19, s19, s24\n" \
  "s_cselect_b32 s20, s20, s23\n" \
  "s_add_u32 s22, s22, 126\n" \
  "s_cmp_eq_u32 s17, 8\n" \
  "s_cbranch_scc1 .Lt27_%=e\n" \
  "s_lshr_b32 s21, s17, 1\n" \
  "s_mul_i32 s21, s21, 335872\n" \
  "s_add_u32 s16, s16, s21\n" \
  "s_and_b32 s17, s17, 1\n" \
  "s_lshl_b32 s17, s17, 1\n" \
  "s_sub_u32 s17, s17, s18\n" \
  "s_add_u32 s21, s17, 0\n" \
  "s_and_b32 s21, s21, 3\n" \
  "s_add_u32 s23, s19, 4\n" \
  "s_cmp_eq_u32 s21, 1\n" \
  "s_cselect_b32 s23, s19, s23\n" \
  "s_cmp_eq_u32 s21, 3\n" \
  "s_cselect_b32 s23, s20, s23\n" \
  "s_cmp_eq_u32 s21, 0\n" \
  "s_cselect_b32 s23, s22, s23\n" \
  "s_andn2_b32 s24, s23, 3\n" \
  "s_add_u32 s24, s24, s23\n" \
  "s_lshl_b32 s24, s24, 10\n" \
  "s_add_u32 s12, s24, s16\n" \
  "s_add_u32 s23, s23, 2\n" \
  "s_andn2_b32 s24, s23, 3\n" \
  "s_add_u32 s24, s24, s23\n" \
  "s_lshl_b32 s24, s24, 10\n" \
  "s_add_u32 s13, s24, s16\n" \
  "s_add_u32 s21, s17, 1\n" \
  "s_and_b32 s21, s21, 3\n" \
  "s_add_u32 s23, s19, 4\n" \
  "s_cmp_eq_u32 s21, 1\n" \
  "s_cselect_b32 s23, s19, s23\n" \
  "s_cmp_eq_u32 s21, 3\n" \
  "s_cselect_b32 s23, s20, s23\n" \
  "s_cmp_eq_u32 s21, 0\n" \
  "s_cselect_b32 s23, s22, s23\n" \
  "s_andn2_b32 s24, s23, 3\n" \
  "s_add_u32 s24, s24, s23\n" \
  "s_lshl_b32 s24, s24, 10\n" \
  "s_add_u32 s14, s24, s16\n" \
  "s_add_u32 s23, s23, 2\n" \
  "s_andn2_b32 s24, s23, 3\n" \
  "s_add_u32 s24, s24, s23\n" \
  "s_lshl_b32 s24, s24, 10\n" \
  "s_add_u32 s15, s24, s16\n" \
  "s_mov_b32 s16, 2\n" \
  "s_mov_b32 s17, 4\n" \
  "s_mov_b32 s18, 8\n" \
  "s_movk_i32 s19, 2048\n" \
  "s_branch .Lt27_%=j\n" \
  ".Lt27_%=e:\n" \
  "s_add_u32 s23, s19, 8\n" \
  "s_andn2_b32 s24, s23, 3\n" \
  "s_add_u32 s24, s24, s23\n" \
  "s_lshl_b32 s24, s24, 10\n" \
  "s_add_u32 s12, s24, s16\n" \
  "s_add_u32 s13, s12, 335872\n" \
  "s_add_u32 s14, s12, 671744\n" \
  "s_add_u32 s15, s12, 1007616\n" \
  "s_mov_b32 s16, 2\n" \
  "s_mov_b32 s17, 3\n" \
  "s_mov_b32 s18, 32\n" \
  "s_movk_i32 s19, 2048\n" \
  ".Lt27_%=j:\n" \
  "v_lshlrev_b32 v200, s16, v200\n" \
  "v_add_u32 v200, v200, v201\n" \
  "v_lshlrev_b32 v202, s17, v202\n" \
  "v_lshl_add_u32 v200, v200, 7, v202\n" \
  "v_add_u32 v200, s28, v200\n" \
  "v_add_u32 v201, s19, v200\n" \
  "v_add_u32 v202, s18, v200\n" \
  "v_add_u32 v203, s18, v201\n" \
  "ds_read_b64_tr_b16 v[224:225], v200\n" \
  "ds_read_b64_tr_b16 v[226:227], v201\n" \
  "ds_read_b64_tr_b16 v[228:229], v202\n" \
  "ds_read_b64_tr_b16 v[230:231], v203\n" \
  "ds_read_b64_tr_b16 v[232:233], v200 offset:64\n" \
  "ds_read_b64_tr_b16 v[234:235], v201 offset:64\n" \
  "ds_read_b64_tr_b16 v[236:237], v202 offset:64\n" \
  "ds_read_b64_tr_b16 v[238:239], v203 offset:64\n" \
  "s_waitcnt lgkmcnt(0)\n" \
  "buffer_store_dwordx4 v[224:227], v10, s[40:43], s12 offen sc1\n" \
  "buffer_store_dwordx4 v[228:231], v10, s[40:43], s13 offen sc1\n" \
  "buffer_store_dwordx4 v[232:235], v10, s[40:43], s14 offen sc1\n" \
  "buffer_store_dwordx4 v[236:239], v10, s[40:43], s15 offen sc1\n" \
  ".Li23_%=c:\n" \
  "s_barrier\n" \
  "s_branch .LSq2x_%=\n" \
  ".Lcode_end_%=:\n" \
  ""
#define ALL_CLOB "v1", "v2", "v3", "v4", "v5", "v6", "v7", "v8", "v9", "v10", "v11", "v12", "v13", "v14", "v15", "v16", "v17", "v18", "v19", "v20", "v21", "v22", "v23", "v24", "v25", "v26", "v27", "v28", "v29", "v30", "v31", "v32", "v33", "v34", "v35", "v36", "v37", "v38", "v39", "v40", "v41", "v42", "v43", "v44", "v45", "v46", "v47", "v48", "v49", "v50", "v51", "v52", "v53", "v54", "v55", "v56", "v57", "v58", "v59", "v60", "v61", "v62", "v63", "v64", "v65", "v66", "v67", "v68", "v69", "v70", "v71", "v72", "v73", "v74", "v75", "v76", "v77", "v78", "v79", "v80", "v81", "v82", "v83", "v84", "v85", "v86", "v87", "v88", "v89", "v90", "v91", "v92", "v93", "v94", "v95", "v96", "v97", "v98", "v99", "v100", "v101", "v102", "v103", "v104", "v105", "v106", "v107", "v108", "v109", "v110", "v111", "v112", "v113", "v114", "v115", "v116", "v117", "v118", "v119", "v120", "v121", "v122", "v123", "v124", "v125", "v126", "v127", "v128", "v129", "v130", "v131", "v132", "v133", "v134", "v135", "v136", "v137", "v138", "v139", "v140", "v141", "v142", "v143", "v144", "v145", "v146", "v147", "v148", "v149", "v150", "v151", "v152", "v153", "v154", "v155", "v156", "v157", "v158", "v159", "v160", "v161", "v162", "v163", "v164", "v165", "v166", "v167", "v168", "v169", "v170", "v171", "v172", "v173", "v174", "v175", "v176", "v177", "v178", "v179", "v180", "v181", "v182", "v183", "v184", "v185", "v186", "v187", "v188", "v189", "v190", "v191", "v192", "v193", "v194", "v195", "v196", "v197", "v198", "v199", "v200", "v201", "v202", "v203", "v204", "v205", "v206", "v207", "v208", "v209", "v210", "v211", "v212", "v213", "v214", "v215", "v216", "v217", "v218", "v219", "v220", "v221", "v222", "v223", "v224", "v225", "v226", "v227", "v228", "v229", "v230", "v231", "v232", "v233", "v234", "v235", "v236", "v237", "v238", "v239", "v240", "v241", "v242", "v243", "v244", "v245", "v246", "v247", "v248", "v249", "v250", "v251", "v252", "v253", "v254", "v255", "s3", "s4", "s5", "s6", "s7", "s8", "s9", "s10", "s11", "s12", "s13", "s14", "s15", "s16", "s17", "s18", "s19", "s20", "s21", "s22", "s23", "s24", "s25", "s26", "s27", "s28", "s29", "s30", "s31", "s34", "s35", "s36", "s37", "s38", "s39", "s40", "s41", "s42", "s43", "s44", "s45", "s46", "s47", "s48", "s49", "s50", "s51", "s52", "s53", "s54", "s55", "s56", "s57", "s58", "s59", "s60", "s61", "s62", "s63", "s64", "s65", "s66", "s67", "s68", "s69", "s70", "s71", "s72", "s73", "s74", "s75", "s76", "s77", "s78", "s79", "s80", "s81", "s82", "s83", "s84", "s85", "s86", "s87", "s88", "s89", "s90", "s91", "s92", "s93", "s94", "s95", "s96", "s97", "s98", "s99", "vcc", "scc", "memory"
constexpr size_t WS_DBG = 96u << 20;
struct Args { const void *x, *wr, *wi, *wg, *wu, *wd, *cvec, *wmod; void *ws, *out; };
extern "C" __global__ __launch_bounds__(512, 2) void k_all(Args A) {
  __shared__ __attribute__((aligned(16))) char smem[MLP_LDS];
  unsigned tid = threadIdx.x;
  unsigned long long ka = (unsigned long long)__builtin_amdgcn_kernarg_segment_ptr();
  asm volatile(PRE_ASM MLP_ASM SIDE_ASM
               : "+{v0}"(tid), "+{s[0:1]}"(ka)
               : "{s2}"(blockIdx.x), "i"((__attribute__((address_space(3))) char*)smem)
               : ALL_CLOB);
}
extern "C" void kernel_launch(void* const* d_in, const int* in_sizes, int n_in, void* d_out, int out_size, void* d_ws, size_t ws_size, hipStream_t stream) {
  Args a;
  a.x = d_in[0]; a.wr = d_in[1]; a.wi = d_in[2]; a.wg = d_in[4]; a.wu = d_in[5];
  a.wd = d_in[6]; a.cvec = d_in[7]; a.wmod = d_in[8]; a.ws = d_ws; a.out = d_out;
  k_all<<<256, 512, 0, stream>>>(a);
}
```
